```python
import torch

def _softmax(x, axis=-1):
    return torch.softmax(x, dim=axis)
def _gelu(x):
    return torch.nn.functional.gelu(x, approximate="tanh")
def _silu(x):
    return torch.nn.functional.silu(x)
def _sigmoid(x):
    return torch.sigmoid(x)
def _layernorm(x, axis=-1, eps=1e-5):
    m = x.mean(dim=axis, keepdim=True)
    v = x.var(dim=axis, keepdim=True, unbiased=False)
    return (x - m) / torch.sqrt(v + eps)
def _scatter_add(tgt, idx, src, axis=0):
    return tgt.clone().index_add_(axis, idx, src)
def _segment_sum(src, seg, n):
    out = torch.zeros((n,) + src.shape[1:], dtype=src.dtype)
    return out.index_add_(0, seg, src)
def _pad(x, pw):
    flat = tuple(v for pair in reversed(pw) for v in pair)
    return torch.nn.functional.pad(x, flat)

kernel_name = 'recurrent_scan__mha_block_cached__geglu_ffn__prompt'

def setup_inputs():

    _w = (lambda a: (lambda t: t.to(torch.bfloat16) if t.is_floating_point() else t)(torch.as_tensor(a)))
    def _A_setup():
        B, Sq, D = 1, 2048, 1024
        H, Sk, Dh = 32, 2048, 128
        return {
            "x": _w((lambda *s: torch.randn(*s, dtype=torch.bfloat16) * 0.05)(B, Sq, D)),
            "k_cache": _w((lambda *s: torch.randn(*s, dtype=torch.bfloat16) * 0.8)(B, H, Sk, Dh)),
            "v_cache": _w((lambda *s: torch.randn(*s, dtype=torch.bfloat16) * 0.2)(B, H, Sk, Dh)),
            "wq": _w((lambda *s: torch.randn(*s, dtype=torch.bfloat16) * 0.8)(D, H * Dh) * (D ** -0.5)),
            "wo": _w((lambda *s: torch.randn(*s, dtype=torch.bfloat16) * 0.2)(H * Dh, D) * ((H * Dh) ** -0.5)),
        }
    _A_in = _A_setup()

    def _B_setup():
        B, S, D, F = 1, 2048, 1024, 3584
        return {
            "x": _w((lambda *s: torch.randn(*s, dtype=torch.bfloat16) * 0.05)(B, S, D)),
            "w_gate": _w((lambda *s: torch.randn(*s, dtype=torch.bfloat16) * 0.1)(D, F)),
            "w_up": _w((lambda *s: torch.randn(*s, dtype=torch.bfloat16) * 0.1)(D, F)),
            "w_down": _w((lambda *s: torch.randn(*s, dtype=torch.bfloat16) * 0.1)(F, D)),
        }
    _B_in = _B_setup()

    return {
        "A_x": _A_in["x"],
        "A_k_cache": _A_in["k_cache"],
        "A_v_cache": _A_in["v_cache"],
        "A_wq": _A_in["wq"],
        "A_wo": _A_in["wo"],
        "B_x": _B_in["x"],
        "B_w_gate": _B_in["w_gate"],
        "B_w_up": _B_in["w_up"],
        "B_w_down": _B_in["w_down"],
    }

def reference(A_x, A_k_cache, A_v_cache, A_wq, A_wo, B_x, B_w_gate, B_w_up, B_w_down):
    def _A_ref(x, k_cache, v_cache, wq, wo):
        B, Sq, D = x.shape
        H, Dh = 32, 128
        q = (lambda x, *d: x.permute(*d))((x @ wq).reshape(B, Sq, H, Dh), 0, 2, 1, 3)
        scores = torch.einsum("bhqd,bhkd->bhqk", q, k_cache) * (Dh ** -0.5)
        p = _softmax(scores, axis=-1)
        o = torch.einsum("bhqk,bhkd->bhqd", p, v_cache)
        return (lambda x, *d: x.permute(*d))(o, 0, 2, 1, 3).reshape(B, Sq, H * Dh) @ wo

    def _B_ref(x, w_gate, w_up, w_down):
        return (_gelu(x @ w_gate) * (x @ w_up)) @ w_down

    x = A_x
    xn = _layernorm(x, axis=-1)
    s = _A_ref(xn, A_k_cache, A_v_cache, A_wq, A_wo)
    g = _silu(_B_ref(xn, B_w_gate, B_w_up, B_w_down))
    return x + s * g
```

```cpp
#include <hip/hip_runtime.h>
#include <hip/hip_bf16.h>
typedef float k1_f2 __attribute__((ext_vector_type(2)));
typedef __bf16 k1_b2 __attribute__((ext_vector_type(2)));
typedef unsigned k1_u4 __attribute__((ext_vector_type(4)));
__device__ __forceinline__ unsigned k1_pack(float a, float b) { k1_f2 v = {a, b}; return __builtin_bit_cast(unsigned, __builtin_convertvector(v, k1_b2)); }
__device__ __forceinline__ float k1_bfr(float x) { return __uint_as_float(k1_pack(x, x) << 16); }
extern "C" __global__ __launch_bounds__(512) void k1_ln(const uint4* __restrict__ x, uint4* __restrict__ xn, unsigned* __restrict__ flags) {
  __shared__ float red[2][8][8];
  const unsigned tid = threadIdx.x, l = tid & 63, w = tid >> 6, t = l & 7, gp = l >> 3, row = blockIdx.x * 8 + t;
  uint4 v[2];
#pragma unroll
  for (int i = 0; i < 2; ++i) v[i] = x[row * 128 + w * 16 + i * 8 + gp];
  if (tid < 256) __hip_atomic_store(flags + blockIdx.x * 256 + tid, 0u, __ATOMIC_RELAXED, __HIP_MEMORY_SCOPE_AGENT);
  float f[16];
#pragma unroll
  for (int i = 0; i < 2; ++i) {
    const unsigned u[4] = {v[i].x, v[i].y, v[i].z, v[i].w};
#pragma unroll
    for (int k = 0; k < 4; ++k) { f[i * 8 + 2 * k] = __uint_as_float(u[k] << 16); f[i * 8 + 2 * k + 1] = __uint_as_float(u[k] & 0xffff0000u); }
  }
  float s = 0.f;
#pragma unroll
  for (int i = 0; i < 16; ++i) s += f[i];
  s += __shfl_xor(s, 8); s += __shfl_xor(s, 16); s += __shfl_xor(s, 32);
  const float mw = s * (1.f / 128.f);
  float q = 0.f;
#pragma unroll
  for (int i = 0; i < 16; ++i) { const float d = f[i] - mw; q += d * d; }
  q += __shfl_xor(q, 8); q += __shfl_xor(q, 16); q += __shfl_xor(q, 32);
  if (gp == 0) { red[0][w][t] = mw; red[1][w][t] = q; }
  __syncthreads();
  float mean = 0.f, var = 0.f;
#pragma unroll
  for (int i = 0; i < 8; ++i) mean += red[0][i][t];
  mean *= 0.125f;
#pragma unroll
  for (int i = 0; i < 8; ++i) { const float d = red[0][i][t] - mean; var += red[1][i][t] + 128.f * d * d; }
  var *= (1.f / 1024.f);
  const float mb = k1_bfr(mean), rs = 1.0f / k1_bfr(sqrtf(k1_bfr(k1_bfr(var) + 1e-5f)));
  const unsigned tt = row >> 4, lane16 = row & 15;
#pragma unroll
  for (int i = 0; i < 2; ++i) {
    k1_u4 o;
#pragma unroll
    for (int k = 0; k < 4; ++k) {
      const unsigned c = k1_pack(f[i * 8 + 2 * k] - mb, f[i * 8 + 2 * k + 1] - mb);
      o[k] = k1_pack(__uint_as_float(c << 16) * rs, __uint_as_float(c & 0xffff0000u) * rs);
    }
    const unsigned ks = w * 4 + i * 2 + (gp >> 2), g = gp & 3;
    asm volatile("global_store_dwordx4 %0, %1, off sc1\n\ts_nop 1" :: "v"(xn + ((((tt >> 2) * 32 + ks) * 4 + (tt & 3)) * 64 + g * 16 + lane16)), "v"(o) : "memory");
  }
}

extern "C" __global__ __launch_bounds__(256, 1) void k2_main(void* ws, const void* wq, const void* wg, const void* wu, const void* kc, const void* vc, const void* wo, const void* wd, const void* x, void* out) {
  __shared__ __attribute__((aligned(16))) char lds_[163840];
  unsigned ldsb_ = (unsigned)(size_t)lds_, tid_ = threadIdx.x, bid_ = blockIdx.x;
  asm volatile(
    ".Lk2_code_start:\n"
    "s_mov_b64 s[34:35], %1\n"
    "s_mov_b32 s38, %11\n"
    "s_mov_b32 s39, %12\n"
    "v_mov_b32 v12, %0\n"
    "v_and_b32 v1, 63, v12\n"
    "v_lshrrev_b32 v12, 6, v12\n"
    "s_nop 0\n"
    "v_readfirstlane_b32 s40, v12\n"
    "s_and_b32 s86, s38, 7\n"
    "s_lshr_b32 s87, s38, 3\n"
    "s_and_b32 s42, s87, 7\n"
    "s_lshr_b32 s87, s87, 3\n"
    "s_lshl_b32 s41, s86, 2\n"
    "s_add_u32 s41, s41, s87\n"
    "v_lshlrev_b32 v2, 4, v1\n"
    "v_lshrrev_b32 v12, 2, v1\n"
    "v_lshrrev_b32 v13, 5, v1\n"
    "v_lshlrev_b32 v13, 1, v13\n"
    "v_and_b32 v14, 3, v1\n"
    "v_xor_b32 v14, v14, v13\n"
    "v_lshlrev_b32 v14, 4, v14\n"
    "s_and_b32 s88, s40, 1\n"
    "s_cmp_eq_u32 s88, 0\n"
    "s_cselect_b64 s[36:37], %3, %4\n"
    "s_mul_i32 s86, s41, 224\n"
    "s_add_u32 s48, s36, s86\n"
    "s_addc_u32 s49, s37, 0\n"
    "s_and_b32 s49, s49, 0xffff\n"
    "s_sub_u32 s50, 0x700000, s86\n"
    "s_mov_b32 s51, 0x20000\n"
    "s_lshl_b32 s86, s41, 8\n"
    "s_mov_b64 s[52:53], %2\n"
    "s_add_u32 s52, s52, s86\n"
    "s_addc_u32 s53, s53, 0\n"
    "s_and_b32 s53, s53, 0xffff\n"
    "s_sub_u32 s54, 0x800000, s86\n"
    "s_mov_b32 s55, 0x20000\n"
    "s_and_b32 s86, s41, 1\n"
    "s_lshl_b32 s87, s86, 5\n"
    "v_mul_u32_u24 v3, 7168, v12\n"
    "v_add3_u32 v3, v3, v14, s87\n"
    "v_add_u32 v4, 0x1c000, v3\n"
    "s_lshl_b32 s87, s88, 17\n"
    "v_lshlrev_b32 v6, 13, v12\n"
    "v_add3_u32 v6, v6, v14, s87\n"
    "v_lshrrev_b32 v12, 1, v1\n"
    "v_bfe_u32 v13, v1, 4, 1\n"
    "v_lshlrev_b32 v13, 2, v13\n"
    "v_xor_b32 v12, v12, v13\n"
    "v_and_b32 v14, 1, v1\n"
    "v_lshlrev_b32 v14, 4, v14\n"
    "s_cmp_eq_u32 s86, 0\n"
    "s_cselect_b32 s87, 192, 0\n"
    "v_mul_u32_u24 v5, 7168, v12\n"
    "v_add3_u32 v5, v5, v14, s87\n"
    "s_lshr_b32 s86, s40, 1\n"
    "s_mul_i32 s78, s86, 0x38000\n"
    "s_lshl_b32 s79, s86, 18\n"
    "s_mul_i32 s86, s86, 22528\n"
    "s_add_u32 s86, s86, s39\n"
    "s_mul_i32 s80, s88, 3072\n"
    "s_add_u32 s80, s80, s86\n"
    "s_lshl_b32 s81, s88, 10\n"
    "s_add_u32 s81, s81, s86\n"
    "s_mul_i32 s82, s88, 10240\n"
    "s_add_u32 s82, s82, s86\n"
    "s_add_u32 m0, s80, 0\n"
    "s_nop 0\n"
    "buffer_load_dwordx4 v3, s[48:51], s78 offen offset:0 lds\n"
    "s_add_u32 m0, s80, 960\n"
    "s_nop 0\n"
    "buffer_load_dwordx4 v3, s[48:51], s78 offen offset:64 lds\n"
    "s_add_u32 m0, s80, 1920\n"
    "s_nop 0\n"
    "buffer_load_dwordx4 v3, s[48:51], s78 offen offset:128 lds\n"
    "s_add_u32 m0, s80, 10240\n"
    "s_nop 0\n"
    "buffer_load_dwordx4 v4, s[48:51], s78 offen offset:0 lds\n"
    "s_add_u32 m0, s80, 11200\n"
    "s_nop 0\n"
    "buffer_load_dwordx4 v4, s[48:51], s78 offen offset:64 lds\n"
    "s_add_u32 m0, s80, 12160\n"
    "s_nop 0\n"
    "buffer_load_dwordx4 v4, s[48:51], s78 offen offset:128 lds\n"
    "s_add_u32 m0, s81, 20480\n"
    "s_nop 0\n"
    "buffer_load_dwordx4 v5, s[48:51], s78 offen lds\n"
    "s_add_u32 m0, s82, 6144\n"
    "s_nop 0\n"
    "buffer_load_dwordx4 v6, s[52:55], s79 offen offset:0 lds\n"
    "s_add_u32 m0, s82, 7104\n"
    "s_nop 0\n"
    "buffer_load_dwordx4 v6, s[52:55], s79 offen offset:64 lds\n"
    "s_add_u32 m0, s82, 8064\n"
    "s_nop 0\n"
    "buffer_load_dwordx4 v6, s[52:55], s79 offen offset:128 lds\n"
    "s_add_u32 m0, s82, 9024\n"
    "s_nop 0\n"
    "buffer_load_dwordx4 v6, s[52:55], s79 offen offset:192 lds\n"
    "s_mov_b32 s83, 0\n"
    "s_mov_b32 s89, 45056\n"
    "s_lshl_b32 s86, s42, 2\n"
    "s_add_u32 s86, s86, s40\n"
    "s_lshl_b32 s86, s86, 17\n"
    "s_add_u32 s44, s34, s86\n"
    "s_addc_u32 s45, s35, 0\n"
    "s_and_b32 s45, s45, 0xffff\n"
    "s_mov_b32 s46, 0x20000\n"
    "s_mov_b32 s47, 0x20000\n"
    "s_mov_b32 s76, 0\n"
    "s_mov_b32 s77, 4096\n"
    "buffer_load_dwordx4 a[128:131], v2, s[44:47], s76 offen offset:0\n"
    "buffer_load_dwordx4 a[132:135], v2, s[44:47], s76 offen offset:1024\n"
    "buffer_load_dwordx4 a[136:139], v2, s[44:47], s76 offen offset:2048\n"
    "buffer_load_dwordx4 a[140:143], v2, s[44:47], s76 offen offset:3072\n"
    "buffer_load_dwordx4 a[144:147], v2, s[44:47], s77 offen offset:0\n"
    "buffer_load_dwordx4 a[148:151], v2, s[44:47], s77 offen offset:1024\n"
    "buffer_load_dwordx4 a[152:155], v2, s[44:47], s77 offen offset:2048\n"
    "buffer_load_dwordx4 a[156:159], v2, s[44:47], s77 offen offset:3072\n"
    "s_add_u32 s78, s78, 0x70000\n"
    "s_add_u32 s79, s79, 0x80000\n"
    "s_add_u32 s83, s83, 1\n"
    "s_cmp_eq_u32 s83, 3\n"
    "s_cselect_b32 s84, 0xfffea000, s89\n"
    "s_cselect_b32 s83, 0, s83\n"
    "s_add_u32 s80, s80, s84\n"
    "s_add_u32 s81, s81, s84\n"
    "s_add_u32 s82, s82, s84\n"
    "s_add_u32 s76, s76, 8192\n"
    "s_add_u32 s77, s77, 8192\n"
    "s_add_u32 m0, s80, 0\n"
    "s_nop 0\n"
    "buffer_load_dwordx4 v3, s[48:51], s78 offen offset:0 lds\n"
    "s_add_u32 m0, s80, 960\n"
    "s_nop 0\n"
    "buffer_load_dwordx4 v3, s[48:51], s78 offen offset:64 lds\n"
    "s_add_u32 m0, s80, 1920\n"
    "s_nop 0\n"
    "buffer_load_dwordx4 v3, s[48:51], s78 offen offset:128 lds\n"
    "s_add_u32 m0, s80, 10240\n"
    "s_nop 0\n"
    "buffer_load_dwordx4 v4, s[48:51], s78 offen offset:0 lds\n"
    "s_add_u32 m0, s80, 11200\n"
    "s_nop 0\n"
    "buffer_load_dwordx4 v4, s[48:51], s78 offen offset:64 lds\n"
    "s_add_u32 m0, s80, 12160\n"
    "s_nop 0\n"
    "buffer_load_dwordx4 v4, s[48:51], s78 offen offset:128 lds\n"
    "s_add_u32 m0, s81, 20480\n"
    "s_nop 0\n"
    "buffer_load_dwordx4 v5, s[48:51], s78 offen lds\n"
    "s_add_u32 m0, s82, 6144\n"
    "s_nop 0\n"
    "buffer_load_dwordx4 v6, s[52:55], s79 offen offset:0 lds\n"
    "s_add_u32 m0, s82, 7104\n"
    "s_nop 0\n"
    "buffer_load_dwordx4 v6, s[52:55], s79 offen offset:64 lds\n"
    "s_add_u32 m0, s82, 8064\n"
    "s_nop 0\n"
    "buffer_load_dwordx4 v6, s[52:55], s79 offen offset:128 lds\n"
    "s_add_u32 m0, s82, 9024\n"
    "s_nop 0\n"
    "buffer_load_dwordx4 v6, s[52:55], s79 offen offset:192 lds\n"
    "s_add_u32 s78, s78, 0x70000\n"
    "s_add_u32 s79, s79, 0x80000\n"
    "s_add_u32 s83, s83, 1\n"
    "s_cmp_eq_u32 s83, 3\n"
    "s_cselect_b32 s84, 0xfffea000, s89\n"
    "s_cselect_b32 s83, 0, s83\n"
    "s_add_u32 s80, s80, s84\n"
    "s_add_u32 s81, s81, s84\n"
    "s_add_u32 s82, s82, s84\n"
    "s_cmp_lt_u32 s40, 2\n"
    "s_cselect_b32 s72, s48, s52\n"
    "s_cselect_b32 s73, s49, s53\n"
    "s_cselect_b32 s74, s50, s54\n"
    "s_mov_b32 s75, 0x20000\n"
    "s_mov_b32 s84, 8192\n"
    "s_cselect_b32 s86, 7168, s84\n"
    "v_lshrrev_b32 v12, 2, v1\n"
    "v_mul_lo_u32 v12, s86, v12\n"
    "v_and_b32 v13, 3, v1\n"
    "v_lshl_add_u32 v29, v13, 6, v12\n"
    "s_lshl_b32 s87, s42, 4\n"
    "s_add_u32 s87, s87, 128\n"
    "s_mul_i32 s85, s87, s86\n"
    "s_lshl_b32 s92, s86, 7\n"
    "buffer_load_dword v31, v29, s[72:75], s85 offen\n"
    "s_add_u32 s85, s85, s92\n"
    "s_getpc_b64 s[68:69]\n"
    ".Lk2_pc:\n"
    "s_and_b32 s69, s69, 0xffff\n"
    "s_mov_b32 s70, .Lk2_code_end-.Lk2_pc\n"
    "s_mov_b32 s71, 0x20000\n"
    "s_lshr_b32 s86, s38, 3\n"
    "s_lshl_b32 s86, s86, 2\n"
    "s_add_u32 s86, s86, s40\n"
    "s_and_b32 s86, s86, 15\n"
    "s_lshl_b32 s86, s86, 12\n"
    "v_lshlrev_b32 v27, 6, v1\n"
    "buffer_load_dword v31, v27, s[68:71], s86 offen\n"
    "s_lshl_b32 s90, s40, 12\n"
    "s_add_u32 s90, s90, .Lk2_epi1-.Lk2_pc\n"
    "s_mov_b32 s91, 16384\n"
    "v_lshrrev_b32 v12, 5, v1\n"
    "v_mul_u32_u24 v12, 10240, v12\n"
    "v_bfe_u32 v13, v1, 4, 1\n"
    "v_lshl_add_u32 v12, v13, 9, v12\n"
    "v_bfe_u32 v14, v1, 2, 2\n"
    "v_lshl_add_u32 v12, v14, 6, v12\n"
    "v_add_u32 v12, s39, v12\n"
    "v_lshl_add_u32 v12, v13, 5, v12\n"
    "v_and_b32 v15, 3, v1\n"
    "v_lshl_add_u32 v7, v15, 3, v12\n"
    "v_xor_b32 v8, 32, v7\n"
    "v_lshlrev_b32 v22, 4, v15\n"
    "v_xor_b32 v22, v22, v12\n"
    "v_lshrrev_b32 v12, 4, v1\n"
    "v_lshlrev_b32 v12, 8, v12\n"
    "v_lshl_add_u32 v12, v14, 5, v12\n"
    "v_lshl_add_u32 v12, v15, 3, v12\n"
    "v_add_u32 v12, s39, v12\n"
    "v_lshl_add_u32 v9, v13, 7, v12\n"
    "v_xor_b32 v10, 0x80, v9\n"
    "v_mov_b32 v16, 0\n"
    "v_mov_b32 v17, 0\n"
    "v_mov_b32 v18, 0\n"
    "v_mov_b32 v19, 0\n"
    "s_nop 1\n"
    "v_mfma_f32_32x32x16_bf16 v[32:47], v[16:19], v[16:19], 0\n"
    "v_mfma_f32_32x32x16_bf16 v[48:63], v[16:19], v[16:19], 0\n"
    "v_mfma_f32_32x32x16_bf16 v[64:79], v[16:19], v[16:19], 0\n"
    "v_mfma_f32_32x32x16_bf16 v[80:95], v[16:19], v[16:19], 0\n"
    "v_mfma_f32_32x32x16_bf16 v[96:111], v[16:19], v[16:19], 0\n"
    "v_mfma_f32_32x32x16_bf16 v[112:127], v[16:19], v[16:19], 0\n"
    "v_mfma_f32_32x32x16_bf16 v[128:143], v[16:19], v[16:19], 0\n"
    "v_mfma_f32_32x32x16_bf16 v[144:159], v[16:19], v[16:19], 0\n"
    "v_mfma_f32_32x32x16_bf16 v[160:175], v[16:19], v[16:19], 0\n"
    "v_mfma_f32_32x32x16_bf16 v[176:191], v[16:19], v[16:19], 0\n"
    "v_mfma_f32_32x32x16_bf16 v[192:207], v[16:19], v[16:19], 0\n"
    "v_mfma_f32_32x32x16_bf16 v[208:223], v[16:19], v[16:19], 0\n"
    "v_mfma_f32_32x32x16_bf16 v[224:239], v[16:19], v[16:19], 0\n"
    "v_mfma_f32_32x32x16_bf16 v[240:255], v[16:19], v[16:19], 0\n"
    "v_mfma_f32_32x32x16_bf16 a[0:15], v[16:19], v[16:19], 0\n"
    "v_mfma_f32_32x32x16_bf16 a[16:31], v[16:19], v[16:19], 0\n"
    "v_mfma_f32_32x32x16_bf16 a[32:47], v[16:19], v[16:19], 0\n"
    "v_mfma_f32_32x32x16_bf16 a[48:63], v[16:19], v[16:19], 0\n"
    "v_mfma_f32_32x32x16_bf16 a[64:79], v[16:19], v[16:19], 0\n"
    "v_mfma_f32_32x32x16_bf16 a[80:95], v[16:19], v[16:19], 0\n"
    "v_mfma_f32_32x32x16_bf16 a[96:111], v[16:19], v[16:19], 0\n"
    "v_mfma_f32_32x32x16_bf16 a[112:127], v[16:19], v[16:19], 0\n"
    "s_mov_b32 s43, 0\n"
    "s_mov_b32 s88, 0\n"
    "s_waitcnt vmcnt(21)\n"
    "s_barrier\n"
    "ds_read_b64_tr_b16 a[192:193], v7 offset:0\n"
    "ds_read_b64_tr_b16 a[194:195], v7 offset:256\n"
    "ds_read_b64_tr_b16 a[196:197], v7 offset:22528\n"
    "ds_read_b64_tr_b16 a[198:199], v7 offset:22784\n"
    "ds_read_b64_tr_b16 a[200:201], v7 offset:3072\n"
    "ds_read_b64_tr_b16 a[202:203], v7 offset:3328\n"
    "ds_read_b64_tr_b16 a[204:205], v7 offset:25600\n"
    "ds_read_b64_tr_b16 a[206:207], v7 offset:25856\n"
    "s_waitcnt lgkmcnt(0)\n"
    "s_waitcnt vmcnt(13)\n"
    "s_nop 7\n"
    "s_nop 7\n"
    ".Lk2_g1:\n"
    "v_mfma_f32_16x16x32_bf16 v[32:35], a[192:195], a[128:131], v[32:35]\n"
    "ds_read_b64_tr_b16 a[208:209], v8 offset:0\n"
    "v_mfma_f32_16x16x32_bf16 v[32:35], a[196:199], a[144:147], v[32:35]\n"
    "buffer_load_dwordx4 a[160:163], v2, s[44:47], s76 offen offset:0\n"
    "v_mfma_f32_16x16x32_bf16 v[36:39], a[192:195], a[132:135], v[36:39]\n"
    "ds_read_b64_tr_b16 a[210:211], v8 offset:256\n"
    "v_mfma_f32_16x16x32_bf16 v[36:39], a[196:199], a[148:151], v[36:39]\n"
    "v_mfma_f32_16x16x32_bf16 v[40:43], a[192:195], a[136:139], v[40:43]\n"
    "ds_read_b64_tr_b16 a[212:213], v8 offset:22528\n"
    "v_mfma_f32_16x16x32_bf16 v[40:43], a[196:199], a[152:155], v[40:43]\n"
    "buffer_load_dwordx4 a[164:167], v2, s[44:47], s76 offen offset:1024\n"
    "v_mfma_f32_16x16x32_bf16 v[44:47], a[192:195], a[140:143], v[44:47]\n"
    "ds_read_b64_tr_b16 a[214:215], v8 offset:22784\n"
    "v_mfma_f32_16x16x32_bf16 v[44:47], a[196:199], a[156:159], v[44:47]\n"
    "s_waitcnt lgkmcnt(4)\n"
    "v_mfma_f32_16x16x32_bf16 v[48:51], a[200:203], a[128:131], v[48:51]\n"
    "ds_read_b64_tr_b16 a[216:217], v8 offset:3072\n"
    "v_mfma_f32_16x16x32_bf16 v[48:51], a[204:207], a[144:147], v[48:51]\n"
    "buffer_load_dwordx4 a[168:171], v2, s[44:47], s76 offen offset:2048\n"
    "v_mfma_f32_16x16x32_bf16 v[52:55], a[200:203], a[132:135], v[52:55]\n"
    "ds_read_b64_tr_b16 a[218:219], v8 offset:3328\n"
    "v_mfma_f32_16x16x32_bf16 v[52:55], a[204:207], a[148:151], v[52:55]\n"
    "v_mfma_f32_16x16x32_bf16 v[56:59], a[200:203], a[136:139], v[56:59]\n"
    "ds_read_b64_tr_b16 a[220:221], v8 offset:25600\n"
    "v_mfma_f32_16x16x32_bf16 v[56:59], a[204:207], a[152:155], v[56:59]\n"
    "buffer_load_dwordx4 a[172:175], v2, s[44:47], s76 offen offset:3072\n"
    "v_mfma_f32_16x16x32_bf16 v[60:63], a[200:203], a[140:143], v[60:63]\n"
    "ds_read_b64_tr_b16 a[222:223], v8 offset:25856\n"
    "v_mfma_f32_16x16x32_bf16 v[60:63], a[204:207], a[156:159], v[60:63]\n"
    "s_waitcnt lgkmcnt(4)\n"
    "v_mfma_f32_16x16x32_bf16 v[64:67], a[208:211], a[128:131], v[64:67]\n"
    "ds_read_b64_tr_b16 a[192:193], v7 offset:1024\n"
    "v_mfma_f32_16x16x32_bf16 v[64:67], a[212:215], a[144:147], v[64:67]\n"
    "buffer_load_dwordx4 a[176:179], v2, s[44:47], s77 offen offset:0\n"
    "v_mfma_f32_16x16x32_bf16 v[68:71], a[208:211], a[132:135], v[68:71]\n"
    "ds_read_b64_tr_b16 a[194:195], v7 offset:1280\n"
    "v_mfma_f32_16x16x32_bf16 v[68:71], a[212:215], a[148:151], v[68:71]\n"
    "v_mfma_f32_16x16x32_bf16 v[72:75], a[208:211], a[136:139], v[72:75]\n"
    "ds_read_b64_tr_b16 a[196:197], v7 offset:23552\n"
    "v_mfma_f32_16x16x32_bf16 v[72:75], a[212:215], a[152:155], v[72:75]\n"
    "buffer_load_dwordx4 a[180:183], v2, s[44:47], s77 offen offset:1024\n"
    "v_mfma_f32_16x16x32_bf16 v[76:79], a[208:211], a[140:143], v[76:79]\n"
    "ds_read_b64_tr_b16 a[198:199], v7 offset:23808\n"
    "v_mfma_f32_16x16x32_bf16 v[76:79], a[212:215], a[156:159], v[76:79]\n"
    "s_waitcnt lgkmcnt(4)\n"
    "v_mfma_f32_16x16x32_bf16 v[80:83], a[216:219], a[128:131], v[80:83]\n"
    "ds_read_b64_tr_b16 a[200:201], v7 offset:4096\n"
    "v_mfma_f32_16x16x32_bf16 v[80:83], a[220:223], a[144:147], v[80:83]\n"
    "buffer_load_dwordx4 a[184:187], v2, s[44:47], s77 offen offset:2048\n"
    "v_mfma_f32_16x16x32_bf16 v[84:87], a[216:219], a[132:135], v[84:87]\n"
    "ds_read_b64_tr_b16 a[202:203], v7 offset:4352\n"
    "v_mfma_f32_16x16x32_bf16 v[84:87], a[220:223], a[148:151], v[84:87]\n"
    "v_mfma_f32_16x16x32_bf16 v[88:91], a[216:219], a[136:139], v[88:91]\n"
    "ds_read_b64_tr_b16 a[204:205], v7 offset:26624\n"
    "v_mfma_f32_16x16x32_bf16 v[88:91], a[220:223], a[152:155], v[88:91]\n"
    "buffer_load_dwordx4 a[188:191], v2, s[44:47], s77 offen offset:3072\n"
    "v_mfma_f32_16x16x32_bf16 v[92:95], a[216:219], a[140:143], v[92:95]\n"
    "ds_read_b64_tr_b16 a[206:207], v7 offset:26880\n"
    "v_mfma_f32_16x16x32_bf16 v[92:95], a[220:223], a[156:159], v[92:95]\n"
    "s_waitcnt lgkmcnt(4)\n"
    "v_mfma_f32_16x16x32_bf16 v[96:99], a[192:195], a[128:131], v[96:99]\n"
    "s_cmp_eq_u32 s43, 5\n"
    "s_cbranch_scc1 .Lk2_gev\n"
    ".Lk2_gev_ret:\n"
    "ds_read_b64_tr_b16 a[208:209], v8 offset:1024\n"
    "v_mfma_f32_16x16x32_bf16 v[96:99], a[196:199], a[144:147], v[96:99]\n"
    "s_add_u32 m0, s80, 0\n"
    "v_mfma_f32_16x16x32_bf16 v[100:103], a[192:195], a[132:135], v[100:103]\n"
    "ds_read_b64_tr_b16 a[210:211], v8 offset:1280\n"
    "v_mfma_f32_16x16x32_bf16 v[100:103], a[196:199], a[148:151], v[100:103]\n"
    "buffer_load_dwordx4 v3, s[48:51], s78 offen offset:0 lds\n"
    "v_mfma_f32_16x16x32_bf16 v[104:107], a[192:195], a[136:139], v[104:107]\n"
    "ds_read_b64_tr_b16 a[212:213], v8 offset:23552\n"
    "v_mfma_f32_16x16x32_bf16 v[104:107], a[196:199], a[152:155], v[104:107]\n"
    "v_mfma_f32_16x16x32_bf16 v[108:111], a[192:195], a[140:143], v[108:111]\n"
    "ds_read_b64_tr_b16 a[214:215], v8 offset:23808\n"
    "v_mfma_f32_16x16x32_bf16 v[108:111], a[196:199], a[156:159], v[108:111]\n"
    "s_waitcnt lgkmcnt(4)\n"
    "s_add_u32 m0, s80, 960\n"
    "v_mfma_f32_16x16x32_bf16 v[112:115], a[200:203], a[128:131], v[112:115]\n"
    "ds_read_b64_tr_b16 a[216:217], v8 offset:4096\n"
    "v_mfma_f32_16x16x32_bf16 v[112:115], a[204:207], a[144:147], v[112:115]\n"
    "buffer_load_dwordx4 v3, s[48:51], s78 offen offset:64 lds\n"
    "v_mfma_f32_16x16x32_bf16 v[116:119], a[200:203], a[132:135], v[116:119]\n"
    "ds_read_b64_tr_b16 a[218:219], v8 offset:4352\n"
    "v_mfma_f32_16x16x32_bf16 v[116:119], a[204:207], a[148:151], v[116:119]\n"
    "v_mfma_f32_16x16x32_bf16 v[120:123], a[200:203], a[136:139], v[120:123]\n"
    "ds_read_b64_tr_b16 a[220:221], v8 offset:26624\n"
    "v_mfma_f32_16x16x32_bf16 v[120:123], a[204:207], a[152:155], v[120:123]\n"
    "s_add_u32 m0, s80, 1920\n"
    "v_mfma_f32_16x16x32_bf16 v[124:127], a[200:203], a[140:143], v[124:127]\n"
    "ds_read_b64_tr_b16 a[222:223], v8 offset:26880\n"
    "v_mfma_f32_16x16x32_bf16 v[124:127], a[204:207], a[156:159], v[124:127]\n"
    "s_waitcnt lgkmcnt(4)\n"
    "buffer_load_dwordx4 v3, s[48:51], s78 offen offset:128 lds\n"
    "v_mfma_f32_16x16x32_bf16 v[128:131], a[208:211], a[128:131], v[128:131]\n"
    "ds_read_b64_tr_b16 a[192:193], v7 offset:2048\n"
    "v_mfma_f32_16x16x32_bf16 v[128:131], a[212:215], a[144:147], v[128:131]\n"
    "v_mfma_f32_16x16x32_bf16 v[132:135], a[208:211], a[132:135], v[132:135]\n"
    "ds_read_b64_tr_b16 a[194:195], v7 offset:2304\n"
    "v_mfma_f32_16x16x32_bf16 v[132:135], a[212:215], a[148:151], v[132:135]\n"
    "s_add_u32 m0, s80, 10240\n"
    "v_mfma_f32_16x16x32_bf16 v[136:139], a[208:211], a[136:139], v[136:139]\n"
    "ds_read_b64_tr_b16 a[196:197], v7 offset:24576\n"
    "v_mfma_f32_16x16x32_bf16 v[136:139], a[212:215], a[152:155], v[136:139]\n"
    "buffer_load_dwordx4 v4, s[48:51], s78 offen offset:0 lds\n"
    "v_mfma_f32_16x16x32_bf16 v[140:143], a[208:211], a[140:143], v[140:143]\n"
    "ds_read_b64_tr_b16 a[198:199], v7 offset:24832\n"
    "v_mfma_f32_16x16x32_bf16 v[140:143], a[212:215], a[156:159], v[140:143]\n"
    "s_waitcnt lgkmcnt(4)\n"
    "v_mfma_f32_16x16x32_bf16 v[144:147], a[216:219], a[128:131], v[144:147]\n"
    "ds_read_b64_tr_b16 a[200:201], v7 offset:5120\n"
    "v_mfma_f32_16x16x32_bf16 v[144:147], a[220:223], a[144:147], v[144:147]\n"
    "s_add_u32 m0, s80, 11200\n"
    "v_mfma_f32_16x16x32_bf16 v[148:151], a[216:219], a[132:135], v[148:151]\n"
    "ds_read_b64_tr_b16 a[202:203], v7 offset:5376\n"
    "v_mfma_f32_16x16x32_bf16 v[148:151], a[220:223], a[148:151], v[148:151]\n"
    "buffer_load_dwordx4 v4, s[48:51], s78 offen offset:64 lds\n"
    "v_mfma_f32_16x16x32_bf16 v[152:155], a[216:219], a[136:139], v[152:155]\n"
    "ds_read_b64_tr_b16 a[204:205], v7 offset:27648\n"
    "v_mfma_f32_16x16x32_bf16 v[152:155], a[220:223], a[152:155], v[152:155]\n"
    "v_mfma_f32_16x16x32_bf16 v[156:159], a[216:219], a[140:143], v[156:159]\n"
    "ds_read_b64_tr_b16 a[206:207], v7 offset:27904\n"
    "v_mfma_f32_16x16x32_bf16 v[156:159], a[220:223], a[156:159], v[156:159]\n"
    "s_waitcnt lgkmcnt(4)\n"
    "s_add_u32 m0, s80, 12160\n"
    "v_mfma_f32_16x16x32_bf16 v[160:163], a[192:195], a[128:131], v[160:163]\n"
    "ds_read_b64_tr_b16 a[208:209], v8 offset:2048\n"
    "v_mfma_f32_16x16x32_bf16 v[160:163], a[196:199], a[144:147], v[160:163]\n"
    "buffer_load_dwordx4 v4, s[48:51], s78 offen offset:128 lds\n"
    "v_mfma_f32_16x16x32_bf16 v[164:167], a[192:195], a[132:135], v[164:167]\n"
    "ds_read_b64_tr_b16 a[210:211], v8 offset:2304\n"
    "v_mfma_f32_16x16x32_bf16 v[164:167], a[196:199], a[148:151], v[164:167]\n"
    "v_mfma_f32_16x16x32_bf16 v[168:171], a[192:195], a[136:139], v[168:171]\n"
    "ds_read_b64_tr_b16 a[212:213], v8 offset:24576\n"
    "v_mfma_f32_16x16x32_bf16 v[168:171], a[196:199], a[152:155], v[168:171]\n"
    "s_add_u32 m0, s81, 20480\n"
    "v_mfma_f32_16x16x32_bf16 v[172:175], a[192:195], a[140:143], v[172:175]\n"
    "ds_read_b64_tr_b16 a[214:215], v8 offset:24832\n"
    "v_mfma_f32_16x16x32_bf16 v[172:175], a[196:199], a[156:159], v[172:175]\n"
    "s_waitcnt lgkmcnt(4)\n"
    "buffer_load_dwordx4 v5, s[48:51], s78 offen lds\n"
    "v_mfma_f32_16x16x32_bf16 v[176:179], a[200:203], a[128:131], v[176:179]\n"
    "ds_read_b64_tr_b16 a[216:217], v8 offset:5120\n"
    "v_mfma_f32_16x16x32_bf16 v[176:179], a[204:207], a[144:147], v[176:179]\n"
    "v_mfma_f32_16x16x32_bf16 v[180:183], a[200:203], a[132:135], v[180:183]\n"
    "ds_read_b64_tr_b16 a[218:219], v8 offset:5376\n"
    "v_mfma_f32_16x16x32_bf16 v[180:183], a[204:207], a[148:151], v[180:183]\n"
    "s_add_u32 m0, s82, 6144\n"
    "v_mfma_f32_16x16x32_bf16 v[184:187], a[200:203], a[136:139], v[184:187]\n"
    "ds_read_b64_tr_b16 a[220:221], v8 offset:27648\n"
    "v_mfma_f32_16x16x32_bf16 v[184:187], a[204:207], a[152:155], v[184:187]\n"
    "buffer_load_dwordx4 v6, s[52:55], s79 offen offset:0 lds\n"
    "v_mfma_f32_16x16x32_bf16 v[188:191], a[200:203], a[140:143], v[188:191]\n"
    "ds_read_b64_tr_b16 a[222:223], v8 offset:27904\n"
    "v_mfma_f32_16x16x32_bf16 v[188:191], a[204:207], a[156:159], v[188:191]\n"
    "s_waitcnt lgkmcnt(4)\n"
    "v_mfma_f32_16x16x32_bf16 v[192:195], a[208:211], a[128:131], v[192:195]\n"
    "ds_read_b64_tr_b16 a[192:193], v9 offset:20480\n"
    "v_mfma_f32_16x16x32_bf16 v[192:195], a[212:215], a[144:147], v[192:195]\n"
    "s_add_u32 m0, s82, 7104\n"
    "v_mfma_f32_16x16x32_bf16 v[196:199], a[208:211], a[132:135], v[196:199]\n"
    "ds_read_b64_tr_b16 a[194:195], v10 offset:20480\n"
    "v_mfma_f32_16x16x32_bf16 v[196:199], a[212:215], a[148:151], v[196:199]\n"
    "buffer_load_dwordx4 v6, s[52:55], s79 offen offset:64 lds\n"
    "v_mfma_f32_16x16x32_bf16 v[200:203], a[208:211], a[136:139], v[200:203]\n"
    "ds_read_b64_tr_b16 a[196:197], v9 offset:43008\n"
    "v_mfma_f32_16x16x32_bf16 v[200:203], a[212:215], a[152:155], v[200:203]\n"
    "v_mfma_f32_16x16x32_bf16 v[204:207], a[208:211], a[140:143], v[204:207]\n"
    "ds_read_b64_tr_b16 a[198:199], v10 offset:43008\n"
    "v_mfma_f32_16x16x32_bf16 v[204:207], a[212:215], a[156:159], v[204:207]\n"
    "s_waitcnt lgkmcnt(4)\n"
    "s_add_u32 m0, s82, 8064\n"
    "v_mfma_f32_16x16x32_bf16 v[208:211], a[216:219], a[128:131], v[208:211]\n"
    "ds_read_b64_tr_b16 a[200:201], v9 offset:21504\n"
    "v_mfma_f32_16x16x32_bf16 v[208:211], a[220:223], a[144:147], v[208:211]\n"
    "buffer_load_dwordx4 v6, s[52:55], s79 offen offset:128 lds\n"
    "v_mfma_f32_16x16x32_bf16 v[212:215], a[216:219], a[132:135], v[212:215]\n"
    "ds_read_b64_tr_b16 a[202:203], v10 offset:21504\n"
    "v_mfma_f32_16x16x32_bf16 v[212:215], a[220:223], a[148:151], v[212:215]\n"
    "v_mfma_f32_16x16x32_bf16 v[216:219], a[216:219], a[136:139], v[216:219]\n"
    "ds_read_b64_tr_b16 a[204:205], v9 offset:44032\n"
    "v_mfma_f32_16x16x32_bf16 v[216:219], a[220:223], a[152:155], v[216:219]\n"
    "s_add_u32 m0, s82, 9024\n"
    "v_mfma_f32_16x16x32_bf16 v[220:223], a[216:219], a[140:143], v[220:223]\n"
    "ds_read_b64_tr_b16 a[206:207], v10 offset:44032\n"
    "v_mfma_f32_16x16x32_bf16 v[220:223], a[220:223], a[156:159], v[220:223]\n"
    "s_waitcnt lgkmcnt(4)\n"
    "buffer_load_dwordx4 v6, s[52:55], s79 offen offset:192 lds\n"
    "v_mfma_f32_16x16x32_bf16 v[224:227], a[192:195], a[128:131], v[224:227]\n"
    "ds_read_b64_tr_b16 a[208:209], v22 offset:6144\n"
    "buffer_load_dword v31, v27, s[68:71], s90 offen\n"
    "v_mfma_f32_16x16x32_bf16 v[224:227], a[196:199], a[144:147], v[224:227]\n"
    "s_add_u32 s78, s78, 0x70000\n"
    "v_mfma_f32_16x16x32_bf16 v[228:231], a[192:195], a[132:135], v[228:231]\n"
    "ds_read_b64_tr_b16 a[210:211], v22 offset:6400\n"
    "s_add_u32 s79, s79, 0x80000\n"
    "v_mfma_f32_16x16x32_bf16 v[228:231], a[196:199], a[148:151], v[228:231]\n"
    "s_add_u32 s83, s83, 1\n"
    "v_mfma_f32_16x16x32_bf16 v[232:235], a[192:195], a[136:139], v[232:235]\n"
    "ds_read_b64_tr_b16 a[212:213], v22 offset:28672\n"
    "s_cmp_eq_u32 s83, 3\n"
    "v_mfma_f32_16x16x32_bf16 v[232:235], a[196:199], a[152:155], v[232:235]\n"
    "s_cselect_b32 s84, 0xfffea000, s89\n"
    "v_mfma_f32_16x16x32_bf16 v[236:239], a[192:195], a[140:143], v[236:239]\n"
    "ds_read_b64_tr_b16 a[214:215], v22 offset:28928\n"
    "s_cselect_b32 s83, 0, s83\n"
    "v_mfma_f32_16x16x32_bf16 v[236:239], a[196:199], a[156:159], v[236:239]\n"
    "s_waitcnt lgkmcnt(4)\n"
    "s_add_u32 s80, s80, s84\n"
    "v_mfma_f32_16x16x32_bf16 v[240:243], a[200:203], a[128:131], v[240:243]\n"
    "ds_read_b64_tr_b16 a[216:217], v22 offset:6152\n"
    "s_add_u32 s81, s81, s84\n"
    "v_mfma_f32_16x16x32_bf16 v[240:243], a[204:207], a[144:147], v[240:243]\n"
    "s_add_u32 s82, s82, s84\n"
    "v_mfma_f32_16x16x32_bf16 v[244:247], a[200:203], a[132:135], v[244:247]\n"
    "ds_read_b64_tr_b16 a[218:219], v22 offset:6408\n"
    "s_add_u32 s76, s76, 8192\n"
    "v_mfma_f32_16x16x32_bf16 v[244:247], a[204:207], a[148:151], v[244:247]\n"
    "s_add_u32 s77, s77, 8192\n"
    "v_mfma_f32_16x16x32_bf16 v[248:251], a[200:203], a[136:139], v[248:251]\n"
    "ds_read_b64_tr_b16 a[220:221], v22 offset:28680\n"
    "s_add_u32 s88, s88, 1\n"
    "v_mfma_f32_16x16x32_bf16 v[248:251], a[204:207], a[152:155], v[248:251]\n"
    "s_cmp_eq_u32 s88, 3\n"
    "v_mfma_f32_16x16x32_bf16 v[252:255], a[200:203], a[140:143], v[252:255]\n"
    "ds_read_b64_tr_b16 a[222:223], v22 offset:28936\n"
    "s_cselect_b32 s87, 0xfffea000, s89\n"
    "v_mfma_f32_16x16x32_bf16 v[252:255], a[204:207], a[156:159], v[252:255]\n"
    "s_waitcnt lgkmcnt(4)\n"
    "s_cselect_b32 s88, 0, s88\n"
    "v_mfma_f32_16x16x32_bf16 a[0:3], a[208:211], a[128:131], a[0:3]\n"
    "ds_read_b64_tr_b16 a[192:193], v22 offset:7168\n"
    "s_add_u32 s90, s90, s91\n"
    "v_mfma_f32_16x16x32_bf16 a[0:3], a[212:215], a[144:147], a[0:3]\n"
    "s_sub_u32 s91, 0, s91\n"
    "v_mfma_f32_16x16x32_bf16 a[4:7], a[208:211], a[132:135], a[4:7]\n"
    "ds_read_b64_tr_b16 a[194:195], v22 offset:7424\n"
    "v_mfma_f32_16x16x32_bf16 a[4:7], a[212:215], a[148:151], a[4:7]\n"
    "v_mfma_f32_16x16x32_bf16 a[8:11], a[208:211], a[136:139], a[8:11]\n"
    "ds_read_b64_tr_b16 a[196:197], v22 offset:29696\n"
    "v_mfma_f32_16x16x32_bf16 a[8:11], a[212:215], a[152:155], a[8:11]\n"
    "v_mfma_f32_16x16x32_bf16 a[12:15], a[208:211], a[140:143], a[12:15]\n"
    "ds_read_b64_tr_b16 a[198:199], v22 offset:29952\n"
    "v_mfma_f32_16x16x32_bf16 a[12:15], a[212:215], a[156:159], a[12:15]\n"
    "s_waitcnt lgkmcnt(4)\n"
    "v_mfma_f32_16x16x32_bf16 a[16:19], a[216:219], a[128:131], a[16:19]\n"
    "ds_read_b64_tr_b16 a[200:201], v22 offset:7176\n"
    "v_mfma_f32_16x16x32_bf16 a[16:19], a[220:223], a[144:147], a[16:19]\n"
    "v_mfma_f32_16x16x32_bf16 a[20:23], a[216:219], a[132:135], a[20:23]\n"
    "ds_read_b64_tr_b16 a[202:203], v22 offset:7432\n"
    "v_mfma_f32_16x16x32_bf16 a[20:23], a[220:223], a[148:151], a[20:23]\n"
    "v_mfma_f32_16x16x32_bf16 a[24:27], a[216:219], a[136:139], a[24:27]\n"
    "ds_read_b64_tr_b16 a[204:205], v22 offset:29704\n"
    "v_mfma_f32_16x16x32_bf16 a[24:27], a[220:223], a[152:155], a[24:27]\n"
    "v_mfma_f32_16x16x32_bf16 a[28:31], a[216:219], a[140:143], a[28:31]\n"
    "ds_read_b64_tr_b16 a[206:207], v22 offset:29960\n"
    "v_mfma_f32_16x16x32_bf16 a[28:31], a[220:223], a[156:159], a[28:31]\n"
    "s_waitcnt lgkmcnt(4)\n"
    "v_mfma_f32_16x16x32_bf16 a[32:35], a[192:195], a[128:131], a[32:35]\n"
    "ds_read_b64_tr_b16 a[208:209], v22 offset:8192\n"
    "v_mfma_f32_16x16x32_bf16 a[32:35], a[196:199], a[144:147], a[32:35]\n"
    "v_mfma_f32_16x16x32_bf16 a[36:39], a[192:195], a[132:135], a[36:39]\n"
    "ds_read_b64_tr_b16 a[210:211], v22 offset:8448\n"
    "v_mfma_f32_16x16x32_bf16 a[36:39], a[196:199], a[148:151], a[36:39]\n"
    "v_mfma_f32_16x16x32_bf16 a[40:43], a[192:195], a[136:139], a[40:43]\n"
    "ds_read_b64_tr_b16 a[212:213], v22 offset:30720\n"
    "v_mfma_f32_16x16x32_bf16 a[40:43], a[196:199], a[152:155], a[40:43]\n"
    "v_mfma_f32_16x16x32_bf16 a[44:47], a[192:195], a[140:143], a[44:47]\n"
    "ds_read_b64_tr_b16 a[214:215], v22 offset:30976\n"
    "v_mfma_f32_16x16x32_bf16 a[44:47], a[196:199], a[156:159], a[44:47]\n"
    "s_waitcnt lgkmcnt(4)\n"
    "v_mfma_f32_16x16x32_bf16 a[48:51], a[200:203], a[128:131], a[48:51]\n"
    "ds_read_b64_tr_b16 a[216:217], v22 offset:8200\n"
    "v_mfma_f32_16x16x32_bf16 a[48:51], a[204:207], a[144:147], a[48:51]\n"
    "v_mfma_f32_16x16x32_bf16 a[52:55], a[200:203], a[132:135], a[52:55]\n"
    "ds_read_b64_tr_b16 a[218:219], v22 offset:8456\n"
    "v_mfma_f32_16x16x32_bf16 a[52:55], a[204:207], a[148:151], a[52:55]\n"
    "v_mfma_f32_16x16x32_bf16 a[56:59], a[200:203], a[136:139], a[56:59]\n"
    "ds_read_b64_tr_b16 a[220:221], v22 offset:30728\n"
    "v_mfma_f32_16x16x32_bf16 a[56:59], a[204:207], a[152:155], a[56:59]\n"
    "v_mfma_f32_16x16x32_bf16 a[60:63], a[200:203], a[140:143], a[60:63]\n"
    "ds_read_b64_tr_b16 a[222:223], v22 offset:30984\n"
    "v_mfma_f32_16x16x32_bf16 a[60:63], a[204:207], a[156:159], a[60:63]\n"
    "s_waitcnt lgkmcnt(4)\n"
    "v_mfma_f32_16x16x32_bf16 a[64:67], a[208:211], a[128:131], a[64:67]\n"
    "ds_read_b64_tr_b16 a[192:193], v22 offset:9216\n"
    "v_mfma_f32_16x16x32_bf16 a[64:67], a[212:215], a[144:147], a[64:67]\n"
    "v_mfma_f32_16x16x32_bf16 a[68:71], a[208:211], a[132:135], a[68:71]\n"
    "ds_read_b64_tr_b16 a[194:195], v22 offset:9472\n"
    "v_mfma_f32_16x16x32_bf16 a[68:71], a[212:215], a[148:151], a[68:71]\n"
    "v_mfma_f32_16x16x32_bf16 a[72:75], a[208:211], a[136:139], a[72:75]\n"
    "ds_read_b64_tr_b16 a[196:197], v22 offset:31744\n"
    "v_mfma_f32_16x16x32_bf16 a[72:75], a[212:215], a[152:155], a[72:75]\n"
    "v_mfma_f32_16x16x32_bf16 a[76:79], a[208:211], a[140:143], a[76:79]\n"
    "ds_read_b64_tr_b16 a[198:199], v22 offset:32000\n"
    "v_mfma_f32_16x16x32_bf16 a[76:79], a[212:215], a[156:159], a[76:79]\n"
    "s_waitcnt lgkmcnt(4)\n"
    "v_mfma_f32_16x16x32_bf16 a[80:83], a[216:219], a[128:131], a[80:83]\n"
    "ds_read_b64_tr_b16 a[200:201], v22 offset:9224\n"
    "v_mfma_f32_16x16x32_bf16 a[80:83], a[220:223], a[144:147], a[80:83]\n"
    "v_mfma_f32_16x16x32_bf16 a[84:87], a[216:219], a[132:135], a[84:87]\n"
    "ds_read_b64_tr_b16 a[202:203], v22 offset:9480\n"
    "v_mfma_f32_16x16x32_bf16 a[84:87], a[220:223], a[148:151], a[84:87]\n"
    "v_mfma_f32_16x16x32_bf16 a[88:91], a[216:219], a[136:139], a[88:91]\n"
    "ds_read_b64_tr_b16 a[204:205], v22 offset:31752\n"
    "v_mfma_f32_16x16x32_bf16 a[88:91], a[220:223], a[152:155], a[88:91]\n"
    "v_mfma_f32_16x16x32_bf16 a[92:95], a[216:219], a[140:143], a[92:95]\n"
    "ds_read_b64_tr_b16 a[206:207], v22 offset:32008\n"
    "v_mfma_f32_16x16x32_bf16 a[92:95], a[220:223], a[156:159], a[92:95]\n"
    "s_waitcnt lgkmcnt(4)\n"
    "v_mfma_f32_16x16x32_bf16 a[96:99], a[192:195], a[128:131], a[96:99]\n"
    "v_mfma_f32_16x16x32_bf16 a[96:99], a[196:199], a[144:147], a[96:99]\n"
    "v_mfma_f32_16x16x32_bf16 a[100:103], a[192:195], a[132:135], a[100:103]\n"
    "s_waitcnt vmcnt(21)\n"
    "s_waitcnt lgkmcnt(0)\n"
    "s_barrier\n"
    "v_mfma_f32_16x16x32_bf16 a[100:103], a[196:199], a[148:151], a[100:103]\n"
    "v_add_u32 v7, s87, v7\n"
    "v_add_u32 v8, s87, v8\n"
    "v_mfma_f32_16x16x32_bf16 a[104:107], a[192:195], a[136:139], a[104:107]\n"
    "v_add_u32 v9, s87, v9\n"
    "v_add_u32 v10, s87, v10\n"
    "v_mfma_f32_16x16x32_bf16 a[104:107], a[196:199], a[152:155], a[104:107]\n"
    "v_add_u32 v22, s87, v22\n"
    "v_mfma_f32_16x16x32_bf16 a[108:111], a[192:195], a[140:143], a[108:111]\n"
    "ds_read_b64_tr_b16 a[208:209], v7 offset:0\n"
    "v_mfma_f32_16x16x32_bf16 a[108:111], a[196:199], a[156:159], a[108:111]\n"
    "ds_read_b64_tr_b16 a[210:211], v7 offset:256\n"
    "v_mfma_f32_16x16x32_bf16 a[112:115], a[200:203], a[128:131], a[112:115]\n"
    "ds_read_b64_tr_b16 a[212:213], v7 offset:22528\n"
    "v_mfma_f32_16x16x32_bf16 a[112:115], a[204:207], a[144:147], a[112:115]\n"
    "ds_read_b64_tr_b16 a[214:215], v7 offset:22784\n"
    "v_mfma_f32_16x16x32_bf16 a[116:119], a[200:203], a[132:135], a[116:119]\n"
    "ds_read_b64_tr_b16 a[216:217], v7 offset:3072\n"
    "v_mfma_f32_16x16x32_bf16 a[116:119], a[204:207], a[148:151], a[116:119]\n"
    "ds_read_b64_tr_b16 a[218:219], v7 offset:3328\n"
    "v_mfma_f32_16x16x32_bf16 a[120:123], a[200:203], a[136:139], a[120:123]\n"
    "ds_read_b64_tr_b16 a[220:221], v7 offset:25600\n"
    "v_mfma_f32_16x16x32_bf16 a[120:123], a[204:207], a[152:155], a[120:123]\n"
    "ds_read_b64_tr_b16 a[222:223], v7 offset:25856\n"
    "v_mfma_f32_16x16x32_bf16 a[124:127], a[200:203], a[140:143], a[124:127]\n"
    "v_mfma_f32_16x16x32_bf16 a[124:127], a[204:207], a[156:159], a[124:127]\n"
    "s_waitcnt lgkmcnt(4)\n"
    "s_waitcnt vmcnt(12)\n"
    "s_cmp_eq_u32 s43, 7\n"
    "s_cbranch_scc1 .Lk2_epi1\n"
    "v_mfma_f32_16x16x32_bf16 v[32:35], a[208:211], a[160:163], v[32:35]\n"
    "ds_read_b64_tr_b16 a[192:193], v8 offset:0\n"
    "v_mfma_f32_16x16x32_bf16 v[32:35], a[212:215], a[176:179], v[32:35]\n"
    "buffer_load_dwordx4 a[128:131], v2, s[44:47], s76 offen offset:0\n"
    "v_mfma_f32_16x16x32_bf16 v[36:39], a[208:211], a[164:167], v[36:39]\n"
    "ds_read_b64_tr_b16 a[194:195], v8 offset:256\n"
    "v_mfma_f32_16x16x32_bf16 v[36:39], a[212:215], a[180:183], v[36:39]\n"
    "v_mfma_f32_16x16x32_bf16 v[40:43], a[208:211], a[168:171], v[40:43]\n"
    "ds_read_b64_tr_b16 a[196:197], v8 offset:22528\n"
    "v_mfma_f32_16x16x32_bf16 v[40:43], a[212:215], a[184:187], v[40:43]\n"
    "buffer_load_dwordx4 a[132:135], v2, s[44:47], s76 offen offset:1024\n"
    "v_mfma_f32_16x16x32_bf16 v[44:47], a[208:211], a[172:175], v[44:47]\n"
    "ds_read_b64_tr_b16 a[198:199], v8 offset:22784\n"
    "v_mfma_f32_16x16x32_bf16 v[44:47], a[212:215], a[188:191], v[44:47]\n"
    "s_waitcnt lgkmcnt(4)\n"
    "v_mfma_f32_16x16x32_bf16 v[48:51], a[216:219], a[160:163], v[48:51]\n"
    "ds_read_b64_tr_b16 a[200:201], v8 offset:3072\n"
    "v_mfma_f32_16x16x32_bf16 v[48:51], a[220:223], a[176:179], v[48:51]\n"
    "buffer_load_dwordx4 a[136:139], v2, s[44:47], s76 offen offset:2048\n"
    "v_mfma_f32_16x16x32_bf16 v[52:55], a[216:219], a[164:167], v[52:55]\n"
    "ds_read_b64_tr_b16 a[202:203], v8 offset:3328\n"
    "v_mfma_f32_16x16x32_bf16 v[52:55], a[220:223], a[180:183], v[52:55]\n"
    "v_mfma_f32_16x16x32_bf16 v[56:59], a[216:219], a[168:171], v[56:59]\n"
    "ds_read_b64_tr_b16 a[204:205], v8 offset:25600\n"
    "v_mfma_f32_16x16x32_bf16 v[56:59], a[220:223], a[184:187], v[56:59]\n"
    "buffer_load_dwordx4 a[140:143], v2, s[44:47], s76 offen offset:3072\n"
    "v_mfma_f32_16x16x32_bf16 v[60:63], a[216:219], a[172:175], v[60:63]\n"
    "ds_read_b64_tr_b16 a[206:207], v8 offset:25856\n"
    "v_mfma_f32_16x16x32_bf16 v[60:63], a[220:223], a[188:191], v[60:63]\n"
    "s_waitcnt lgkmcnt(4)\n"
    "v_mfma_f32_16x16x32_bf16 v[64:67], a[192:195], a[160:163], v[64:67]\n"
    "ds_read_b64_tr_b16 a[208:209], v7 offset:1024\n"
    "v_mfma_f32_16x16x32_bf16 v[64:67], a[196:199], a[176:179], v[64:67]\n"
    "buffer_load_dwordx4 a[144:147], v2, s[44:47], s77 offen offset:0\n"
    "v_mfma_f32_16x16x32_bf16 v[68:71], a[192:195], a[164:167], v[68:71]\n"
    "ds_read_b64_tr_b16 a[210:211], v7 offset:1280\n"
    "v_mfma_f32_16x16x32_bf16 v[68:71], a[196:199], a[180:183], v[68:71]\n"
    "v_mfma_f32_16x16x32_bf16 v[72:75], a[192:195], a[168:171], v[72:75]\n"
    "ds_read_b64_tr_b16 a[212:213], v7 offset:23552\n"
    "v_mfma_f32_16x16x32_bf16 v[72:75], a[196:199], a[184:187], v[72:75]\n"
    "buffer_load_dwordx4 a[148:151], v2, s[44:47], s77 offen offset:1024\n"
    "v_mfma_f32_16x16x32_bf16 v[76:79], a[192:195], a[172:175], v[76:79]\n"
    "ds_read_b64_tr_b16 a[214:215], v7 offset:23808\n"
    "v_mfma_f32_16x16x32_bf16 v[76:79], a[196:199], a[188:191], v[76:79]\n"
    "s_waitcnt lgkmcnt(4)\n"
    "v_mfma_f32_16x16x32_bf16 v[80:83], a[200:203], a[160:163], v[80:83]\n"
    "ds_read_b64_tr_b16 a[216:217], v7 offset:4096\n"
    "v_mfma_f32_16x16x32_bf16 v[80:83], a[204:207], a[176:179], v[80:83]\n"
    "buffer_load_dwordx4 a[152:155], v2, s[44:47], s77 offen offset:2048\n"
    "v_mfma_f32_16x16x32_bf16 v[84:87], a[200:203], a[164:167], v[84:87]\n"
    "ds_read_b64_tr_b16 a[218:219], v7 offset:4352\n"
    "v_mfma_f32_16x16x32_bf16 v[84:87], a[204:207], a[180:183], v[84:87]\n"
    "v_mfma_f32_16x16x32_bf16 v[88:91], a[200:203], a[168:171], v[88:91]\n"
    "ds_read_b64_tr_b16 a[220:221], v7 offset:26624\n"
    "v_mfma_f32_16x16x32_bf16 v[88:91], a[204:207], a[184:187], v[88:91]\n"
    "buffer_load_dwordx4 a[156:159], v2, s[44:47], s77 offen offset:3072\n"
    "v_mfma_f32_16x16x32_bf16 v[92:95], a[200:203], a[172:175], v[92:95]\n"
    "ds_read_b64_tr_b16 a[222:223], v7 offset:26880\n"
    "v_mfma_f32_16x16x32_bf16 v[92:95], a[204:207], a[188:191], v[92:95]\n"
    "s_waitcnt lgkmcnt(4)\n"
    "v_mfma_f32_16x16x32_bf16 v[96:99], a[208:211], a[160:163], v[96:99]\n"
    "ds_read_b64_tr_b16 a[192:193], v8 offset:1024\n"
    "v_mfma_f32_16x16x32_bf16 v[96:99], a[212:215], a[176:179], v[96:99]\n"
    "s_add_u32 m0, s80, 0\n"
    "v_mfma_f32_16x16x32_bf16 v[100:103], a[208:211], a[164:167], v[100:103]\n"
    "ds_read_b64_tr_b16 a[194:195], v8 offset:1280\n"
    "v_mfma_f32_16x16x32_bf16 v[100:103], a[212:215], a[180:183], v[100:103]\n"
    "buffer_load_dwordx4 v3, s[48:51], s78 offen offset:0 lds\n"
    "v_mfma_f32_16x16x32_bf16 v[104:107], a[208:211], a[168:171], v[104:107]\n"
    "ds_read_b64_tr_b16 a[196:197], v8 offset:23552\n"
    "v_mfma_f32_16x16x32_bf16 v[104:107], a[212:215], a[184:187], v[104:107]\n"
    "v_mfma_f32_16x16x32_bf16 v[108:111], a[208:211], a[172:175], v[108:111]\n"
    "ds_read_b64_tr_b16 a[198:199], v8 offset:23808\n"
    "v_mfma_f32_16x16x32_bf16 v[108:111], a[212:215], a[188:191], v[108:111]\n"
    "s_waitcnt lgkmcnt(4)\n"
    "s_add_u32 m0, s80, 960\n"
    "v_mfma_f32_16x16x32_bf16 v[112:115], a[216:219], a[160:163], v[112:115]\n"
    "ds_read_b64_tr_b16 a[200:201], v8 offset:4096\n"
    "v_mfma_f32_16x16x32_bf16 v[112:115], a[220:223], a[176:179], v[112:115]\n"
    "buffer_load_dwordx4 v3, s[48:51], s78 offen offset:64 lds\n"
    "v_mfma_f32_16x16x32_bf16 v[116:119], a[216:219], a[164:167], v[116:119]\n"
    "ds_read_b64_tr_b16 a[202:203], v8 offset:4352\n"
    "v_mfma_f32_16x16x32_bf16 v[116:119], a[220:223], a[180:183], v[116:119]\n"
    "v_mfma_f32_16x16x32_bf16 v[120:123], a[216:219], a[168:171], v[120:123]\n"
    "ds_read_b64_tr_b16 a[204:205], v8 offset:26624\n"
    "v_mfma_f32_16x16x32_bf16 v[120:123], a[220:223], a[184:187], v[120:123]\n"
    "s_add_u32 m0, s80, 1920\n"
    "v_mfma_f32_16x16x32_bf16 v[124:127], a[216:219], a[172:175], v[124:127]\n"
    "ds_read_b64_tr_b16 a[206:207], v8 offset:26880\n"
    "v_mfma_f32_16x16x32_bf16 v[124:127], a[220:223], a[188:191], v[124:127]\n"
    "s_waitcnt lgkmcnt(4)\n"
    "buffer_load_dwordx4 v3, s[48:51], s78 offen offset:128 lds\n"
    "v_mfma_f32_16x16x32_bf16 v[128:131], a[192:195], a[160:163], v[128:131]\n"
    "ds_read_b64_tr_b16 a[208:209], v7 offset:2048\n"
    "v_mfma_f32_16x16x32_bf16 v[128:131], a[196:199], a[176:179], v[128:131]\n"
    "v_mfma_f32_16x16x32_bf16 v[132:135], a[192:195], a[164:167], v[132:135]\n"
    "ds_read_b64_tr_b16 a[210:211], v7 offset:2304\n"
    "v_mfma_f32_16x16x32_bf16 v[132:135], a[196:199], a[180:183], v[132:135]\n"
    "s_add_u32 m0, s80, 10240\n"
    "v_mfma_f32_16x16x32_bf16 v[136:139], a[192:195], a[168:171], v[136:139]\n"
    "ds_read_b64_tr_b16 a[212:213], v7 offset:24576\n"
    "v_mfma_f32_16x16x32_bf16 v[136:139], a[196:199], a[184:187], v[136:139]\n"
    "buffer_load_dwordx4 v4, s[48:51], s78 offen offset:0 lds\n"
    "v_mfma_f32_16x16x32_bf16 v[140:143], a[192:195], a[172:175], v[140:143]\n"
    "ds_read_b64_tr_b16 a[214:215], v7 offset:24832\n"
    "v_mfma_f32_16x16x32_bf16 v[140:143], a[196:199], a[188:191], v[140:143]\n"
    "s_waitcnt lgkmcnt(4)\n"
    "v_mfma_f32_16x16x32_bf16 v[144:147], a[200:203], a[160:163], v[144:147]\n"
    "ds_read_b64_tr_b16 a[216:217], v7 offset:5120\n"
    "v_mfma_f32_16x16x32_bf16 v[144:147], a[204:207], a[176:179], v[144:147]\n"
    "s_add_u32 m0, s80, 11200\n"
    "v_mfma_f32_16x16x32_bf16 v[148:151], a[200:203], a[164:167], v[148:151]\n"
    "ds_read_b64_tr_b16 a[218:219], v7 offset:5376\n"
    "v_mfma_f32_16x16x32_bf16 v[148:151], a[204:207], a[180:183], v[148:151]\n"
    "buffer_load_dwordx4 v4, s[48:51], s78 offen offset:64 lds\n"
    "v_mfma_f32_16x16x32_bf16 v[152:155], a[200:203], a[168:171], v[152:155]\n"
    "ds_read_b64_tr_b16 a[220:221], v7 offset:27648\n"
    "v_mfma_f32_16x16x32_bf16 v[152:155], a[204:207], a[184:187], v[152:155]\n"
    "v_mfma_f32_16x16x32_bf16 v[156:159], a[200:203], a[172:175], v[156:159]\n"
    "ds_read_b64_tr_b16 a[222:223], v7 offset:27904\n"
    "v_mfma_f32_16x16x32_bf16 v[156:159], a[204:207], a[188:191], v[156:159]\n"
    "s_waitcnt lgkmcnt(4)\n"
    "s_add_u32 m0, s80, 12160\n"
    "v_mfma_f32_16x16x32_bf16 v[160:163], a[208:211], a[160:163], v[160:163]\n"
    "ds_read_b64_tr_b16 a[192:193], v8 offset:2048\n"
    "v_mfma_f32_16x16x32_bf16 v[160:163], a[212:215], a[176:179], v[160:163]\n"
    "buffer_load_dwordx4 v4, s[48:51], s78 offen offset:128 lds\n"
    "v_mfma_f32_16x16x32_bf16 v[164:167], a[208:211], a[164:167], v[164:167]\n"
    "ds_read_b64_tr_b16 a[194:195], v8 offset:2304\n"
    "v_mfma_f32_16x16x32_bf16 v[164:167], a[212:215], a[180:183], v[164:167]\n"
    "v_mfma_f32_16x16x32_bf16 v[168:171], a[208:211], a[168:171], v[168:171]\n"
    "ds_read_b64_tr_b16 a[196:197], v8 offset:24576\n"
    "v_mfma_f32_16x16x32_bf16 v[168:171], a[212:215], a[184:187], v[168:171]\n"
    "s_add_u32 m0, s81, 20480\n"
    "v_mfma_f32_16x16x32_bf16 v[172:175], a[208:211], a[172:175], v[172:175]\n"
    "ds_read_b64_tr_b16 a[198:199], v8 offset:24832\n"
    "v_mfma_f32_16x16x32_bf16 v[172:175], a[212:215], a[188:191], v[172:175]\n"
    "s_waitcnt lgkmcnt(4)\n"
    "buffer_load_dwordx4 v5, s[48:51], s78 offen lds\n"
    "v_mfma_f32_16x16x32_bf16 v[176:179], a[216:219], a[160:163], v[176:179]\n"
    "ds_read_b64_tr_b16 a[200:201], v8 offset:5120\n"
    "v_mfma_f32_16x16x32_bf16 v[176:179], a[220:223], a[176:179], v[176:179]\n"
    "v_mfma_f32_16x16x32_bf16 v[180:183], a[216:219], a[164:167], v[180:183]\n"
    "ds_read_b64_tr_b16 a[202:203], v8 offset:5376\n"
    "v_mfma_f32_16x16x32_bf16 v[180:183], a[220:223], a[180:183], v[180:183]\n"
    "s_add_u32 m0, s82, 6144\n"
    "v_mfma_f32_16x16x32_bf16 v[184:187], a[216:219], a[168:171], v[184:187]\n"
    "ds_read_b64_tr_b16 a[204:205], v8 offset:27648\n"
    "v_mfma_f32_16x16x32_bf16 v[184:187], a[220:223], a[184:187], v[184:187]\n"
    "buffer_load_dwordx4 v6, s[52:55], s79 offen offset:0 lds\n"
    "v_mfma_f32_16x16x32_bf16 v[188:191], a[216:219], a[172:175], v[188:191]\n"
    "ds_read_b64_tr_b16 a[206:207], v8 offset:27904\n"
    "v_mfma_f32_16x16x32_bf16 v[188:191], a[220:223], a[188:191], v[188:191]\n"
    "s_waitcnt lgkmcnt(4)\n"
    "v_mfma_f32_16x16x32_bf16 v[192:195], a[192:195], a[160:163], v[192:195]\n"
    "ds_read_b64_tr_b16 a[208:209], v9 offset:20480\n"
    "v_mfma_f32_16x16x32_bf16 v[192:195], a[196:199], a[176:179], v[192:195]\n"
    "s_add_u32 m0, s82, 7104\n"
    "v_mfma_f32_16x16x32_bf16 v[196:199], a[192:195], a[164:167], v[196:199]\n"
    "ds_read_b64_tr_b16 a[210:211], v10 offset:20480\n"
    "v_mfma_f32_16x16x32_bf16 v[196:199], a[196:199], a[180:183], v[196:199]\n"
    "buffer_load_dwordx4 v6, s[52:55], s79 offen offset:64 lds\n"
    "v_mfma_f32_16x16x32_bf16 v[200:203], a[192:195], a[168:171], v[200:203]\n"
    "ds_read_b64_tr_b16 a[212:213], v9 offset:43008\n"
    "v_mfma_f32_16x16x32_bf16 v[200:203], a[196:199], a[184:187], v[200:203]\n"
    "v_mfma_f32_16x16x32_bf16 v[204:207], a[192:195], a[172:175], v[204:207]\n"
    "ds_read_b64_tr_b16 a[214:215], v10 offset:43008\n"
    "v_mfma_f32_16x16x32_bf16 v[204:207], a[196:199], a[188:191], v[204:207]\n"
    "s_waitcnt lgkmcnt(4)\n"
    "s_add_u32 m0, s82, 8064\n"
    "v_mfma_f32_16x16x32_bf16 v[208:211], a[200:203], a[160:163], v[208:211]\n"
    "ds_read_b64_tr_b16 a[216:217], v9 offset:21504\n"
    "v_mfma_f32_16x16x32_bf16 v[208:211], a[204:207], a[176:179], v[208:211]\n"
    "buffer_load_dwordx4 v6, s[52:55], s79 offen offset:128 lds\n"
    "v_mfma_f32_16x16x32_bf16 v[212:215], a[200:203], a[164:167], v[212:215]\n"
    "ds_read_b64_tr_b16 a[218:219], v10 offset:21504\n"
    "v_mfma_f32_16x16x32_bf16 v[212:215], a[204:207], a[180:183], v[212:215]\n"
    "v_mfma_f32_16x16x32_bf16 v[216:219], a[200:203], a[168:171], v[216:219]\n"
    "ds_read_b64_tr_b16 a[220:221], v9 offset:44032\n"
    "v_mfma_f32_16x16x32_bf16 v[216:219], a[204:207], a[184:187], v[216:219]\n"
    "s_add_u32 m0, s82, 9024\n"
    "v_mfma_f32_16x16x32_bf16 v[220:223], a[200:203], a[172:175], v[220:223]\n"
    "ds_read_b64_tr_b16 a[222:223], v10 offset:44032\n"
    "v_mfma_f32_16x16x32_bf16 v[220:223], a[204:207], a[188:191], v[220:223]\n"
    "s_waitcnt lgkmcnt(4)\n"
    "buffer_load_dwordx4 v6, s[52:55], s79 offen offset:192 lds\n"
    "v_mfma_f32_16x16x32_bf16 v[224:227], a[208:211], a[160:163], v[224:227]\n"
    "ds_read_b64_tr_b16 a[192:193], v22 offset:6144\n"
    "buffer_load_dword v30, v29, s[72:75], s85 offen\n"
    "v_mfma_f32_16x16x32_bf16 v[224:227], a[212:215], a[176:179], v[224:227]\n"
    "s_add_u32 s78, s78, 0x70000\n"
    "v_mfma_f32_16x16x32_bf16 v[228:231], a[208:211], a[164:167], v[228:231]\n"
    "ds_read_b64_tr_b16 a[194:195], v22 offset:6400\n"
    "s_add_u32 s79, s79, 0x80000\n"
    "v_mfma_f32_16x16x32_bf16 v[228:231], a[212:215], a[180:183], v[228:231]\n"
    "s_add_u32 s83, s83, 1\n"
    "v_mfma_f32_16x16x32_bf16 v[232:235], a[208:211], a[168:171], v[232:235]\n"
    "ds_read_b64_tr_b16 a[196:197], v22 offset:28672\n"
    "s_cmp_eq_u32 s83, 3\n"
    "v_mfma_f32_16x16x32_bf16 v[232:235], a[212:215], a[184:187], v[232:235]\n"
    "s_cselect_b32 s84, 0xfffea000, s89\n"
    "v_mfma_f32_16x16x32_bf16 v[236:239], a[208:211], a[172:175], v[236:239]\n"
    "ds_read_b64_tr_b16 a[198:199], v22 offset:28928\n"
    "s_cselect_b32 s83, 0, s83\n"
    "v_mfma_f32_16x16x32_bf16 v[236:239], a[212:215], a[188:191], v[236:239]\n"
    "s_waitcnt lgkmcnt(4)\n"
    "s_add_u32 s80, s80, s84\n"
    "v_mfma_f32_16x16x32_bf16 v[240:243], a[216:219], a[160:163], v[240:243]\n"
    "ds_read_b64_tr_b16 a[200:201], v22 offset:6152\n"
    "s_add_u32 s81, s81, s84\n"
    "v_mfma_f32_16x16x32_bf16 v[240:243], a[220:223], a[176:179], v[240:243]\n"
    "s_add_u32 s82, s82, s84\n"
    "v_mfma_f32_16x16x32_bf16 v[244:247], a[216:219], a[164:167], v[244:247]\n"
    "ds_read_b64_tr_b16 a[202:203], v22 offset:6408\n"
    "s_add_u32 s76, s76, 8192\n"
    "v_mfma_f32_16x16x32_bf16 v[244:247], a[220:223], a[180:183], v[244:247]\n"
    "s_add_u32 s77, s77, 8192\n"
    "v_mfma_f32_16x16x32_bf16 v[248:251], a[216:219], a[168:171], v[248:251]\n"
    "ds_read_b64_tr_b16 a[204:205], v22 offset:28680\n"
    "s_add_u32 s88, s88, 1\n"
    "v_mfma_f32_16x16x32_bf16 v[248:251], a[220:223], a[184:187], v[248:251]\n"
    "s_cmp_eq_u32 s88, 3\n"
    "v_mfma_f32_16x16x32_bf16 v[252:255], a[216:219], a[172:175], v[252:255]\n"
    "ds_read_b64_tr_b16 a[206:207], v22 offset:28936\n"
    "s_cselect_b32 s87, 0xfffea000, s89\n"
    "v_mfma_f32_16x16x32_bf16 v[252:255], a[220:223], a[188:191], v[252:255]\n"
    "s_waitcnt lgkmcnt(4)\n"
    "s_cselect_b32 s88, 0, s88\n"
    "v_mfma_f32_16x16x32_bf16 a[0:3], a[192:195], a[160:163], a[0:3]\n"
    "ds_read_b64_tr_b16 a[208:209], v22 offset:7168\n"
    "s_add_u32 s85, s85, s92\n"
    "v_mfma_f32_16x16x32_bf16 a[0:3], a[196:199], a[176:179], a[0:3]\n"
    "v_mfma_f32_16x16x32_bf16 a[4:7], a[192:195], a[164:167], a[4:7]\n"
    "ds_read_b64_tr_b16 a[210:211], v22 offset:7424\n"
    "v_mfma_f32_16x16x32_bf16 a[4:7], a[196:199], a[180:183], a[4:7]\n"
    "v_mfma_f32_16x16x32_bf16 a[8:11], a[192:195], a[168:171], a[8:11]\n"
    "ds_read_b64_tr_b16 a[212:213], v22 offset:29696\n"
    "v_mfma_f32_16x16x32_bf16 a[8:11], a[196:199], a[184:187], a[8:11]\n"
    "v_mfma_f32_16x16x32_bf16 a[12:15], a[192:195], a[172:175], a[12:15]\n"
    "ds_read_b64_tr_b16 a[214:215], v22 offset:29952\n"
    "v_mfma_f32_16x16x32_bf16 a[12:15], a[196:199], a[188:191], a[12:15]\n"
    "s_waitcnt lgkmcnt(4)\n"
    "v_mfma_f32_16x16x32_bf16 a[16:19], a[200:203], a[160:163], a[16:19]\n"
    "ds_read_b64_tr_b16 a[216:217], v22 offset:7176\n"
    "v_mfma_f32_16x16x32_bf16 a[16:19], a[204:207], a[176:179], a[16:19]\n"
    "v_mfma_f32_16x16x32_bf16 a[20:23], a[200:203], a[164:167], a[20:23]\n"
    "ds_read_b64_tr_b16 a[218:219], v22 offset:7432\n"
    "v_mfma_f32_16x16x32_bf16 a[20:23], a[204:207], a[180:183], a[20:23]\n"
    "v_mfma_f32_16x16x32_bf16 a[24:27], a[200:203], a[168:171], a[24:27]\n"
    "ds_read_b64_tr_b16 a[220:221], v22 offset:29704\n"
    "v_mfma_f32_16x16x32_bf16 a[24:27], a[204:207], a[184:187], a[24:27]\n"
    "v_mfma_f32_16x16x32_bf16 a[28:31], a[200:203], a[172:175], a[28:31]\n"
    "ds_read_b64_tr_b16 a[222:223], v22 offset:29960\n"
    "v_mfma_f32_16x16x32_bf16 a[28:31], a[204:207], a[188:191], a[28:31]\n"
    "s_waitcnt lgkmcnt(4)\n"
    "v_mfma_f32_16x16x32_bf16 a[32:35], a[208:211], a[160:163], a[32:35]\n"
    "ds_read_b64_tr_b16 a[192:193], v22 offset:8192\n"
    "v_mfma_f32_16x16x32_bf16 a[32:35], a[212:215], a[176:179], a[32:35]\n"
    "v_mfma_f32_16x16x32_bf16 a[36:39], a[208:211], a[164:167], a[36:39]\n"
    "ds_read_b64_tr_b16 a[194:195], v22 offset:8448\n"
    "v_mfma_f32_16x16x32_bf16 a[36:39], a[212:215], a[180:183], a[36:39]\n"
    "v_mfma_f32_16x16x32_bf16 a[40:43], a[208:211], a[168:171], a[40:43]\n"
    "ds_read_b64_tr_b16 a[196:197], v22 offset:30720\n"
    "v_mfma_f32_16x16x32_bf16 a[40:43], a[212:215], a[184:187], a[40:43]\n"
    "v_mfma_f32_16x16x32_bf16 a[44:47], a[208:211], a[172:175], a[44:47]\n"
    "ds_read_b64_tr_b16 a[198:199], v22 offset:30976\n"
    "v_mfma_f32_16x16x32_bf16 a[44:47], a[212:215], a[188:191], a[44:47]\n"
    "s_waitcnt lgkmcnt(4)\n"
    "v_mfma_f32_16x16x32_bf16 a[48:51], a[216:219], a[160:163], a[48:51]\n"
    "ds_read_b64_tr_b16 a[200:201], v22 offset:8200\n"
    "v_mfma_f32_16x16x32_bf16 a[48:51], a[220:223], a[176:179], a[48:51]\n"
    "v_mfma_f32_16x16x32_bf16 a[52:55], a[216:219], a[164:167], a[52:55]\n"
    "ds_read_b64_tr_b16 a[202:203], v22 offset:8456\n"
    "v_mfma_f32_16x16x32_bf16 a[52:55], a[220:223], a[180:183], a[52:55]\n"
    "v_mfma_f32_16x16x32_bf16 a[56:59], a[216:219], a[168:171], a[56:59]\n"
    "ds_read_b64_tr_b16 a[204:205], v22 offset:30728\n"
    "v_mfma_f32_16x16x32_bf16 a[56:59], a[220:223], a[184:187], a[56:59]\n"
    "v_mfma_f32_16x16x32_bf16 a[60:63], a[216:219], a[172:175], a[60:63]\n"
    "ds_read_b64_tr_b16 a[206:207], v22 offset:30984\n"
    "v_mfma_f32_16x16x32_bf16 a[60:63], a[220:223], a[188:191], a[60:63]\n"
    "s_waitcnt lgkmcnt(4)\n"
    "v_mfma_f32_16x16x32_bf16 a[64:67], a[192:195], a[160:163], a[64:67]\n"
    "ds_read_b64_tr_b16 a[208:209], v22 offset:9216\n"
    "v_mfma_f32_16x16x32_bf16 a[64:67], a[196:199], a[176:179], a[64:67]\n"
    "v_mfma_f32_16x16x32_bf16 a[68:71], a[192:195], a[164:167], a[68:71]\n"
    "ds_read_b64_tr_b16 a[210:211], v22 offset:9472\n"
    "v_mfma_f32_16x16x32_bf16 a[68:71], a[196:199], a[180:183], a[68:71]\n"
    "v_mfma_f32_16x16x32_bf16 a[72:75], a[192:195], a[168:171], a[72:75]\n"
    "ds_read_b64_tr_b16 a[212:213], v22 offset:31744\n"
    "v_mfma_f32_16x16x32_bf16 a[72:75], a[196:199], a[184:187], a[72:75]\n"
    "v_mfma_f32_16x16x32_bf16 a[76:79], a[192:195], a[172:175], a[76:79]\n"
    "ds_read_b64_tr_b16 a[214:215], v22 offset:32000\n"
    "v_mfma_f32_16x16x32_bf16 a[76:79], a[196:199], a[188:191], a[76:79]\n"
    "s_waitcnt lgkmcnt(4)\n"
    "v_mfma_f32_16x16x32_bf16 a[80:83], a[200:203], a[160:163], a[80:83]\n"
    "ds_read_b64_tr_b16 a[216:217], v22 offset:9224\n"
    "v_mfma_f32_16x16x32_bf16 a[80:83], a[204:207], a[176:179], a[80:83]\n"
    "v_mfma_f32_16x16x32_bf16 a[84:87], a[200:203], a[164:167], a[84:87]\n"
    "ds_read_b64_tr_b16 a[218:219], v22 offset:9480\n"
    "v_mfma_f32_16x16x32_bf16 a[84:87], a[204:207], a[180:183], a[84:87]\n"
    "v_mfma_f32_16x16x32_bf16 a[88:91], a[200:203], a[168:171], a[88:91]\n"
    "ds_read_b64_tr_b16 a[220:221], v22 offset:31752\n"
    "v_mfma_f32_16x16x32_bf16 a[88:91], a[204:207], a[184:187], a[88:91]\n"
    "v_mfma_f32_16x16x32_bf16 a[92:95], a[200:203], a[172:175], a[92:95]\n"
    "ds_read_b64_tr_b16 a[222:223], v22 offset:32008\n"
    "v_mfma_f32_16x16x32_bf16 a[92:95], a[204:207], a[188:191], a[92:95]\n"
    "s_waitcnt lgkmcnt(4)\n"
    "v_mfma_f32_16x16x32_bf16 a[96:99], a[208:211], a[160:163], a[96:99]\n"
    "v_mfma_f32_16x16x32_bf16 a[96:99], a[212:215], a[176:179], a[96:99]\n"
    "v_mfma_f32_16x16x32_bf16 a[100:103], a[208:211], a[164:167], a[100:103]\n"
    "s_waitcnt vmcnt(21)\n"
    "s_waitcnt lgkmcnt(0)\n"
    "s_barrier\n"
    "v_mfma_f32_16x16x32_bf16 a[100:103], a[212:215], a[180:183], a[100:103]\n"
    "v_add_u32 v7, s87, v7\n"
    "v_add_u32 v8, s87, v8\n"
    "v_mfma_f32_16x16x32_bf16 a[104:107], a[208:211], a[168:171], a[104:107]\n"
    "v_add_u32 v9, s87, v9\n"
    "v_add_u32 v10, s87, v10\n"
    "v_mfma_f32_16x16x32_bf16 a[104:107], a[212:215], a[184:187], a[104:107]\n"
    "v_add_u32 v22, s87, v22\n"
    "v_mfma_f32_16x16x32_bf16 a[108:111], a[208:211], a[172:175], a[108:111]\n"
    "ds_read_b64_tr_b16 a[192:193], v7 offset:0\n"
    "v_mfma_f32_16x16x32_bf16 a[108:111], a[212:215], a[188:191], a[108:111]\n"
    "ds_read_b64_tr_b16 a[194:195], v7 offset:256\n"
    "v_mfma_f32_16x16x32_bf16 a[112:115], a[216:219], a[160:163], a[112:115]\n"
    "ds_read_b64_tr_b16 a[196:197], v7 offset:22528\n"
    "v_mfma_f32_16x16x32_bf16 a[112:115], a[220:223], a[176:179], a[112:115]\n"
    "ds_read_b64_tr_b16 a[198:199], v7 offset:22784\n"
    "v_mfma_f32_16x16x32_bf16 a[116:119], a[216:219], a[164:167], a[116:119]\n"
    "ds_read_b64_tr_b16 a[200:201], v7 offset:3072\n"
    "v_mfma_f32_16x16x32_bf16 a[116:119], a[220:223], a[180:183], a[116:119]\n"
    "ds_read_b64_tr_b16 a[202:203], v7 offset:3328\n"
    "v_mfma_f32_16x16x32_bf16 a[120:123], a[216:219], a[168:171], a[120:123]\n"
    "ds_read_b64_tr_b16 a[204:205], v7 offset:25600\n"
    "v_mfma_f32_16x16x32_bf16 a[120:123], a[220:223], a[184:187], a[120:123]\n"
    "ds_read_b64_tr_b16 a[206:207], v7 offset:25856\n"
    "v_mfma_f32_16x16x32_bf16 a[124:127], a[216:219], a[172:175], a[124:127]\n"
    "v_mfma_f32_16x16x32_bf16 a[124:127], a[220:223], a[188:191], a[124:127]\n"
    "s_waitcnt lgkmcnt(4)\n"
    "s_waitcnt vmcnt(12)\n"
    "s_add_u32 s43, s43, 1\n"
    "s_cmp_lt_u32 s43, 8\n"
    "s_cbranch_scc1 .Lk2_g1\n"
    ".Lk2_epi1:\n"
    "v_mfma_f32_16x16x32_bf16 v[32:35], a[208:211], a[160:163], v[32:35]\n"
    "ds_read_b64_tr_b16 a[192:193], v8 offset:0\n"
    "ds_read_b64_tr_b16 a[194:195], v8 offset:256\n"
    "v_mfma_f32_16x16x32_bf16 v[32:35], a[212:215], a[176:179], v[32:35]\n"
    "s_lshl_b32 s86, s42, 3\n"
    "s_lshl_b32 s87, s40, 1\n"
    "v_mfma_f32_16x16x32_bf16 v[36:39], a[208:211], a[164:167], v[36:39]\n"
    "s_add_u32 s86, s86, s87\n"
    "s_mul_i32 s87, s86, 229376\n"
    "v_mfma_f32_16x16x32_bf16 v[36:39], a[212:215], a[180:183], v[36:39]\n"
    "s_add_u32 s87, s87, 0x1400000\n"
    "v_mfma_f32_16x16x32_bf16 v[40:43], a[208:211], a[168:171], v[40:43]\n"
    "ds_read_b64_tr_b16 a[196:197], v8 offset:22528\n"
    "ds_read_b64_tr_b16 a[198:199], v8 offset:22784\n"
    "v_mfma_f32_16x16x32_bf16 v[40:43], a[212:215], a[184:187], v[40:43]\n"
    "s_add_u32 s56, s34, s87\n"
    "s_addc_u32 s57, s35, 0\n"
    "v_mfma_f32_16x16x32_bf16 v[44:47], a[208:211], a[172:175], v[44:47]\n"
    "s_and_b32 s57, s57, 0xffff\n"
    "s_mov_b32 s58, 458752\n"
    "v_mfma_f32_16x16x32_bf16 v[44:47], a[212:215], a[188:191], v[44:47]\n"
    "s_waitcnt lgkmcnt(4)\n"
    "s_mov_b32 s59, 0x20000\n"
    "s_lshl_b32 s87, s86, 18\n"
    "v_mfma_f32_16x16x32_bf16 v[48:51], a[216:219], a[160:163], v[48:51]\n"
    "ds_read_b64_tr_b16 a[200:201], v8 offset:3072\n"
    "ds_read_b64_tr_b16 a[202:203], v8 offset:3328\n"
    "v_mfma_f32_16x16x32_bf16 v[48:51], a[220:223], a[176:179], v[48:51]\n"
    "s_add_u32 s87, s87, 0x400000\n"
    "v_mfma_f32_16x16x32_bf16 v[52:55], a[216:219], a[164:167], v[52:55]\n"
    "s_add_u32 s68, s34, s87\n"
    "s_addc_u32 s69, s35, 0\n"
    "v_mfma_f32_16x16x32_bf16 v[52:55], a[220:223], a[180:183], v[52:55]\n"
    "s_and_b32 s69, s69, 0xffff\n"
    "s_mov_b32 s70, 524288\n"
    "v_mfma_f32_16x16x32_bf16 v[56:59], a[216:219], a[168:171], v[56:59]\n"
    "ds_read_b64_tr_b16 a[204:205], v8 offset:25600\n"
    "ds_read_b64_tr_b16 a[206:207], v8 offset:25856\n"
    "v_mfma_f32_16x16x32_bf16 v[56:59], a[220:223], a[184:187], v[56:59]\n"
    "s_mov_b32 s71, 0x20000\n"
    "s_and_b32 s86, s41, 1\n"
    "v_mfma_f32_16x16x32_bf16 v[60:63], a[216:219], a[172:175], v[60:63]\n"
    "s_mul_i32 s87, s41, 7\n"
    "s_add_u32 s88, s87, s86\n"
    "v_mfma_f32_16x16x32_bf16 v[60:63], a[220:223], a[188:191], v[60:63]\n"
    "s_waitcnt lgkmcnt(4)\n"
    "s_lshl_b32 s76, s88, 10\n"
    "s_add_u32 s77, s76, 229376\n"
    "v_mfma_f32_16x16x32_bf16 v[64:67], a[192:195], a[160:163], v[64:67]\n"
    "ds_read_b64_tr_b16 a[208:209], v7 offset:1024\n"
    "ds_read_b64_tr_b16 a[210:211], v7 offset:1280\n"
    "v_mfma_f32_16x16x32_bf16 v[64:67], a[196:199], a[176:179], v[64:67]\n"
    "s_mul_i32 s88, s86, -8184\n"
    "s_add_u32 s88, s88, 6144\n"
    "v_mfma_f32_16x16x32_bf16 v[68:71], a[192:195], a[164:167], v[68:71]\n"
    "s_add_u32 s78, s76, s88\n"
    "s_add_u32 s79, s78, 229376\n"
    "v_mfma_f32_16x16x32_bf16 v[68:71], a[196:199], a[180:183], v[68:71]\n"
    "s_add_u32 s88, s76, 4096\n"
    "s_add_u32 s89, s77, 4096\n"
    "v_mfma_f32_16x16x32_bf16 v[72:75], a[192:195], a[168:171], v[72:75]\n"
    "ds_read_b64_tr_b16 a[212:213], v7 offset:23552\n"
    "ds_read_b64_tr_b16 a[214:215], v7 offset:23808\n"
    "v_mfma_f32_16x16x32_bf16 v[72:75], a[196:199], a[184:187], v[72:75]\n"
    "s_mov_b32 s90, 0xbdd2d3e8\n"
    "s_mov_b32 s91, 0xbdd2d3e8\n"
    "v_mfma_f32_16x16x32_bf16 v[76:79], a[192:195], a[172:175], v[76:79]\n"
    "v_mov_b32 v12, 0xc0135761\n"
    "v_mov_b32 v13, 0xc0135761\n"
    "v_mfma_f32_16x16x32_bf16 v[76:79], a[196:199], a[188:191], v[76:79]\n"
    "s_waitcnt lgkmcnt(4)\n"
    "s_lshl_b32 s86, s41, 19\n"
    "s_mov_b64 s[60:61], %5\n"
    "v_mfma_f32_16x16x32_bf16 v[80:83], a[200:203], a[160:163], v[80:83]\n"
    "ds_read_b64_tr_b16 a[216:217], v7 offset:4096\n"
    "ds_read_b64_tr_b16 a[218:219], v7 offset:4352\n"
    "v_mfma_f32_16x16x32_bf16 v[80:83], a[204:207], a[176:179], v[80:83]\n"
    "s_add_u32 s60, s60, s86\n"
    "s_addc_u32 s61, s61, 0\n"
    "v_mfma_f32_16x16x32_bf16 v[84:87], a[200:203], a[164:167], v[84:87]\n"
    "s_and_b32 s61, s61, 0xffff\n"
    "s_mov_b32 s62, 0x80000\n"
    "v_mfma_f32_16x16x32_bf16 v[84:87], a[204:207], a[180:183], v[84:87]\n"
    "s_mov_b32 s63, 0x20000\n"
    "s_mov_b64 s[64:65], %6\n"
    "v_mfma_f32_16x16x32_bf16 v[88:91], a[200:203], a[168:171], v[88:91]\n"
    "ds_read_b64_tr_b16 a[220:221], v7 offset:26624\n"
    "ds_read_b64_tr_b16 a[222:223], v7 offset:26880\n"
    "v_mfma_f32_16x16x32_bf16 v[88:91], a[204:207], a[184:187], v[88:91]\n"
    "s_add_u32 s64, s64, s86\n"
    "s_addc_u32 s65, s65, 0\n"
    "v_mfma_f32_16x16x32_bf16 v[92:95], a[200:203], a[172:175], v[92:95]\n"
    "s_and_b32 s65, s65, 0xffff\n"
    "s_mov_b32 s66, 0x80000\n"
    "v_mfma_f32_16x16x32_bf16 v[92:95], a[204:207], a[188:191], v[92:95]\n"
    "s_waitcnt lgkmcnt(4)\n"
    "s_mov_b32 s67, 0x20000\n"
    "v_mfma_f32_16x16x32_bf16 v[96:99], a[208:211], a[160:163], v[96:99]\n"
    "ds_read_b64_tr_b16 a[192:193], v8 offset:1024\n"
    "ds_read_b64_tr_b16 a[194:195], v8 offset:1280\n"
    "v_mfma_f32_16x16x32_bf16 v[96:99], a[212:215], a[176:179], v[96:99]\n"
    "v_mfma_f32_16x16x32_bf16 v[100:103], a[208:211], a[164:167], v[100:103]\n"
    "v_mfma_f32_16x16x32_bf16 v[100:103], a[212:215], a[180:183], v[100:103]\n"
    "v_mfma_f32_16x16x32_bf16 v[104:107], a[208:211], a[168:171], v[104:107]\n"
    "ds_read_b64_tr_b16 a[196:197], v8 offset:23552\n"
    "ds_read_b64_tr_b16 a[198:199], v8 offset:23808\n"
    "v_mfma_f32_16x16x32_bf16 v[104:107], a[212:215], a[184:187], v[104:107]\n"
    "v_mul_f32 v24, v32, v32\n"
    "v_mul_f32 v25, v33, v33\n"
    "v_mfma_f32_16x16x32_bf16 v[108:111], a[208:211], a[172:175], v[108:111]\n"
    "v_mul_f32 v26, v34, v34\n"
    "v_mul_f32 v27, v35, v35\n"
    "v_mfma_f32_16x16x32_bf16 v[108:111], a[212:215], a[188:191], v[108:111]\n"
    "s_waitcnt lgkmcnt(4)\n"
    "v_mul_f32 v28, v64, v64\n"
    "v_mul_f32 v29, v65, v65\n"
    "v_mfma_f32_16x16x32_bf16 v[112:115], a[216:219], a[160:163], v[112:115]\n"
    "ds_read_b64_tr_b16 a[200:201], v8 offset:4096\n"
    "ds_read_b64_tr_b16 a[202:203], v8 offset:4352\n"
    "v_mfma_f32_16x16x32_bf16 v[112:115], a[220:223], a[176:179], v[112:115]\n"
    "v_mul_f32 v30, v66, v66\n"
    "s_waitcnt vmcnt(0)\n"
    "v_mul_f32 v31, v67, v67\n"
    "v_mfma_f32_16x16x32_bf16 v[116:119], a[216:219], a[164:167], v[116:119]\n"
    "v_fma_f32 v24, v24, s90, v12\n"
    "v_fma_f32 v25, v25, s90, v12\n"
    "v_mfma_f32_16x16x32_bf16 v[116:119], a[220:223], a[180:183], v[116:119]\n"
    "v_fma_f32 v26, v26, s90, v12\n"
    "v_fma_f32 v27, v27, s90, v12\n"
    "v_mfma_f32_16x16x32_bf16 v[120:123], a[216:219], a[168:171], v[120:123]\n"
    "ds_read_b64_tr_b16 a[204:205], v8 offset:26624\n"
    "ds_read_b64_tr_b16 a[206:207], v8 offset:26880\n"
    "v_mfma_f32_16x16x32_bf16 v[120:123], a[220:223], a[184:187], v[120:123]\n"
    "v_fma_f32 v28, v28, s90, v12\n"
    "v_fma_f32 v29, v29, s90, v12\n"
    "v_mfma_f32_16x16x32_bf16 v[124:127], a[216:219], a[172:175], v[124:127]\n"
    "v_fma_f32 v30, v30, s90, v12\n"
    "v_fma_f32 v31, v31, s90, v12\n"
    "v_mfma_f32_16x16x32_bf16 v[124:127], a[220:223], a[188:191], v[124:127]\n"
    "s_waitcnt lgkmcnt(4)\n"
    "v_mul_f32 v24, v24, v32\n"
    "v_mul_f32 v25, v25, v33\n"
    "v_mfma_f32_16x16x32_bf16 v[128:131], a[192:195], a[160:163], v[128:131]\n"
    "ds_read_b64_tr_b16 a[208:209], v7 offset:2048\n"
    "ds_read_b64_tr_b16 a[210:211], v7 offset:2304\n"
    "v_mfma_f32_16x16x32_bf16 v[128:131], a[196:199], a[176:179], v[128:131]\n"
    "v_mul_f32 v26, v26, v34\n"
    "v_mul_f32 v27, v27, v35\n"
    "v_mfma_f32_16x16x32_bf16 v[132:135], a[192:195], a[164:167], v[132:135]\n"
    "v_mul_f32 v28, v28, v64\n"
    "v_mul_f32 v29, v29, v65\n"
    "v_mfma_f32_16x16x32_bf16 v[132:135], a[196:199], a[180:183], v[132:135]\n"
    "v_mul_f32 v30, v30, v66\n"
    "v_mul_f32 v31, v31, v67\n"
    "v_mfma_f32_16x16x32_bf16 v[136:139], a[192:195], a[168:171], v[136:139]\n"
    "ds_read_b64_tr_b16 a[212:213], v7 offset:24576\n"
    "ds_read_b64_tr_b16 a[214:215], v7 offset:24832\n"
    "v_mfma_f32_16x16x32_bf16 v[136:139], a[196:199], a[184:187], v[136:139]\n"
    "v_exp_f32 v24, v24\n"
    "v_mfma_f32_16x16x32_bf16 v[140:143], a[192:195], a[172:175], v[140:143]\n"
    "v_exp_f32 v25, v25\n"
    "v_mfma_f32_16x16x32_bf16 v[140:143], a[196:199], a[188:191], v[140:143]\n"
    "s_waitcnt lgkmcnt(4)\n"
    "v_mul_f32 v48, v48, v32\n"
    "v_mul_f32 v49, v49, v33\n"
    "v_mfma_f32_16x16x32_bf16 v[144:147], a[200:203], a[160:163], v[144:147]\n"
    "ds_read_b64_tr_b16 a[216:217], v7 offset:5120\n"
    "ds_read_b64_tr_b16 a[218:219], v7 offset:5376\n"
    "v_mfma_f32_16x16x32_bf16 v[144:147], a[204:207], a[176:179], v[144:147]\n"
    "v_exp_f32 v26, v26\n"
    "v_mfma_f32_16x16x32_bf16 v[148:151], a[200:203], a[164:167], v[148:151]\n"
    "v_exp_f32 v27, v27\n"
    "v_mfma_f32_16x16x32_bf16 v[148:151], a[204:207], a[180:183], v[148:151]\n"
    "v_mul_f32 v50, v50, v34\n"
    "v_mul_f32 v51, v51, v35\n"
    "v_mfma_f32_16x16x32_bf16 v[152:155], a[200:203], a[168:171], v[152:155]\n"
    "ds_read_b64_tr_b16 a[220:221], v7 offset:27648\n"
    "ds_read_b64_tr_b16 a[222:223], v7 offset:27904\n"
    "v_mfma_f32_16x16x32_bf16 v[152:155], a[204:207], a[184:187], v[152:155]\n"
    "v_exp_f32 v28, v28\n"
    "v_mfma_f32_16x16x32_bf16 v[156:159], a[200:203], a[172:175], v[156:159]\n"
    "v_exp_f32 v29, v29\n"
    "v_mfma_f32_16x16x32_bf16 v[156:159], a[204:207], a[188:191], v[156:159]\n"
    "s_waitcnt lgkmcnt(4)\n"
    "v_mul_f32 v80, v80, v64\n"
    "v_mul_f32 v81, v81, v65\n"
    "v_mfma_f32_16x16x32_bf16 v[160:163], a[208:211], a[160:163], v[160:163]\n"
    "ds_read_b64_tr_b16 a[192:193], v8 offset:2048\n"
    "ds_read_b64_tr_b16 a[194:195], v8 offset:2304\n"
    "v_mfma_f32_16x16x32_bf16 v[160:163], a[212:215], a[176:179], v[160:163]\n"
    "v_exp_f32 v30, v30\n"
    "v_mfma_f32_16x16x32_bf16 v[164:167], a[208:211], a[164:167], v[164:167]\n"
    "v_exp_f32 v31, v31\n"
    "v_mfma_f32_16x16x32_bf16 v[164:167], a[212:215], a[180:183], v[164:167]\n"
    "v_mul_f32 v82, v82, v66\n"
    "v_mul_f32 v83, v83, v67\n"
    "v_mfma_f32_16x16x32_bf16 v[168:171], a[208:211], a[168:171], v[168:171]\n"
    "ds_read_b64_tr_b16 a[196:197], v8 offset:24576\n"
    "ds_read_b64_tr_b16 a[198:199], v8 offset:24832\n"
    "v_mfma_f32_16x16x32_bf16 v[168:171], a[212:215], a[184:187], v[168:171]\n"
    "v_add_f32 v24, 1.0, v24\n"
    "v_add_f32 v25, 1.0, v25\n"
    "v_mfma_f32_16x16x32_bf16 v[172:175], a[208:211], a[172:175], v[172:175]\n"
    "v_add_f32 v26, 1.0, v26\n"
    "v_add_f32 v27, 1.0, v27\n"
    "v_mfma_f32_16x16x32_bf16 v[172:175], a[212:215], a[188:191], v[172:175]\n"
    "s_waitcnt lgkmcnt(4)\n"
    "v_add_f32 v28, 1.0, v28\n"
    "v_add_f32 v29, 1.0, v29\n"
    "v_mfma_f32_16x16x32_bf16 v[176:179], a[216:219], a[160:163], v[176:179]\n"
    "ds_read_b64_tr_b16 a[200:201], v8 offset:5120\n"
    "ds_read_b64_tr_b16 a[202:203], v8 offset:5376\n"
    "v_mfma_f32_16x16x32_bf16 v[176:179], a[220:223], a[176:179], v[176:179]\n"
    "v_add_f32 v30, 1.0, v30\n"
    "v_add_f32 v31, 1.0, v31\n"
    "v_mfma_f32_16x16x32_bf16 v[180:183], a[216:219], a[164:167], v[180:183]\n"
    "v_rcp_f32 v24, v24\n"
    "v_mfma_f32_16x16x32_bf16 v[180:183], a[220:223], a[180:183], v[180:183]\n"
    "v_rcp_f32 v25, v25\n"
    "v_mfma_f32_16x16x32_bf16 v[184:187], a[216:219], a[168:171], v[184:187]\n"
    "ds_read_b64_tr_b16 a[204:205], v8 offset:27648\n"
    "ds_read_b64_tr_b16 a[206:207], v8 offset:27904\n"
    "v_mfma_f32_16x16x32_bf16 v[184:187], a[220:223], a[184:187], v[184:187]\n"
    "v_rcp_f32 v26, v26\n"
    "v_mfma_f32_16x16x32_bf16 v[188:191], a[216:219], a[172:175], v[188:191]\n"
    "v_rcp_f32 v27, v27\n"
    "v_mfma_f32_16x16x32_bf16 v[188:191], a[220:223], a[188:191], v[188:191]\n"
    "s_waitcnt lgkmcnt(4)\n"
    "v_rcp_f32 v28, v28\n"
    "v_mfma_f32_16x16x32_bf16 v[192:195], a[192:195], a[160:163], v[192:195]\n"
    "ds_read_b64_tr_b16 a[208:209], v9 offset:20480\n"
    "ds_read_b64_tr_b16 a[210:211], v10 offset:20480\n"
    "v_mfma_f32_16x16x32_bf16 v[192:195], a[196:199], a[176:179], v[192:195]\n"
    "v_rcp_f32 v29, v29\n"
    "v_mfma_f32_16x16x32_bf16 v[196:199], a[192:195], a[164:167], v[196:199]\n"
    "v_rcp_f32 v30, v30\n"
    "v_mfma_f32_16x16x32_bf16 v[196:199], a[196:199], a[180:183], v[196:199]\n"
    "v_rcp_f32 v31, v31\n"
    "v_mfma_f32_16x16x32_bf16 v[200:203], a[192:195], a[168:171], v[200:203]\n"
    "ds_read_b64_tr_b16 a[212:213], v9 offset:43008\n"
    "ds_read_b64_tr_b16 a[214:215], v10 offset:43008\n"
    "v_mfma_f32_16x16x32_bf16 v[200:203], a[196:199], a[184:187], v[200:203]\n"
    "v_mul_f32 v48, v48, v24\n"
    "v_mul_f32 v49, v49, v25\n"
    "v_mfma_f32_16x16x32_bf16 v[204:207], a[192:195], a[172:175], v[204:207]\n"
    "v_mul_f32 v50, v50, v26\n"
    "v_mul_f32 v51, v51, v27\n"
    "v_mfma_f32_16x16x32_bf16 v[204:207], a[196:199], a[188:191], v[204:207]\n"
    "s_waitcnt lgkmcnt(4)\n"
    "v_mul_f32 v80, v80, v28\n"
    "v_mul_f32 v81, v81, v29\n"
    "v_mfma_f32_16x16x32_bf16 v[208:211], a[200:203], a[160:163], v[208:211]\n"
    "ds_read_b64_tr_b16 a[216:217], v9 offset:21504\n"
    "ds_read_b64_tr_b16 a[218:219], v10 offset:21504\n"
    "v_mfma_f32_16x16x32_bf16 v[208:211], a[204:207], a[176:179], v[208:211]\n"
    "v_mul_f32 v82, v82, v30\n"
    "v_mul_f32 v83, v83, v31\n"
    "v_mfma_f32_16x16x32_bf16 v[212:215], a[200:203], a[164:167], v[212:215]\n"
    "v_cvt_pk_bf16_f32 v32, v48, v49\n"
    "v_cvt_pk_bf16_f32 v33, v50, v51\n"
    "v_mfma_f32_16x16x32_bf16 v[212:215], a[204:207], a[180:183], v[212:215]\n"
    "v_cvt_pk_bf16_f32 v34, v80, v81\n"
    "v_cvt_pk_bf16_f32 v35, v82, v83\n"
    "v_mfma_f32_16x16x32_bf16 v[216:219], a[200:203], a[168:171], v[216:219]\n"
    "ds_read_b64_tr_b16 a[220:221], v9 offset:44032\n"
    "ds_read_b64_tr_b16 a[222:223], v10 offset:44032\n"
    "v_mfma_f32_16x16x32_bf16 v[216:219], a[204:207], a[184:187], v[216:219]\n"
    "buffer_store_dwordx4 v[32:35], v2, s[56:59], s76 offen offset:0 sc1\n"
    "v_mul_f32 v24, v36, v36\n"
    "v_mfma_f32_16x16x32_bf16 v[220:223], a[200:203], a[172:175], v[220:223]\n"
    "v_mul_f32 v25, v37, v37\n"
    "v_mul_f32 v26, v38, v38\n"
    "v_mfma_f32_16x16x32_bf16 v[220:223], a[204:207], a[188:191], v[220:223]\n"
    "s_waitcnt lgkmcnt(4)\n"
    "v_mul_f32 v27, v39, v39\n"
    "v_mul_f32 v28, v68, v68\n"
    "v_mfma_f32_16x16x32_bf16 v[224:227], a[208:211], a[160:163], v[224:227]\n"
    "ds_read_b64_tr_b16 a[192:193], v22 offset:6144\n"
    "ds_read_b64_tr_b16 a[194:195], v22 offset:6400\n"
    "v_mfma_f32_16x16x32_bf16 v[224:227], a[212:215], a[176:179], v[224:227]\n"
    "v_mul_f32 v29, v69, v69\n"
    "v_mul_f32 v30, v70, v70\n"
    "v_mfma_f32_16x16x32_bf16 v[228:231], a[208:211], a[164:167], v[228:231]\n"
    "v_lshrrev_b32 v14, 4, v1\n"
    "v_mul_f32 v31, v71, v71\n"
    "v_mfma_f32_16x16x32_bf16 v[228:231], a[212:215], a[180:183], v[228:231]\n"
    "v_and_b32 v15, 15, v1\n"
    "v_fma_f32 v24, v24, s90, v12\n"
    "v_mfma_f32_16x16x32_bf16 v[232:235], a[208:211], a[168:171], v[232:235]\n"
    "ds_read_b64_tr_b16 a[196:197], v22 offset:28672\n"
    "ds_read_b64_tr_b16 a[198:199], v22 offset:28928\n"
    "v_mfma_f32_16x16x32_bf16 v[232:235], a[212:215], a[184:187], v[232:235]\n"
    "v_xor_b32 v15, v15, v14\n"
    "v_lshlrev_b32 v15, 4, v15\n"
    "v_mfma_f32_16x16x32_bf16 v[236:239], a[208:211], a[172:175], v[236:239]\n"
    "s_lshl_b32 s86, s40, 12\n"
    "v_fma_f32 v25, v25, s90, v12\n"
    "v_mfma_f32_16x16x32_bf16 v[236:239], a[212:215], a[188:191], v[236:239]\n"
    "s_waitcnt lgkmcnt(4)\n"
    "v_lshl_add_u32 v14, v14, 8, s86\n"
    "v_fma_f32 v26, v26, s90, v12\n"
    "v_mfma_f32_16x16x32_bf16 v[240:243], a[216:219], a[160:163], v[240:243]\n"
    "ds_read_b64_tr_b16 a[200:201], v22 offset:6152\n"
    "ds_read_b64_tr_b16 a[202:203], v22 offset:6408\n"
    "v_mfma_f32_16x16x32_bf16 v[240:243], a[220:223], a[176:179], v[240:243]\n"
    "v_xor_b32 v10, 0, v15\n"
    "v_add_u32 v3, v14, v10\n"
    "v_mfma_f32_16x16x32_bf16 v[244:247], a[216:219], a[164:167], v[244:247]\n"
    "v_xor_b32 v10, 64, v15\n"
    "v_fma_f32 v27, v27, s90, v12\n"
    "v_mfma_f32_16x16x32_bf16 v[244:247], a[220:223], a[180:183], v[244:247]\n"
    "v_add_u32 v4, v14, v10\n"
    "v_fma_f32 v28, v28, s90, v12\n"
    "v_mfma_f32_16x16x32_bf16 v[248:251], a[216:219], a[168:171], v[248:251]\n"
    "ds_read_b64_tr_b16 a[204:205], v22 offset:28680\n"
    "ds_read_b64_tr_b16 a[206:207], v22 offset:28936\n"
    "v_mfma_f32_16x16x32_bf16 v[248:251], a[220:223], a[184:187], v[248:251]\n"
    "v_xor_b32 v10, 128, v15\n"
    "v_add_u32 v5, v14, v10\n"
    "v_mfma_f32_16x16x32_bf16 v[252:255], a[216:219], a[172:175], v[252:255]\n"
    "v_xor_b32 v10, 192, v15\n"
    "v_fma_f32 v29, v29, s90, v12\n"
    "v_mfma_f32_16x16x32_bf16 v[252:255], a[220:223], a[188:191], v[252:255]\n"
    "s_waitcnt lgkmcnt(4)\n"
    "v_add_u32 v6, v14, v10\n"
    "v_fma_f32 v30, v30, s90, v12\n"
    "v_mfma_f32_16x16x32_bf16 a[0:3], a[192:195], a[160:163], a[0:3]\n"
    "ds_read_b64_tr_b16 a[208:209], v22 offset:7168\n"
    "ds_read_b64_tr_b16 a[210:211], v22 offset:7424\n"
    "v_mfma_f32_16x16x32_bf16 a[0:3], a[196:199], a[176:179], a[0:3]\n"
    "v_bfe_u32 v14, v1, 2, 3\n"
    "v_lshrrev_b32 v15, 2, v14\n"
    "v_mfma_f32_16x16x32_bf16 a[4:7], a[192:195], a[164:167], a[4:7]\n"
    "v_lshlrev_b32 v15, 1, v15\n"
    "v_fma_f32 v31, v31, s90, v12\n"
    "v_mfma_f32_16x16x32_bf16 a[4:7], a[196:199], a[180:183], a[4:7]\n"
    "v_and_b32 v10, 3, v1\n"
    "v_mul_f32 v24, v24, v36\n"
    "v_mfma_f32_16x16x32_bf16 a[8:11], a[192:195], a[168:171], a[8:11]\n"
    "ds_read_b64_tr_b16 a[212:213], v22 offset:29696\n"
    "ds_read_b64_tr_b16 a[214:215], v22 offset:29952\n"
    "v_mfma_f32_16x16x32_bf16 a[8:11], a[196:199], a[184:187], a[8:11]\n"
    "v_xor_b32 v10, v10, v15\n"
    "v_lshlrev_b32 v10, 4, v10\n"
    "v_mfma_f32_16x16x32_bf16 a[12:15], a[192:195], a[172:175], a[12:15]\n"
    "v_lshrrev_b32 v15, 5, v1\n"
    "v_mul_f32 v25, v25, v37\n"
    "v_mfma_f32_16x16x32_bf16 a[12:15], a[196:199], a[188:191], a[12:15]\n"
    "s_waitcnt lgkmcnt(4)\n"
    "v_lshl_add_u32 v10, v15, 6, v10\n"
    "v_mul_f32 v26, v26, v38\n"
    "v_mfma_f32_16x16x32_bf16 a[16:19], a[200:203], a[160:163], a[16:19]\n"
    "ds_read_b64_tr_b16 a[216:217], v22 offset:7176\n"
    "ds_read_b64_tr_b16 a[218:219], v22 offset:7432\n"
    "v_mfma_f32_16x16x32_bf16 a[16:19], a[204:207], a[176:179], a[16:19]\n"
    "v_lshl_add_u32 v8, v14, 11, v10\n"
    "v_lshl_add_u32 v14, v14, 8, v10\n"
    "v_mfma_f32_16x16x32_bf16 a[20:23], a[200:203], a[164:167], a[20:23]\n"
    "v_add_u32 v7, s86, v14\n"
    "v_mul_f32 v27, v27, v39\n"
    "v_mfma_f32_16x16x32_bf16 a[20:23], a[204:207], a[180:183], a[20:23]\n"
    "v_mov_b32 v9, v7\n"
    "v_mul_f32 v28, v28, v68\n"
    "v_mfma_f32_16x16x32_bf16 a[24:27], a[200:203], a[168:171], a[24:27]\n"
    "ds_read_b64_tr_b16 a[220:221], v22 offset:29704\n"
    "ds_read_b64_tr_b16 a[222:223], v22 offset:29960\n"
    "v_mfma_f32_16x16x32_bf16 a[24:27], a[204:207], a[184:187], a[24:27]\n"
    "s_add_u32 s81, s86, s39\n"
    "s_mov_b32 s80, 0\n"
    "v_mfma_f32_16x16x32_bf16 a[28:31], a[200:203], a[172:175], a[28:31]\n"
    "v_and_b32 v14, 15, v1\n"
    "v_mul_f32 v29, v29, v69\n"
    "v_mfma_f32_16x16x32_bf16 a[28:31], a[204:207], a[188:191], a[28:31]\n"
    "s_waitcnt lgkmcnt(4)\n"
    "v_lshrrev_b32 v15, 4, v1\n"
    "v_mul_f32 v30, v30, v70\n"
    "v_mfma_f32_16x16x32_bf16 a[32:35], a[208:211], a[160:163], a[32:35]\n"
    "ds_read_b64_tr_b16 a[192:193], v22 offset:8192\n"
    "ds_read_b64_tr_b16 a[194:195], v22 offset:8448\n"
    "v_mfma_f32_16x16x32_bf16 a[32:35], a[212:215], a[176:179], a[32:35]\n"
    "v_xor_b32 v15, v15, v14\n"
    "v_lshlrev_b32 v15, 4, v15\n"
    "v_mfma_f32_16x16x32_bf16 a[36:39], a[208:211], a[164:167], a[36:39]\n"
    "v_lshl_add_u32 v14, v14, 8, s39\n"
    "v_mul_f32 v31, v31, v71\n"
    "v_mfma_f32_16x16x32_bf16 a[36:39], a[212:215], a[180:183], a[36:39]\n"
    "v_xor_b32 v10, 0, v15\n"
    "v_mfma_f32_16x16x32_bf16 a[40:43], a[208:211], a[168:171], a[40:43]\n"
    "ds_read_b64_tr_b16 a[196:197], v22 offset:30720\n"
    "ds_read_b64_tr_b16 a[198:199], v22 offset:30976\n"
    "v_mfma_f32_16x16x32_bf16 a[40:43], a[212:215], a[184:187], a[40:43]\n"
    "v_add_u32 v16, v14, v10\n"
    "v_xor_b32 v10, 64, v15\n"
    "v_mfma_f32_16x16x32_bf16 a[44:47], a[208:211], a[172:175], a[44:47]\n"
    "v_add_u32 v17, v14, v10\n"
    "v_mfma_f32_16x16x32_bf16 a[44:47], a[212:215], a[188:191], a[44:47]\n"
    "s_waitcnt lgkmcnt(4)\n"
    "v_xor_b32 v10, 128, v15\n"
    "v_mfma_f32_16x16x32_bf16 a[48:51], a[216:219], a[160:163], a[48:51]\n"
    "ds_read_b64_tr_b16 a[200:201], v22 offset:8200\n"
    "ds_read_b64_tr_b16 a[202:203], v22 offset:8456\n"
    "v_mfma_f32_16x16x32_bf16 a[48:51], a[220:223], a[176:179], a[48:51]\n"
    "v_add_u32 v18, v14, v10\n"
    "v_xor_b32 v10, 192, v15\n"
    "v_mfma_f32_16x16x32_bf16 a[52:55], a[216:219], a[164:167], a[52:55]\n"
    "v_add_u32 v19, v14, v10\n"
    "v_mfma_f32_16x16x32_bf16 a[52:55], a[220:223], a[180:183], a[52:55]\n"
    "v_lshrrev_b32 v14, 5, v1\n"
    "v_mfma_f32_16x16x32_bf16 a[56:59], a[216:219], a[168:171], a[56:59]\n"
    "ds_read_b64_tr_b16 a[204:205], v22 offset:30728\n"
    "ds_read_b64_tr_b16 a[206:207], v22 offset:30984\n"
    "v_mfma_f32_16x16x32_bf16 a[56:59], a[220:223], a[184:187], a[56:59]\n"
    "v_lshlrev_b32 v14, 11, v14\n"
    "v_bfe_u32 v15, v1, 2, 3\n"
    "v_mfma_f32_16x16x32_bf16 a[60:63], a[216:219], a[172:175], a[60:63]\n"
    "v_lshl_add_u32 v14, v15, 6, v14\n"
    "v_mfma_f32_16x16x32_bf16 a[60:63], a[220:223], a[188:191], a[60:63]\n"
    "s_waitcnt lgkmcnt(4)\n"
    "v_and_b32 v15, 3, v1\n"
    "v_mfma_f32_16x16x32_bf16 a[64:67], a[192:195], a[160:163], a[64:67]\n"
    "ds_read_b64_tr_b16 a[208:209], v22 offset:9216\n"
    "ds_read_b64_tr_b16 a[210:211], v22 offset:9472\n"
    "v_mfma_f32_16x16x32_bf16 a[64:67], a[196:199], a[176:179], a[64:67]\n"
    "v_lshl_add_u32 v14, v15, 3, v14\n"
    "s_add_u32 s86, s39, 0x10000\n"
    "v_mfma_f32_16x16x32_bf16 a[68:71], a[192:195], a[164:167], a[68:71]\n"
    "v_add_u32 v14, s86, v14\n"
    "v_mfma_f32_16x16x32_bf16 a[68:71], a[196:199], a[180:183], a[68:71]\n"
    "v_bfe_u32 v15, v1, 4, 1\n"
    "v_mfma_f32_16x16x32_bf16 a[72:75], a[192:195], a[168:171], a[72:75]\n"
    "ds_read_b64_tr_b16 a[212:213], v22 offset:31744\n"
    "ds_read_b64_tr_b16 a[214:215], v22 offset:32000\n"
    "v_mfma_f32_16x16x32_bf16 a[72:75], a[196:199], a[184:187], a[72:75]\n"
    "v_lshl_add_u32 v20, v15, 5, v14\n"
    "v_xor_b32 v21, 32, v20\n"
    "v_mfma_f32_16x16x32_bf16 a[76:79], a[192:195], a[172:175], a[76:79]\n"
    "s_mov_b32 s92, 0x3e0293ee\n"
    "v_mfma_f32_16x16x32_bf16 a[76:79], a[196:199], a[188:191], a[76:79]\n"
    "s_waitcnt lgkmcnt(4)\n"
    "s_mov_b32 s93, 0x3e0293ee\n"
    "v_mfma_f32_16x16x32_bf16 a[80:83], a[200:203], a[160:163], a[80:83]\n"
    "ds_read_b64_tr_b16 a[216:217], v22 offset:9224\n"
    "ds_read_b64_tr_b16 a[218:219], v22 offset:9480\n"
    "v_mfma_f32_16x16x32_bf16 a[80:83], a[204:207], a[176:179], a[80:83]\n"
    "v_exp_f32 v24, v24\n"
    "v_mfma_f32_16x16x32_bf16 a[84:87], a[200:203], a[164:167], a[84:87]\n"
    "v_exp_f32 v25, v25\n"
    "v_mfma_f32_16x16x32_bf16 a[84:87], a[204:207], a[180:183], a[84:87]\n"
    "v_mul_f32 v52, v52, v36\n"
    "v_mul_f32 v53, v53, v37\n"
    "v_mfma_f32_16x16x32_bf16 a[88:91], a[200:203], a[168:171], a[88:91]\n"
    "ds_read_b64_tr_b16 a[220:221], v22 offset:31752\n"
    "ds_read_b64_tr_b16 a[222:223], v22 offset:32008\n"
    "v_mfma_f32_16x16x32_bf16 a[88:91], a[204:207], a[184:187], a[88:91]\n"
    "v_exp_f32 v26, v26\n"
    "v_mfma_f32_16x16x32_bf16 a[92:95], a[200:203], a[172:175], a[92:95]\n"
    "v_exp_f32 v27, v27\n"
    "v_mfma_f32_16x16x32_bf16 a[92:95], a[204:207], a[188:191], a[92:95]\n"
    "s_waitcnt lgkmcnt(4)\n"
    "v_mul_f32 v54, v54, v38\n"
    "v_mul_f32 v55, v55, v39\n"
    "v_mfma_f32_16x16x32_bf16 a[96:99], a[208:211], a[160:163], a[96:99]\n"
    "v_exp_f32 v28, v28\n"
    "v_mfma_f32_16x16x32_bf16 a[96:99], a[212:215], a[176:179], a[96:99]\n"
    "v_exp_f32 v29, v29\n"
    "v_mfma_f32_16x16x32_bf16 a[100:103], a[208:211], a[164:167], a[100:103]\n"
    "s_waitcnt lgkmcnt(0)\n"
    "s_barrier\n"
    "v_mul_f32 v84, v84, v68\n"
    "v_mul_f32 v85, v85, v69\n"
    "v_mfma_f32_16x16x32_bf16 a[100:103], a[212:215], a[180:183], a[100:103]\n"
    "s_add_u32 m0, s81, 0\n"
    "v_mfma_f32_16x16x32_bf16 a[104:107], a[208:211], a[168:171], a[104:107]\n"
    "buffer_load_dwordx4 v3, s[60:63], s80 offen offset:0 sc1 lds\n"
    "v_mfma_f32_16x16x32_bf16 a[104:107], a[212:215], a[184:187], a[104:107]\n"
    "buffer_load_dwordx4 v4, s[60:63], s80 offen offset:1024 sc1 lds\n"
    "v_mfma_f32_16x16x32_bf16 a[108:111], a[208:211], a[172:175], a[108:111]\n"
    "buffer_load_dwordx4 v5, s[60:63], s80 offen offset:2048 sc1 lds\n"
    "v_mfma_f32_16x16x32_bf16 a[108:111], a[212:215], a[188:191], a[108:111]\n"
    "buffer_load_dwordx4 v6, s[60:63], s80 offen offset:3072 sc1 lds\n"
    "v_mfma_f32_16x16x32_bf16 a[112:115], a[216:219], a[160:163], a[112:115]\n"
    "s_add_u32 m0, s81, 65536\n"
    "v_mfma_f32_16x16x32_bf16 a[112:115], a[220:223], a[176:179], a[112:115]\n"
    "buffer_load_dwordx4 v7, s[64:67], s80 offen offset:0 sc1 lds\n"
    "v_mfma_f32_16x16x32_bf16 a[116:119], a[216:219], a[164:167], a[116:119]\n"
    "s_add_u32 m0, s81, 66432\n"
    "v_mfma_f32_16x16x32_bf16 a[116:119], a[220:223], a[180:183], a[116:119]\n"
    "buffer_load_dwordx4 v9, s[64:67], s80 offen offset:128 sc1 lds\n"
    "v_mfma_f32_16x16x32_bf16 a[120:123], a[216:219], a[168:171], a[120:123]\n"
    "s_add_u32 m0, s81, 65536\n"
    "v_mfma_f32_16x16x32_bf16 a[120:123], a[220:223], a[184:187], a[120:123]\n"
    "buffer_load_dwordx4 v7, s[64:67], s80 offen offset:2048 sc1 lds\n"
    "v_mfma_f32_16x16x32_bf16 a[124:127], a[216:219], a[172:175], a[124:127]\n"
    "s_add_u32 m0, s81, 66432\n"
    "v_mfma_f32_16x16x32_bf16 a[124:127], a[220:223], a[188:191], a[124:127]\n"
    "buffer_load_dwordx4 v9, s[64:67], s80 offen offset:2176 sc1 lds\n"
    "s_add_u32 s80, s80, 16384\n"
    "v_exp_f32 v30, v30\n"
    "s_add_u32 m0, s81, 16384\n"
    "v_exp_f32 v31, v31\n"
    "v_mul_f32 v86, v86, v70\n"
    "v_mul_f32 v87, v87, v71\n"
    "v_add_f32 v24, 1.0, v24\n"
    "v_add_f32 v25, 1.0, v25\n"
    "v_add_f32 v26, 1.0, v26\n"
    "v_add_f32 v27, 1.0, v27\n"
    "v_add_f32 v28, 1.0, v28\n"
    "v_add_f32 v29, 1.0, v29\n"
    "v_add_f32 v30, 1.0, v30\n"
    "buffer_load_dwordx4 v3, s[60:63], s80 offen offset:0 sc1 lds\n"
    "v_add_f32 v31, 1.0, v31\n"
    "v_rcp_f32 v24, v24\n"
    "v_rcp_f32 v25, v25\n"
    "v_rcp_f32 v26, v26\n"
    "v_rcp_f32 v27, v27\n"
    "v_rcp_f32 v28, v28\n"
    "v_rcp_f32 v29, v29\n"
    "v_rcp_f32 v30, v30\n"
    "v_rcp_f32 v31, v31\n"
    "v_mul_f32 v52, v52, v24\n"
    "buffer_load_dwordx4 v4, s[60:63], s80 offen offset:1024 sc1 lds\n"
    "v_mul_f32 v53, v53, v25\n"
    "v_mul_f32 v54, v54, v26\n"
    "v_mul_f32 v55, v55, v27\n"
    "v_mul_f32 v84, v84, v28\n"
    "v_mul_f32 v85, v85, v29\n"
    "v_mul_f32 v86, v86, v30\n"
    "v_mul_f32 v87, v87, v31\n"
    "v_cvt_pk_bf16_f32 v36, v52, v53\n"
    "v_cvt_pk_bf16_f32 v37, v54, v55\n"
    "v_cvt_pk_bf16_f32 v38, v84, v85\n"
    "buffer_load_dwordx4 v5, s[60:63], s80 offen offset:2048 sc1 lds\n"
    "v_cvt_pk_bf16_f32 v39, v86, v87\n"
    "buffer_store_dwordx4 v[36:39], v2, s[56:59], s76 offen offset:1024 sc1\n"
    "v_pk_mul_f32 v[24:25], v[40:41], v[40:41]\n"
    "v_pk_mul_f32 v[26:27], v[42:43], v[42:43]\n"
    "v_pk_mul_f32 v[28:29], v[72:73], v[72:73]\n"
    "v_pk_mul_f32 v[30:31], v[74:75], v[74:75]\n"
    "v_pk_fma_f32 v[24:25], v[24:25], s[90:91], v[12:13]\n"
    "v_pk_fma_f32 v[26:27], v[26:27], s[90:91], v[12:13]\n"
    "v_pk_fma_f32 v[28:29], v[28:29], s[90:91], v[12:13]\n"
    "v_pk_fma_f32 v[30:31], v[30:31], s[90:91], v[12:13]\n"
    "buffer_load_dwordx4 v6, s[60:63], s80 offen offset:3072 sc1 lds\n"
    "s_add_u32 m0, s81, 81920\n"
    "v_pk_mul_f32 v[24:25], v[24:25], v[40:41]\n"
    "v_pk_mul_f32 v[26:27], v[26:27], v[42:43]\n"
    "v_pk_mul_f32 v[28:29], v[28:29], v[72:73]\n"
    "v_pk_mul_f32 v[30:31], v[30:31], v[74:75]\n"
    "v_exp_f32 v24, v24\n"
    "v_exp_f32 v25, v25\n"
    "v_pk_mul_f32 v[56:57], v[56:57], v[40:41]\n"
    "v_exp_f32 v26, v26\n"
    "v_exp_f32 v27, v27\n"
    "v_pk_mul_f32 v[58:59], v[58:59], v[42:43]\n"
    "buffer_load_dwordx4 v7, s[64:67], s80 offen offset:0 sc1 lds\n"
    "s_add_u32 m0, s81, 82816\n"
    "v_exp_f32 v28, v28\n"
    "v_exp_f32 v29, v29\n"
    "v_pk_mul_f32 v[88:89], v[88:89], v[72:73]\n"
    "v_exp_f32 v30, v30\n"
    "v_exp_f32 v31, v31\n"
    "v_pk_mul_f32 v[90:91], v[90:91], v[74:75]\n"
    "v_pk_add_f32 v[24:25], v[24:25], 1.0 op_sel_hi:[1,0]\n"
    "v_pk_add_f32 v[26:27], v[26:27], 1.0 op_sel_hi:[1,0]\n"
    "v_pk_add_f32 v[28:29], v[28:29], 1.0 op_sel_hi:[1,0]\n"
    "v_pk_add_f32 v[30:31], v[30:31], 1.0 op_sel_hi:[1,0]\n"
    "buffer_load_dwordx4 v9, s[64:67], s80 offen offset:128 sc1 lds\n"
    "s_add_u32 m0, s81, 81920\n"
    "v_rcp_f32 v24, v24\n"
    "v_rcp_f32 v25, v25\n"
    "v_rcp_f32 v26, v26\n"
    "v_rcp_f32 v27, v27\n"
    "v_rcp_f32 v28, v28\n"
    "v_rcp_f32 v29, v29\n"
    "v_rcp_f32 v30, v30\n"
    "v_rcp_f32 v31, v31\n"
    "v_pk_mul_f32 v[56:57], v[56:57], v[24:25]\n"
    "v_pk_mul_f32 v[58:59], v[58:59], v[26:27]\n"
    "buffer_load_dwordx4 v7, s[64:67], s80 offen offset:2048 sc1 lds\n"
    "s_add_u32 m0, s81, 82816\n"
    "v_pk_mul_f32 v[88:89], v[88:89], v[28:29]\n"
    "v_pk_mul_f32 v[90:91], v[90:91], v[30:31]\n"
    "v_cvt_pk_bf16_f32 v40, v56, v57\n"
    "v_cvt_pk_bf16_f32 v41, v58, v59\n"
    "v_cvt_pk_bf16_f32 v42, v88, v89\n"
    "v_cvt_pk_bf16_f32 v43, v90, v91\n"
    "buffer_store_dwordx4 v[40:43], v2, s[56:59], s77 offen offset:0 sc1\n"
    "v_pk_mul_f32 v[24:25], v[44:45], v[44:45]\n"
    "v_pk_mul_f32 v[26:27], v[46:47], v[46:47]\n"
    "v_pk_mul_f32 v[28:29], v[76:77], v[76:77]\n"
    "buffer_load_dwordx4 v9, s[64:67], s80 offen offset:2176 sc1 lds\n"
    "s_add_u32 s80, s80, 16384\n"
    "v_pk_mul_f32 v[30:31], v[78:79], v[78:79]\n"
    "v_pk_fma_f32 v[24:25], v[24:25], s[90:91], v[12:13]\n"
    "v_pk_fma_f32 v[26:27], v[26:27], s[90:91], v[12:13]\n"
    "v_pk_fma_f32 v[28:29], v[28:29], s[90:91], v[12:13]\n"
    "v_pk_fma_f32 v[30:31], v[30:31], s[90:91], v[12:13]\n"
    "v_pk_mul_f32 v[24:25], v[24:25], v[44:45]\n"
    "v_pk_mul_f32 v[26:27], v[26:27], v[46:47]\n"
    "v_pk_mul_f32 v[28:29], v[28:29], v[76:77]\n"
    "v_pk_mul_f32 v[30:31], v[30:31], v[78:79]\n"
    "v_exp_f32 v24, v24\n"
    "s_add_u32 m0, s81, 32768\n"
    "v_exp_f32 v25, v25\n"
    "v_pk_mul_f32 v[60:61], v[60:61], v[44:45]\n"
    "v_exp_f32 v26, v26\n"
    "v_exp_f32 v27, v27\n"
    "v_pk_mul_f32 v[62:63], v[62:63], v[46:47]\n"
    "v_exp_f32 v28, v28\n"
    "v_exp_f32 v29, v29\n"
    "v_pk_mul_f32 v[92:93], v[92:93], v[76:77]\n"
    "v_exp_f32 v30, v30\n"
    "v_exp_f32 v31, v31\n"
    "buffer_load_dwordx4 v3, s[60:63], s80 offen offset:0 sc1 lds\n"
    "v_pk_mul_f32 v[94:95], v[94:95], v[78:79]\n"
    "v_pk_add_f32 v[24:25], v[24:25], 1.0 op_sel_hi:[1,0]\n"
    "v_pk_add_f32 v[26:27], v[26:27], 1.0 op_sel_hi:[1,0]\n"
    "v_pk_add_f32 v[28:29], v[28:29], 1.0 op_sel_hi:[1,0]\n"
    "v_pk_add_f32 v[30:31], v[30:31], 1.0 op_sel_hi:[1,0]\n"
    "v_rcp_f32 v24, v24\n"
    "v_rcp_f32 v25, v25\n"
    "v_rcp_f32 v26, v26\n"
    "v_rcp_f32 v27, v27\n"
    "v_rcp_f32 v28, v28\n"
    "buffer_load_dwordx4 v4, s[60:63], s80 offen offset:1024 sc1 lds\n"
    "v_rcp_f32 v29, v29\n"
    "v_rcp_f32 v30, v30\n"
    "v_rcp_f32 v31, v31\n"
    "v_pk_mul_f32 v[60:61], v[60:61], v[24:25]\n"
    "v_pk_mul_f32 v[62:63], v[62:63], v[26:27]\n"
    "v_pk_mul_f32 v[92:93], v[92:93], v[28:29]\n"
    "v_pk_mul_f32 v[94:95], v[94:95], v[30:31]\n"
    "v_cvt_pk_bf16_f32 v44, v60, v61\n"
    "v_cvt_pk_bf16_f32 v45, v62, v63\n"
    "v_cvt_pk_bf16_f32 v46, v92, v93\n"
    "buffer_load_dwordx4 v5, s[60:63], s80 offen offset:2048 sc1 lds\n"
    "v_cvt_pk_bf16_f32 v47, v94, v95\n"
    "buffer_store_dwordx4 v[44:47], v2, s[56:59], s77 offen offset:1024 sc1\n"
    "v_pk_mul_f32 v[24:25], v[96:97], v[96:97]\n"
    "v_pk_mul_f32 v[26:27], v[98:99], v[98:99]\n"
    "v_pk_mul_f32 v[28:29], v[128:129], v[128:129]\n"
    "v_pk_mul_f32 v[30:31], v[130:131], v[130:131]\n"
    "v_pk_fma_f32 v[24:25], v[24:25], s[90:91], v[12:13]\n"
    "v_pk_fma_f32 v[26:27], v[26:27], s[90:91], v[12:13]\n"
    "v_pk_fma_f32 v[28:29], v[28:29], s[90:91], v[12:13]\n"
    "v_pk_fma_f32 v[30:31], v[30:31], s[90:91], v[12:13]\n"
    "buffer_load_dwordx4 v6, s[60:63], s80 offen offset:3072 sc1 lds\n"
    "s_add_u32 m0, s81, 98304\n"
    "v_pk_mul_f32 v[24:25], v[24:25], v[96:97]\n"
    "v_pk_mul_f32 v[26:27], v[26:27], v[98:99]\n"
    "v_pk_mul_f32 v[28:29], v[28:29], v[128:129]\n"
    "v_pk_mul_f32 v[30:31], v[30:31], v[130:131]\n"
    "v_exp_f32 v24, v24\n"
    "v_accvgpr_read_b32 v48, a0\n"
    "v_accvgpr_read_b32 v49, a1\n"
    "v_exp_f32 v25, v25\n"
    "v_accvgpr_read_b32 v50, a2\n"
    "v_accvgpr_read_b32 v51, a3\n"
    "v_pk_mul_f32 v[112:113], v[112:113], v[96:97]\n"
    "v_exp_f32 v26, v26\n"
    "v_accvgpr_read_b32 v52, a16\n"
    "v_accvgpr_read_b32 v53, a17\n"
    "v_exp_f32 v27, v27\n"
    "v_accvgpr_read_b32 v54, a18\n"
    "v_accvgpr_read_b32 v55, a19\n"
    "v_pk_mul_f32 v[114:115], v[114:115], v[98:99]\n"
    "buffer_load_dwordx4 v7, s[64:67], s80 offen offset:0 sc1 lds\n"
    "s_add_u32 m0, s81, 99200\n"
    "v_exp_f32 v28, v28\n"
    "v_pk_mul_f32 v[48:49], v[48:49], s[92:93]\n"
    "v_pk_mul_f32 v[50:51], v[50:51], s[92:93]\n"
    "v_exp_f32 v29, v29\n"
    "v_pk_mul_f32 v[52:53], v[52:53], s[92:93]\n"
    "v_pk_mul_f32 v[54:55], v[54:55], s[92:93]\n"
    "v_pk_mul_f32 v[144:145], v[144:145], v[128:129]\n"
    "v_exp_f32 v30, v30\n"
    "v_cvt_pk_bf16_f32 v56, v48, v49\n"
    "v_cvt_pk_bf16_f32 v57, v50, v51\n"
    "v_exp_f32 v31, v31\n"
    "v_cvt_pk_bf16_f32 v58, v52, v53\n"
    "v_cvt_pk_bf16_f32 v59, v54, v55\n"
    "v_pk_mul_f32 v[146:147], v[146:147], v[130:131]\n"
    "v_pk_add_f32 v[24:25], v[24:25], 1.0 op_sel_hi:[1,0]\n"
    "v_pk_add_f32 v[26:27], v[26:27], 1.0 op_sel_hi:[1,0]\n"
    "v_pk_add_f32 v[28:29], v[28:29], 1.0 op_sel_hi:[1,0]\n"
    "v_pk_add_f32 v[30:31], v[30:31], 1.0 op_sel_hi:[1,0]\n"
    "buffer_load_dwordx4 v9, s[64:67], s80 offen offset:128 sc1 lds\n"
    "s_add_u32 m0, s81, 98304\n"
    "v_rcp_f32 v24, v24\n"
    "v_accvgpr_write_b32 a128, v56\n"
    "v_accvgpr_write_b32 a129, v57\n"
    "v_rcp_f32 v25, v25\n"
    "v_accvgpr_write_b32 a130, v58\n"
    "v_accvgpr_write_b32 a131, v59\n"
    "v_rcp_f32 v26, v26\n"
    "v_accvgpr_read_b32 v48, a4\n"
    "v_accvgpr_read_b32 v49, a5\n"
    "v_rcp_f32 v27, v27\n"
    "v_accvgpr_read_b32 v50, a6\n"
    "v_accvgpr_read_b32 v51, a7\n"
    "v_rcp_f32 v28, v28\n"
    "v_accvgpr_read_b32 v52, a20\n"
    "v_accvgpr_read_b32 v53, a21\n"
    "v_rcp_f32 v29, v29\n"
    "v_accvgpr_read_b32 v54, a22\n"
    "v_accvgpr_read_b32 v55, a23\n"
    "v_rcp_f32 v30, v30\n"
    "v_pk_mul_f32 v[48:49], v[48:49], s[92:93]\n"
    "v_pk_mul_f32 v[50:51], v[50:51], s[92:93]\n"
    "v_rcp_f32 v31, v31\n"
    "v_pk_mul_f32 v[52:53], v[52:53], s[92:93]\n"
    "v_pk_mul_f32 v[54:55], v[54:55], s[92:93]\n"
    "v_pk_mul_f32 v[112:113], v[112:113], v[24:25]\n"
    "v_pk_mul_f32 v[114:115], v[114:115], v[26:27]\n"
    "buffer_load_dwordx4 v7, s[64:67], s80 offen offset:2048 sc1 lds\n"
    "s_add_u32 m0, s81, 99200\n"
    "v_pk_mul_f32 v[144:145], v[144:145], v[28:29]\n"
    "v_pk_mul_f32 v[146:147], v[146:147], v[30:31]\n"
    "v_cvt_pk_bf16_f32 v96, v112, v113\n"
    "v_cvt_pk_bf16_f32 v97, v114, v115\n"
    "v_cvt_pk_bf16_f32 v98, v144, v145\n"
    "v_cvt_pk_bf16_f32 v99, v146, v147\n"
    "buffer_store_dwordx4 v[96:99], v2, s[56:59], s76 offen offset:2048 sc1\n"
    "v_pk_mul_f32 v[24:25], v[100:101], v[100:101]\n"
    "v_pk_mul_f32 v[26:27], v[102:103], v[102:103]\n"
    "v_pk_mul_f32 v[28:29], v[132:133], v[132:133]\n"
    "buffer_load_dwordx4 v9, s[64:67], s80 offen offset:2176 sc1 lds\n"
    "s_add_u32 s80, s80, 16384\n"
    "v_pk_mul_f32 v[30:31], v[134:135], v[134:135]\n"
    "v_pk_fma_f32 v[24:25], v[24:25], s[90:91], v[12:13]\n"
    "v_pk_fma_f32 v[26:27], v[26:27], s[90:91], v[12:13]\n"
    "v_pk_fma_f32 v[28:29], v[28:29], s[90:91], v[12:13]\n"
    "v_pk_fma_f32 v[30:31], v[30:31], s[90:91], v[12:13]\n"
    "v_pk_mul_f32 v[24:25], v[24:25], v[100:101]\n"
    "v_pk_mul_f32 v[26:27], v[26:27], v[102:103]\n"
    "v_pk_mul_f32 v[28:29], v[28:29], v[132:133]\n"
    "v_pk_mul_f32 v[30:31], v[30:31], v[134:135]\n"
    "v_exp_f32 v24, v24\n"
    "v_cvt_pk_bf16_f32 v56, v48, v49\n"
    "v_cvt_pk_bf16_f32 v57, v50, v51\n"
    "s_add_u32 m0, s81, 49152\n"
    "v_exp_f32 v25, v25\n"
    "v_cvt_pk_bf16_f32 v58, v52, v53\n"
    "v_cvt_pk_bf16_f32 v59, v54, v55\n"
    "v_pk_mul_f32 v[116:117], v[116:117], v[100:101]\n"
    "v_exp_f32 v26, v26\n"
    "v_accvgpr_write_b32 a144, v56\n"
    "v_accvgpr_write_b32 a145, v57\n"
    "v_exp_f32 v27, v27\n"
    "v_accvgpr_write_b32 a146, v58\n"
    "v_accvgpr_write_b32 a147, v59\n"
    "v_pk_mul_f32 v[118:119], v[118:119], v[102:103]\n"
    "v_exp_f32 v28, v28\n"
    "v_accvgpr_read_b32 v48, a8\n"
    "v_accvgpr_read_b32 v49, a9\n"
    "v_exp_f32 v29, v29\n"
    "v_accvgpr_read_b32 v50, a10\n"
    "v_accvgpr_read_b32 v51, a11\n"
    "v_pk_mul_f32 v[148:149], v[148:149], v[132:133]\n"
    "v_exp_f32 v30, v30\n"
    "v_accvgpr_read_b32 v52, a24\n"
    "v_accvgpr_read_b32 v53, a25\n"
    "v_exp_f32 v31, v31\n"
    "v_accvgpr_read_b32 v54, a26\n"
    "v_accvgpr_read_b32 v55, a27\n"
    "buffer_load_dwordx4 v3, s[60:63], s80 offen offset:0 sc1 lds\n"
    "v_pk_mul_f32 v[150:151], v[150:151], v[134:135]\n"
    "v_pk_add_f32 v[24:25], v[24:25], 1.0 op_sel_hi:[1,0]\n"
    "v_pk_add_f32 v[26:27], v[26:27], 1.0 op_sel_hi:[1,0]\n"
    "v_pk_add_f32 v[28:29], v[28:29], 1.0 op_sel_hi:[1,0]\n"
    "v_pk_add_f32 v[30:31], v[30:31], 1.0 op_sel_hi:[1,0]\n"
    "v_rcp_f32 v24, v24\n"
    "v_pk_mul_f32 v[48:49], v[48:49], s[92:93]\n"
    "v_pk_mul_f32 v[50:51], v[50:51], s[92:93]\n"
    "v_rcp_f32 v25, v25\n"
    "v_pk_mul_f32 v[52:53], v[52:53], s[92:93]\n"
    "v_pk_mul_f32 v[54:55], v[54:55], s[92:93]\n"
    "v_rcp_f32 v26, v26\n"
    "v_cvt_pk_bf16_f32 v56, v48, v49\n"
    "v_cvt_pk_bf16_f32 v57, v50, v51\n"
    "v_rcp_f32 v27, v27\n"
    "v_cvt_pk_bf16_f32 v58, v52, v53\n"
    "v_cvt_pk_bf16_f32 v59, v54, v55\n"
    "v_rcp_f32 v28, v28\n"
    "v_accvgpr_write_b32 a160, v56\n"
    "v_accvgpr_write_b32 a161, v57\n"
    "buffer_load_dwordx4 v4, s[60:63], s80 offen offset:1024 sc1 lds\n"
    "v_rcp_f32 v29, v29\n"
    "v_accvgpr_write_b32 a162, v58\n"
    "v_accvgpr_write_b32 a163, v59\n"
    "v_rcp_f32 v30, v30\n"
    "v_accvgpr_read_b32 v48, a12\n"
    "v_accvgpr_read_b32 v49, a13\n"
    "v_rcp_f32 v31, v31\n"
    "v_accvgpr_read_b32 v50, a14\n"
    "v_accvgpr_read_b32 v51, a15\n"
    "v_pk_mul_f32 v[116:117], v[116:117], v[24:25]\n"
    "v_pk_mul_f32 v[118:119], v[118:119], v[26:27]\n"
    "v_pk_mul_f32 v[148:149], v[148:149], v[28:29]\n"
    "v_pk_mul_f32 v[150:151], v[150:151], v[30:31]\n"
    "v_cvt_pk_bf16_f32 v100, v116, v117\n"
    "v_cvt_pk_bf16_f32 v101, v118, v119\n"
    "v_cvt_pk_bf16_f32 v102, v148, v149\n"
    "buffer_load_dwordx4 v5, s[60:63], s80 offen offset:2048 sc1 lds\n"
    "v_cvt_pk_bf16_f32 v103, v150, v151\n"
    "buffer_store_dwordx4 v[100:103], v2, s[56:59], s76 offen offset:3072 sc1\n"
    "v_pk_mul_f32 v[24:25], v[104:105], v[104:105]\n"
    "v_pk_mul_f32 v[26:27], v[106:107], v[106:107]\n"
    "v_pk_mul_f32 v[28:29], v[136:137], v[136:137]\n"
    "v_pk_mul_f32 v[30:31], v[138:139], v[138:139]\n"
    "v_pk_fma_f32 v[24:25], v[24:25], s[90:91], v[12:13]\n"
    "v_pk_fma_f32 v[26:27], v[26:27], s[90:91], v[12:13]\n"
    "v_pk_fma_f32 v[28:29], v[28:29], s[90:91], v[12:13]\n"
    "v_pk_fma_f32 v[30:31], v[30:31], s[90:91], v[12:13]\n"
    "buffer_load_dwordx4 v6, s[60:63], s80 offen offset:3072 sc1 lds\n"
    "s_add_u32 m0, s81, 114688\n"
    "v_pk_mul_f32 v[24:25], v[24:25], v[104:105]\n"
    "v_pk_mul_f32 v[26:27], v[26:27], v[106:107]\n"
    "v_pk_mul_f32 v[28:29], v[28:29], v[136:137]\n"
    "v_pk_mul_f32 v[30:31], v[30:31], v[138:139]\n"
    "v_exp_f32 v24, v24\n"
    "v_accvgpr_read_b32 v52, a28\n"
    "v_accvgpr_read_b32 v53, a29\n"
    "v_exp_f32 v25, v25\n"
    "v_accvgpr_read_b32 v54, a30\n"
    "v_accvgpr_read_b32 v55, a31\n"
    "v_pk_mul_f32 v[120:121], v[120:121], v[104:105]\n"
    "v_exp_f32 v26, v26\n"
    "v_pk_mul_f32 v[48:49], v[48:49], s[92:93]\n"
    "v_pk_mul_f32 v[50:51], v[50:51], s[92:93]\n"
    "v_exp_f32 v27, v27\n"
    "v_pk_mul_f32 v[52:53], v[52:53], s[92:93]\n"
    "v_pk_mul_f32 v[54:55], v[54:55], s[92:93]\n"
    "v_pk_mul_f32 v[122:123], v[122:123], v[106:107]\n"
    "buffer_load_dwordx4 v7, s[64:67], s80 offen offset:0 sc1 lds\n"
    "s_add_u32 m0, s81, 115584\n"
    "v_exp_f32 v28, v28\n"
    "v_cvt_pk_bf16_f32 v56, v48, v49\n"
    "v_cvt_pk_bf16_f32 v57, v50, v51\n"
    "v_exp_f32 v29, v29\n"
    "v_cvt_pk_bf16_f32 v58, v52, v53\n"
    "v_cvt_pk_bf16_f32 v59, v54, v55\n"
    "v_pk_mul_f32 v[152:153], v[152:153], v[136:137]\n"
    "v_exp_f32 v30, v30\n"
    "v_accvgpr_write_b32 a176, v56\n"
    "v_accvgpr_write_b32 a177, v57\n"
    "v_exp_f32 v31, v31\n"
    "v_accvgpr_write_b32 a178, v58\n"
    "v_accvgpr_write_b32 a179, v59\n"
    "v_pk_mul_f32 v[154:155], v[154:155], v[138:139]\n"
    "v_pk_add_f32 v[24:25], v[24:25], 1.0 op_sel_hi:[1,0]\n"
    "v_pk_add_f32 v[26:27], v[26:27], 1.0 op_sel_hi:[1,0]\n"
    "v_pk_add_f32 v[28:29], v[28:29], 1.0 op_sel_hi:[1,0]\n"
    "v_pk_add_f32 v[30:31], v[30:31], 1.0 op_sel_hi:[1,0]\n"
    "buffer_load_dwordx4 v9, s[64:67], s80 offen offset:128 sc1 lds\n"
    "s_add_u32 m0, s81, 114688\n"
    "v_rcp_f32 v24, v24\n"
    "v_accvgpr_read_b32 v64, a32\n"
    "v_accvgpr_read_b32 v65, a33\n"
    "v_rcp_f32 v25, v25\n"
    "v_accvgpr_read_b32 v66, a34\n"
    "v_accvgpr_read_b32 v67, a35\n"
    "v_rcp_f32 v26, v26\n"
    "v_accvgpr_read_b32 v68, a48\n"
    "v_accvgpr_read_b32 v69, a49\n"
    "v_rcp_f32 v27, v27\n"
    "v_accvgpr_read_b32 v70, a50\n"
    "v_accvgpr_read_b32 v71, a51\n"
    "v_rcp_f32 v28, v28\n"
    "v_pk_mul_f32 v[64:65], v[64:65], s[92:93]\n"
    "v_pk_mul_f32 v[66:67], v[66:67], s[92:93]\n"
    "v_rcp_f32 v29, v29\n"
    "v_pk_mul_f32 v[68:69], v[68:69], s[92:93]\n"
    "v_pk_mul_f32 v[70:71], v[70:71], s[92:93]\n"
    "v_rcp_f32 v30, v30\n"
    "v_cvt_pk_bf16_f32 v72, v64, v65\n"
    "v_cvt_pk_bf16_f32 v73, v66, v67\n"
    "v_rcp_f32 v31, v31\n"
    "v_cvt_pk_bf16_f32 v74, v68, v69\n"
    "v_cvt_pk_bf16_f32 v75, v70, v71\n"
    "v_pk_mul_f32 v[120:121], v[120:121], v[24:25]\n"
    "v_pk_mul_f32 v[122:123], v[122:123], v[26:27]\n"
    "buffer_load_dwordx4 v7, s[64:67], s80 offen offset:2048 sc1 lds\n"
    "s_add_u32 m0, s81, 115584\n"
    "v_pk_mul_f32 v[152:153], v[152:153], v[28:29]\n"
    "v_pk_mul_f32 v[154:155], v[154:155], v[30:31]\n"
    "v_cvt_pk_bf16_f32 v104, v120, v121\n"
    "v_cvt_pk_bf16_f32 v105, v122, v123\n"
    "v_cvt_pk_bf16_f32 v106, v152, v153\n"
    "v_cvt_pk_bf16_f32 v107, v154, v155\n"
    "buffer_store_dwordx4 v[104:107], v2, s[56:59], s77 offen offset:2048 sc1\n"
    "v_pk_mul_f32 v[24:25], v[108:109], v[108:109]\n"
    "v_pk_mul_f32 v[26:27], v[110:111], v[110:111]\n"
    "v_pk_mul_f32 v[28:29], v[140:141], v[140:141]\n"
    "buffer_load_dwordx4 v9, s[64:67], s80 offen offset:2176 sc1 lds\n"
    "s_add_u32 s80, s80, 16384\n"
    "v_pk_mul_f32 v[30:31], v[142:143], v[142:143]\n"
    "v_pk_fma_f32 v[24:25], v[24:25], s[90:91], v[12:13]\n"
    "v_pk_fma_f32 v[26:27], v[26:27], s[90:91], v[12:13]\n"
    "v_pk_fma_f32 v[28:29], v[28:29], s[90:91], v[12:13]\n"
    "v_pk_fma_f32 v[30:31], v[30:31], s[90:91], v[12:13]\n"
    "v_pk_mul_f32 v[24:25], v[24:25], v[108:109]\n"
    "v_pk_mul_f32 v[26:27], v[26:27], v[110:111]\n"
    "v_pk_mul_f32 v[28:29], v[28:29], v[140:141]\n"
    "v_pk_mul_f32 v[30:31], v[30:31], v[142:143]\n"
    "v_exp_f32 v24, v24\n"
    "v_accvgpr_write_b32 a132, v72\n"
    "v_accvgpr_write_b32 a133, v73\n"
    "v_exp_f32 v25, v25\n"
    "v_accvgpr_write_b32 a134, v74\n"
    "v_accvgpr_write_b32 a135, v75\n"
    "v_pk_mul_f32 v[124:125], v[124:125], v[108:109]\n"
    "v_exp_f32 v26, v26\n"
    "v_accvgpr_read_b32 v64, a36\n"
    "v_accvgpr_read_b32 v65, a37\n"
    "v_exp_f32 v27, v27\n"
    "v_accvgpr_read_b32 v66, a38\n"
    "v_accvgpr_read_b32 v67, a39\n"
    "v_pk_mul_f32 v[126:127], v[126:127], v[110:111]\n"
    "v_exp_f32 v28, v28\n"
    "v_accvgpr_read_b32 v68, a52\n"
    "v_accvgpr_read_b32 v69, a53\n"
    "v_exp_f32 v29, v29\n"
    "v_accvgpr_read_b32 v70, a54\n"
    "v_accvgpr_read_b32 v71, a55\n"
    "v_pk_mul_f32 v[156:157], v[156:157], v[140:141]\n"
    "v_exp_f32 v30, v30\n"
    "v_pk_mul_f32 v[64:65], v[64:65], s[92:93]\n"
    "v_pk_mul_f32 v[66:67], v[66:67], s[92:93]\n"
    "v_exp_f32 v31, v31\n"
    "v_pk_mul_f32 v[68:69], v[68:69], s[92:93]\n"
    "v_pk_mul_f32 v[70:71], v[70:71], s[92:93]\n"
    "v_pk_mul_f32 v[158:159], v[158:159], v[142:143]\n"
    "v_pk_add_f32 v[24:25], v[24:25], 1.0 op_sel_hi:[1,0]\n"
    "v_pk_add_f32 v[26:27], v[26:27], 1.0 op_sel_hi:[1,0]\n"
    "v_pk_add_f32 v[28:29], v[28:29], 1.0 op_sel_hi:[1,0]\n"
    "v_pk_add_f32 v[30:31], v[30:31], 1.0 op_sel_hi:[1,0]\n"
    "v_rcp_f32 v24, v24\n"
    "v_cvt_pk_bf16_f32 v72, v64, v65\n"
    "v_cvt_pk_bf16_f32 v73, v66, v67\n"
    "v_rcp_f32 v25, v25\n"
    "v_cvt_pk_bf16_f32 v74, v68, v69\n"
    "v_cvt_pk_bf16_f32 v75, v70, v71\n"
    "v_rcp_f32 v26, v26\n"
    "v_accvgpr_write_b32 a148, v72\n"
    "v_accvgpr_write_b32 a149, v73\n"
    "v_rcp_f32 v27, v27\n"
    "v_accvgpr_write_b32 a150, v74\n"
    "v_accvgpr_write_b32 a151, v75\n"
    "v_rcp_f32 v28, v28\n"
    "v_accvgpr_read_b32 v64, a40\n"
    "v_accvgpr_read_b32 v65, a41\n"
    "v_rcp_f32 v29, v29\n"
    "v_accvgpr_read_b32 v66, a42\n"
    "v_accvgpr_read_b32 v67, a43\n"
    "v_rcp_f32 v30, v30\n"
    "v_accvgpr_read_b32 v68, a56\n"
    "v_accvgpr_read_b32 v69, a57\n"
    "v_rcp_f32 v31, v31\n"
    "v_accvgpr_read_b32 v70, a58\n"
    "v_accvgpr_read_b32 v71, a59\n"
    "v_pk_mul_f32 v[124:125], v[124:125], v[24:25]\n"
    "v_pk_mul_f32 v[126:127], v[126:127], v[26:27]\n"
    "v_pk_mul_f32 v[156:157], v[156:157], v[28:29]\n"
    "v_pk_mul_f32 v[158:159], v[158:159], v[30:31]\n"
    "v_cvt_pk_bf16_f32 v108, v124, v125\n"
    "v_cvt_pk_bf16_f32 v109, v126, v127\n"
    "v_cvt_pk_bf16_f32 v110, v156, v157\n"
    "v_cvt_pk_bf16_f32 v111, v158, v159\n"
    "buffer_store_dwordx4 v[108:111], v2, s[56:59], s77 offen offset:3072 sc1\n"
    "v_pk_mul_f32 v[24:25], v[160:161], v[160:161]\n"
    "v_pk_mul_f32 v[26:27], v[162:163], v[162:163]\n"
    "v_pk_mul_f32 v[28:29], v[192:193], v[192:193]\n"
    "v_pk_mul_f32 v[30:31], v[194:195], v[194:195]\n"
    "v_pk_fma_f32 v[24:25], v[24:25], s[90:91], v[12:13]\n"
    "v_pk_fma_f32 v[26:27], v[26:27], s[90:91], v[12:13]\n"
    "v_pk_fma_f32 v[28:29], v[28:29], s[90:91], v[12:13]\n"
    "v_pk_fma_f32 v[30:31], v[30:31], s[90:91], v[12:13]\n"
    "v_pk_mul_f32 v[24:25], v[24:25], v[160:161]\n"
    "v_pk_mul_f32 v[26:27], v[26:27], v[162:163]\n"
    "v_pk_mul_f32 v[28:29], v[28:29], v[192:193]\n"
    "v_pk_mul_f32 v[30:31], v[30:31], v[194:195]\n"
    "v_exp_f32 v24, v24\n"
    "v_pk_mul_f32 v[64:65], v[64:65], s[92:93]\n"
    "v_pk_mul_f32 v[66:67], v[66:67], s[92:93]\n"
    "v_exp_f32 v25, v25\n"
    "v_pk_mul_f32 v[68:69], v[68:69], s[92:93]\n"
    "v_pk_mul_f32 v[70:71], v[70:71], s[92:93]\n"
    "v_pk_mul_f32 v[176:177], v[176:177], v[160:161]\n"
    "v_exp_f32 v26, v26\n"
    "v_cvt_pk_bf16_f32 v72, v64, v65\n"
    "v_cvt_pk_bf16_f32 v73, v66, v67\n"
    "v_exp_f32 v27, v27\n"
    "v_cvt_pk_bf16_f32 v74, v68, v69\n"
    "v_cvt_pk_bf16_f32 v75, v70, v71\n"
    "v_pk_mul_f32 v[178:179], v[178:179], v[162:163]\n"
    "v_exp_f32 v28, v28\n"
    "v_accvgpr_write_b32 a164, v72\n"
    "v_accvgpr_write_b32 a165, v73\n"
    "v_exp_f32 v29, v29\n"
    "v_accvgpr_write_b32 a166, v74\n"
    "v_accvgpr_write_b32 a167, v75\n"
    "v_pk_mul_f32 v[208:209], v[208:209], v[192:193]\n"
    "v_exp_f32 v30, v30\n"
    "v_accvgpr_read_b32 v64, a44\n"
    "v_accvgpr_read_b32 v65, a45\n"
    "v_exp_f32 v31, v31\n"
    "v_accvgpr_read_b32 v66, a46\n"
    "v_accvgpr_read_b32 v67, a47\n"
    "v_pk_mul_f32 v[210:211], v[210:211], v[194:195]\n"
    "v_pk_add_f32 v[24:25], v[24:25], 1.0 op_sel_hi:[1,0]\n"
    "v_pk_add_f32 v[26:27], v[26:27], 1.0 op_sel_hi:[1,0]\n"
    "v_pk_add_f32 v[28:29], v[28:29], 1.0 op_sel_hi:[1,0]\n"
    "v_pk_add_f32 v[30:31], v[30:31], 1.0 op_sel_hi:[1,0]\n"
    "v_rcp_f32 v24, v24\n"
    "v_accvgpr_read_b32 v68, a60\n"
    "v_accvgpr_read_b32 v69, a61\n"
    "v_rcp_f32 v25, v25\n"
    "v_accvgpr_read_b32 v70, a62\n"
    "v_accvgpr_read_b32 v71, a63\n"
    "v_rcp_f32 v26, v26\n"
    "v_pk_mul_f32 v[64:65], v[64:65], s[92:93]\n"
    "v_pk_mul_f32 v[66:67], v[66:67], s[92:93]\n"
    "v_rcp_f32 v27, v27\n"
    "v_pk_mul_f32 v[68:69], v[68:69], s[92:93]\n"
    "v_pk_mul_f32 v[70:71], v[70:71], s[92:93]\n"
    "v_rcp_f32 v28, v28\n"
    "v_cvt_pk_bf16_f32 v72, v64, v65\n"
    "v_cvt_pk_bf16_f32 v73, v66, v67\n"
    "v_rcp_f32 v29, v29\n"
    "v_cvt_pk_bf16_f32 v74, v68, v69\n"
    "v_cvt_pk_bf16_f32 v75, v70, v71\n"
    "v_rcp_f32 v30, v30\n"
    "v_accvgpr_write_b32 a180, v72\n"
    "v_accvgpr_write_b32 a181, v73\n"
    "v_rcp_f32 v31, v31\n"
    "v_accvgpr_write_b32 a182, v74\n"
    "v_accvgpr_write_b32 a183, v75\n"
    "v_pk_mul_f32 v[176:177], v[176:177], v[24:25]\n"
    "v_pk_mul_f32 v[178:179], v[178:179], v[26:27]\n"
    "v_pk_mul_f32 v[208:209], v[208:209], v[28:29]\n"
    "v_pk_mul_f32 v[210:211], v[210:211], v[30:31]\n"
    "v_cvt_pk_bf16_f32 v160, v176, v177\n"
    "v_cvt_pk_bf16_f32 v161, v178, v179\n"
    "v_cvt_pk_bf16_f32 v162, v208, v209\n"
    "v_cvt_pk_bf16_f32 v163, v210, v211\n"
    "buffer_store_dwordx4 v[160:163], v2, s[56:59], s88 offen offset:0 sc1\n"
    "v_pk_mul_f32 v[24:25], v[164:165], v[164:165]\n"
    "v_pk_mul_f32 v[26:27], v[166:167], v[166:167]\n"
    "v_pk_mul_f32 v[28:29], v[196:197], v[196:197]\n"
    "v_pk_mul_f32 v[30:31], v[198:199], v[198:199]\n"
    "v_pk_fma_f32 v[24:25], v[24:25], s[90:91], v[12:13]\n"
    "v_pk_fma_f32 v[26:27], v[26:27], s[90:91], v[12:13]\n"
    "v_pk_fma_f32 v[28:29], v[28:29], s[90:91], v[12:13]\n"
    "v_pk_fma_f32 v[30:31], v[30:31], s[90:91], v[12:13]\n"
    "v_pk_mul_f32 v[24:25], v[24:25], v[164:165]\n"
    "v_pk_mul_f32 v[26:27], v[26:27], v[166:167]\n"
    "v_pk_mul_f32 v[28:29], v[28:29], v[196:197]\n"
    "v_pk_mul_f32 v[30:31], v[30:31], v[198:199]\n"
    "v_exp_f32 v24, v24\n"
    "s_waitcnt vmcnt(32)\n"
    "s_barrier\n"
    "ds_read_b128 a[192:195], v16 offset:0\n"
    "v_exp_f32 v25, v25\n"
    "ds_read_b128 a[196:199], v17 offset:0\n"
    "ds_read_b128 a[200:203], v18 offset:0\n"
    "v_pk_mul_f32 v[180:181], v[180:181], v[164:165]\n"
    "v_exp_f32 v26, v26\n"
    "ds_read_b128 a[204:207], v19 offset:0\n"
    "ds_read_b128 a[208:211], v16 offset:4096\n"
    "v_exp_f32 v27, v27\n"
    "ds_read_b128 a[212:215], v17 offset:4096\n"
    "ds_read_b128 a[216:219], v18 offset:4096\n"
    "v_pk_mul_f32 v[182:183], v[182:183], v[166:167]\n"
    "v_exp_f32 v28, v28\n"
    "ds_read_b128 a[220:223], v19 offset:4096\n"
    "ds_read_b128 a[224:227], v16 offset:8192\n"
    "v_exp_f32 v29, v29\n"
    "ds_read_b128 a[228:231], v17 offset:8192\n"
    "ds_read_b128 a[232:235], v18 offset:8192\n"
    "v_pk_mul_f32 v[212:213], v[212:213], v[196:197]\n"
    "v_exp_f32 v30, v30\n"
    "ds_read_b128 a[236:239], v19 offset:8192\n"
    "ds_read_b128 a[240:243], v16 offset:12288\n"
    "v_exp_f32 v31, v31\n"
    "ds_read_b128 a[244:247], v17 offset:12288\n"
    "ds_read_b128 a[248:251], v18 offset:12288\n"
    "v_pk_mul_f32 v[214:215], v[214:215], v[198:199]\n"
    "v_pk_add_f32 v[24:25], v[24:25], 1.0 op_sel_hi:[1,0]\n"
    "v_pk_add_f32 v[26:27], v[26:27], 1.0 op_sel_hi:[1,0]\n"
    "v_pk_add_f32 v[28:29], v[28:29], 1.0 op_sel_hi:[1,0]\n"
    "v_pk_add_f32 v[30:31], v[30:31], 1.0 op_sel_hi:[1,0]\n"
    "v_rcp_f32 v24, v24\n"
    "ds_read_b128 a[252:255], v19 offset:12288\n"
    "v_accvgpr_read_b32 v48, a64\n"
    "v_rcp_f32 v25, v25\n"
    "v_accvgpr_read_b32 v49, a65\n"
    "v_accvgpr_read_b32 v50, a66\n"
    "v_rcp_f32 v26, v26\n"
    "v_accvgpr_read_b32 v51, a67\n"
    "v_accvgpr_read_b32 v52, a80\n"
    "v_rcp_f32 v27, v27\n"
    "v_accvgpr_read_b32 v53, a81\n"
    "v_accvgpr_read_b32 v54, a82\n"
    "v_rcp_f32 v28, v28\n"
    "v_accvgpr_read_b32 v55, a83\n"
    "v_pk_mul_f32 v[48:49], v[48:49], s[92:93]\n"
    "v_rcp_f32 v29, v29\n"
    "v_pk_mul_f32 v[50:51], v[50:51], s[92:93]\n"
    "v_pk_mul_f32 v[52:53], v[52:53], s[92:93]\n"
    "v_rcp_f32 v30, v30\n"
    "v_pk_mul_f32 v[54:55], v[54:55], s[92:93]\n"
    "v_cvt_pk_bf16_f32 v56, v48, v49\n"
    "v_rcp_f32 v31, v31\n"
    "v_cvt_pk_bf16_f32 v57, v50, v51\n"
    "v_cvt_pk_bf16_f32 v58, v52, v53\n"
    "v_pk_mul_f32 v[180:181], v[180:181], v[24:25]\n"
    "v_pk_mul_f32 v[182:183], v[182:183], v[26:27]\n"
    "v_pk_mul_f32 v[212:213], v[212:213], v[28:29]\n"
    "v_pk_mul_f32 v[214:215], v[214:215], v[30:31]\n"
    "v_cvt_pk_bf16_f32 v164, v180, v181\n"
    "v_cvt_pk_bf16_f32 v165, v182, v183\n"
    "v_cvt_pk_bf16_f32 v166, v212, v213\n"
    "v_cvt_pk_bf16_f32 v167, v214, v215\n"
    "buffer_store_dwordx4 v[164:167], v2, s[56:59], s88 offen offset:1024 sc1\n"
    "v_pk_mul_f32 v[24:25], v[168:169], v[168:169]\n"
    "v_pk_mul_f32 v[26:27], v[170:171], v[170:171]\n"
    "v_pk_mul_f32 v[28:29], v[200:201], v[200:201]\n"
    "v_pk_mul_f32 v[30:31], v[202:203], v[202:203]\n"
    "v_pk_fma_f32 v[24:25], v[24:25], s[90:91], v[12:13]\n"
    "v_pk_fma_f32 v[26:27], v[26:27], s[90:91], v[12:13]\n"
    "v_pk_fma_f32 v[28:29], v[28:29], s[90:91], v[12:13]\n"
    "v_pk_fma_f32 v[30:31], v[30:31], s[90:91], v[12:13]\n"
    "v_pk_mul_f32 v[24:25], v[24:25], v[168:169]\n"
    "v_pk_mul_f32 v[26:27], v[26:27], v[170:171]\n"
    "v_pk_mul_f32 v[28:29], v[28:29], v[200:201]\n"
    "v_pk_mul_f32 v[30:31], v[30:31], v[202:203]\n"
    "v_exp_f32 v24, v24\n"
    "v_cvt_pk_bf16_f32 v59, v54, v55\n"
    "v_accvgpr_write_b32 a136, v56\n"
    "v_exp_f32 v25, v25\n"
    "v_accvgpr_write_b32 a137, v57\n"
    "v_accvgpr_write_b32 a138, v58\n"
    "v_pk_mul_f32 v[184:185], v[184:185], v[168:169]\n"
    "v_exp_f32 v26, v26\n"
    "v_accvgpr_write_b32 a139, v59\n"
    "ds_read_b64_tr_b16 a[0:1], v20 offset:0\n"
    "v_exp_f32 v27, v27\n"
    "ds_read_b64_tr_b16 a[2:3], v20 offset:4096\n"
    "ds_read_b64_tr_b16 a[4:5], v20 offset:8192\n"
    "v_pk_mul_f32 v[186:187], v[186:187], v[170:171]\n"
    "v_exp_f32 v28, v28\n"
    "ds_read_b64_tr_b16 a[6:7], v20 offset:12288\n"
    "v_accvgpr_read_b32 v48, a68\n"
    "v_exp_f32 v29, v29\n"
    "v_accvgpr_read_b32 v49, a69\n"
    "v_accvgpr_read_b32 v50, a70\n"
    "v_pk_mul_f32 v[216:217], v[216:217], v[200:201]\n"
    "v_exp_f32 v30, v30\n"
    "v_accvgpr_read_b32 v51, a71\n"
    "v_accvgpr_read_b32 v52, a84\n"
    "v_exp_f32 v31, v31\n"
    "v_accvgpr_read_b32 v53, a85\n"
    "v_accvgpr_read_b32 v54, a86\n"
    "v_pk_mul_f32 v[218:219], v[218:219], v[202:203]\n"
    "v_pk_add_f32 v[24:25], v[24:25], 1.0 op_sel_hi:[1,0]\n"
    "v_pk_add_f32 v[26:27], v[26:27], 1.0 op_sel_hi:[1,0]\n"
    "v_pk_add_f32 v[28:29], v[28:29], 1.0 op_sel_hi:[1,0]\n"
    "v_pk_add_f32 v[30:31], v[30:31], 1.0 op_sel_hi:[1,0]\n"
    "v_rcp_f32 v24, v24\n"
    "v_accvgpr_read_b32 v55, a87\n"
    "v_pk_mul_f32 v[48:49], v[48:49], s[92:93]\n"
    "v_rcp_f32 v25, v25\n"
    "v_pk_mul_f32 v[50:51], v[50:51], s[92:93]\n"
    "v_pk_mul_f32 v[52:53], v[52:53], s[92:93]\n"
    "v_rcp_f32 v26, v26\n"
    "v_pk_mul_f32 v[54:55], v[54:55], s[92:93]\n"
    "v_cvt_pk_bf16_f32 v56, v48, v49\n"
    "v_rcp_f32 v27, v27\n"
    "v_cvt_pk_bf16_f32 v57, v50, v51\n"
    "v_cvt_pk_bf16_f32 v58, v52, v53\n"
    "v_rcp_f32 v28, v28\n"
    "v_cvt_pk_bf16_f32 v59, v54, v55\n"
    "v_accvgpr_write_b32 a152, v56\n"
    "v_rcp_f32 v29, v29\n"
    "v_accvgpr_write_b32 a153, v57\n"
    "v_accvgpr_write_b32 a154, v58\n"
    "v_rcp_f32 v30, v30\n"
    "v_accvgpr_write_b32 a155, v59\n"
    "ds_read_b64_tr_b16 a[8:9], v21 offset:0\n"
    "v_rcp_f32 v31, v31\n"
    "ds_read_b64_tr_b16 a[10:11], v21 offset:4096\n"
    "ds_read_b64_tr_b16 a[12:13], v21 offset:8192\n"
    "v_pk_mul_f32 v[184:185], v[184:185], v[24:25]\n"
    "v_pk_mul_f32 v[186:187], v[186:187], v[26:27]\n"
    "v_pk_mul_f32 v[216:217], v[216:217], v[28:29]\n"
    "v_pk_mul_f32 v[218:219], v[218:219], v[30:31]\n"
    "v_cvt_pk_bf16_f32 v168, v184, v185\n"
    "v_cvt_pk_bf16_f32 v169, v186, v187\n"
    "v_cvt_pk_bf16_f32 v170, v216, v217\n"
    "v_cvt_pk_bf16_f32 v171, v218, v219\n"
    "buffer_store_dwordx4 v[168:171], v2, s[56:59], s89 offen offset:0 sc1\n"
    "v_pk_mul_f32 v[24:25], v[172:173], v[172:173]\n"
    "v_pk_mul_f32 v[26:27], v[174:175], v[174:175]\n"
    "v_pk_mul_f32 v[28:29], v[204:205], v[204:205]\n"
    "v_pk_mul_f32 v[30:31], v[206:207], v[206:207]\n"
    "v_pk_fma_f32 v[24:25], v[24:25], s[90:91], v[12:13]\n"
    "v_pk_fma_f32 v[26:27], v[26:27], s[90:91], v[12:13]\n"
    "v_pk_fma_f32 v[28:29], v[28:29], s[90:91], v[12:13]\n"
    "v_pk_fma_f32 v[30:31], v[30:31], s[90:91], v[12:13]\n"
    "v_pk_mul_f32 v[24:25], v[24:25], v[172:173]\n"
    "v_pk_mul_f32 v[26:27], v[26:27], v[174:175]\n"
    "v_pk_mul_f32 v[28:29], v[28:29], v[204:205]\n"
    "v_pk_mul_f32 v[30:31], v[30:31], v[206:207]\n"
    "v_exp_f32 v24, v24\n"
    "ds_read_b64_tr_b16 a[14:15], v21 offset:12288\n"
    "v_accvgpr_read_b32 v48, a72\n"
    "v_exp_f32 v25, v25\n"
    "v_accvgpr_read_b32 v49, a73\n"
    "v_accvgpr_read_b32 v50, a74\n"
    "v_pk_mul_f32 v[188:189], v[188:189], v[172:173]\n"
    "v_exp_f32 v26, v26\n"
    "v_accvgpr_read_b32 v51, a75\n"
    "v_accvgpr_read_b32 v52, a88\n"
    "v_exp_f32 v27, v27\n"
    "v_accvgpr_read_b32 v53, a89\n"
    "v_accvgpr_read_b32 v54, a90\n"
    "v_pk_mul_f32 v[190:191], v[190:191], v[174:175]\n"
    "v_exp_f32 v28, v28\n"
    "v_accvgpr_read_b32 v55, a91\n"
    "v_pk_mul_f32 v[48:49], v[48:49], s[92:93]\n"
    "v_exp_f32 v29, v29\n"
    "v_pk_mul_f32 v[50:51], v[50:51], s[92:93]\n"
    "v_pk_mul_f32 v[52:53], v[52:53], s[92:93]\n"
    "v_pk_mul_f32 v[220:221], v[220:221], v[204:205]\n"
    "v_exp_f32 v30, v30\n"
    "v_pk_mul_f32 v[54:55], v[54:55], s[92:93]\n"
    "v_cvt_pk_bf16_f32 v56, v48, v49\n"
    "v_exp_f32 v31, v31\n"
    "v_cvt_pk_bf16_f32 v57, v50, v51\n"
    "v_cvt_pk_bf16_f32 v58, v52, v53\n"
    "v_pk_mul_f32 v[222:223], v[222:223], v[206:207]\n"
    "v_pk_add_f32 v[24:25], v[24:25], 1.0 op_sel_hi:[1,0]\n"
    "v_pk_add_f32 v[26:27], v[26:27], 1.0 op_sel_hi:[1,0]\n"
    "v_pk_add_f32 v[28:29], v[28:29], 1.0 op_sel_hi:[1,0]\n"
    "v_pk_add_f32 v[30:31], v[30:31], 1.0 op_sel_hi:[1,0]\n"
    "v_rcp_f32 v24, v24\n"
    "v_cvt_pk_bf16_f32 v59, v54, v55\n"
    "v_accvgpr_write_b32 a168, v56\n"
    "v_rcp_f32 v25, v25\n"
    "v_accvgpr_write_b32 a169, v57\n"
    "v_accvgpr_write_b32 a170, v58\n"
    "v_rcp_f32 v26, v26\n"
    "v_accvgpr_write_b32 a171, v59\n"
    "ds_read_b64_tr_b16 a[16:17], v20 offset:512\n"
    "v_rcp_f32 v27, v27\n"
    "ds_read_b64_tr_b16 a[18:19], v20 offset:4608\n"
    "ds_read_b64_tr_b16 a[20:21], v20 offset:8704\n"
    "v_rcp_f32 v28, v28\n"
    "ds_read_b64_tr_b16 a[22:23], v20 offset:12800\n"
    "v_accvgpr_read_b32 v48, a76\n"
    "v_rcp_f32 v29, v29\n"
    "v_accvgpr_read_b32 v49, a77\n"
    "v_accvgpr_read_b32 v50, a78\n"
    "v_rcp_f32 v30, v30\n"
    "v_accvgpr_read_b32 v51, a79\n"
    "v_accvgpr_read_b32 v52, a92\n"
    "v_rcp_f32 v31, v31\n"
    "v_accvgpr_read_b32 v53, a93\n"
    "v_accvgpr_read_b32 v54, a94\n"
    "v_pk_mul_f32 v[188:189], v[188:189], v[24:25]\n"
    "v_pk_mul_f32 v[190:191], v[190:191], v[26:27]\n"
    "v_pk_mul_f32 v[220:221], v[220:221], v[28:29]\n"
    "v_pk_mul_f32 v[222:223], v[222:223], v[30:31]\n"
    "v_cvt_pk_bf16_f32 v172, v188, v189\n"
    "v_cvt_pk_bf16_f32 v173, v190, v191\n"
    "v_cvt_pk_bf16_f32 v174, v220, v221\n"
    "v_cvt_pk_bf16_f32 v175, v222, v223\n"
    "buffer_store_dwordx4 v[172:175], v2, s[56:59], s89 offen offset:1024 sc1\n"
    "v_pk_mul_f32 v[24:25], v[224:225], v[224:225]\n"
    "v_pk_mul_f32 v[26:27], v[226:227], v[226:227]\n"
    "v_pk_mul_f32 v[28:29], v[228:229], v[228:229]\n"
    "v_pk_mul_f32 v[30:31], v[230:231], v[230:231]\n"
    "v_pk_fma_f32 v[24:25], v[24:25], s[90:91], v[12:13]\n"
    "v_pk_fma_f32 v[26:27], v[26:27], s[90:91], v[12:13]\n"
    "v_pk_fma_f32 v[28:29], v[28:29], s[90:91], v[12:13]\n"
    "v_pk_fma_f32 v[30:31], v[30:31], s[90:91], v[12:13]\n"
    "v_pk_mul_f32 v[24:25], v[24:25], v[224:225]\n"
    "v_pk_mul_f32 v[26:27], v[26:27], v[226:227]\n"
    "v_pk_mul_f32 v[28:29], v[28:29], v[228:229]\n"
    "v_pk_mul_f32 v[30:31], v[30:31], v[230:231]\n"
    "v_exp_f32 v24, v24\n"
    "v_accvgpr_read_b32 v55, a95\n"
    "v_pk_mul_f32 v[48:49], v[48:49], s[92:93]\n"
    "v_exp_f32 v25, v25\n"
    "v_pk_mul_f32 v[50:51], v[50:51], s[92:93]\n"
    "v_pk_mul_f32 v[52:53], v[52:53], s[92:93]\n"
    "v_pk_mul_f32 v[240:241], v[240:241], v[224:225]\n"
    "v_exp_f32 v26, v26\n"
    "v_pk_mul_f32 v[54:55], v[54:55], s[92:93]\n"
    "v_cvt_pk_bf16_f32 v56, v48, v49\n"
    "v_exp_f32 v27, v27\n"
    "v_cvt_pk_bf16_f32 v57, v50, v51\n"
    "v_cvt_pk_bf16_f32 v58, v52, v53\n"
    "v_pk_mul_f32 v[242:243], v[242:243], v[226:227]\n"
    "v_exp_f32 v28, v28\n"
    "v_cvt_pk_bf16_f32 v59, v54, v55\n"
    "v_accvgpr_write_b32 a184, v56\n"
    "v_exp_f32 v29, v29\n"
    "v_accvgpr_write_b32 a185, v57\n"
    "v_accvgpr_write_b32 a186, v58\n"
    "v_pk_mul_f32 v[244:245], v[244:245], v[228:229]\n"
    "v_exp_f32 v30, v30\n"
    "v_accvgpr_write_b32 a187, v59\n"
    "ds_read_b64_tr_b16 a[24:25], v21 offset:512\n"
    "v_exp_f32 v31, v31\n"
    "ds_read_b64_tr_b16 a[26:27], v21 offset:4608\n"
    "ds_read_b64_tr_b16 a[28:29], v21 offset:8704\n"
    "v_pk_mul_f32 v[246:247], v[246:247], v[230:231]\n"
    "v_pk_add_f32 v[24:25], v[24:25], 1.0 op_sel_hi:[1,0]\n"
    "v_pk_add_f32 v[26:27], v[26:27], 1.0 op_sel_hi:[1,0]\n"
    "v_pk_add_f32 v[28:29], v[28:29], 1.0 op_sel_hi:[1,0]\n"
    "v_pk_add_f32 v[30:31], v[30:31], 1.0 op_sel_hi:[1,0]\n"
    "v_rcp_f32 v24, v24\n"
    "ds_read_b64_tr_b16 a[30:31], v21 offset:12800\n"
    "v_accvgpr_read_b32 v64, a96\n"
    "v_rcp_f32 v25, v25\n"
    "v_accvgpr_read_b32 v65, a97\n"
    "v_accvgpr_read_b32 v66, a98\n"
    "v_rcp_f32 v26, v26\n"
    "v_accvgpr_read_b32 v67, a99\n"
    "v_accvgpr_read_b32 v68, a112\n"
    "v_rcp_f32 v27, v27\n"
    "v_accvgpr_read_b32 v69, a113\n"
    "v_accvgpr_read_b32 v70, a114\n"
    "v_rcp_f32 v28, v28\n"
    "v_accvgpr_read_b32 v71, a115\n"
    "v_pk_mul_f32 v[64:65], v[64:65], s[92:93]\n"
    "v_rcp_f32 v29, v29\n"
    "v_pk_mul_f32 v[66:67], v[66:67], s[92:93]\n"
    "v_pk_mul_f32 v[68:69], v[68:69], s[92:93]\n"
    "v_rcp_f32 v30, v30\n"
    "v_pk_mul_f32 v[70:71], v[70:71], s[92:93]\n"
    "v_cvt_pk_bf16_f32 v72, v64, v65\n"
    "v_rcp_f32 v31, v31\n"
    "v_cvt_pk_bf16_f32 v73, v66, v67\n"
    "v_cvt_pk_bf16_f32 v74, v68, v69\n"
    "v_pk_mul_f32 v[240:241], v[240:241], v[24:25]\n"
    "v_pk_mul_f32 v[242:243], v[242:243], v[26:27]\n"
    "v_pk_mul_f32 v[244:245], v[244:245], v[28:29]\n"
    "v_pk_mul_f32 v[246:247], v[246:247], v[30:31]\n"
    "v_cvt_pk_bf16_f32 v224, v240, v241\n"
    "v_cvt_pk_bf16_f32 v225, v242, v243\n"
    "v_cvt_pk_bf16_f32 v228, v244, v245\n"
    "v_cvt_pk_bf16_f32 v229, v246, v247\n"
    "buffer_store_dwordx2 v[224:225], v2, s[56:59], s78 offen offset:0 sc1\n"
    "buffer_store_dwordx2 v[228:229], v2, s[56:59], s78 offen offset:1024 sc1\n"
    "v_pk_mul_f32 v[24:25], v[232:233], v[232:233]\n"
    "v_pk_mul_f32 v[26:27], v[234:235], v[234:235]\n"
    "v_pk_mul_f32 v[28:29], v[236:237], v[236:237]\n"
    "v_pk_mul_f32 v[30:31], v[238:239], v[238:239]\n"
    "v_pk_fma_f32 v[24:25], v[24:25], s[90:91], v[12:13]\n"
    "v_pk_fma_f32 v[26:27], v[26:27], s[90:91], v[12:13]\n"
    "v_pk_fma_f32 v[28:29], v[28:29], s[90:91], v[12:13]\n"
    "v_pk_fma_f32 v[30:31], v[30:31], s[90:91], v[12:13]\n"
    "v_pk_mul_f32 v[24:25], v[24:25], v[232:233]\n"
    "v_pk_mul_f32 v[26:27], v[26:27], v[234:235]\n"
    "v_pk_mul_f32 v[28:29], v[28:29], v[236:237]\n"
    "v_pk_mul_f32 v[30:31], v[30:31], v[238:239]\n"
    "v_exp_f32 v24, v24\n"
    "v_cvt_pk_bf16_f32 v75, v70, v71\n"
    "v_accvgpr_write_b32 a140, v72\n"
    "v_exp_f32 v25, v25\n"
    "v_accvgpr_write_b32 a141, v73\n"
    "v_accvgpr_write_b32 a142, v74\n"
    "v_pk_mul_f32 v[248:249], v[248:249], v[232:233]\n"
    "v_exp_f32 v26, v26\n"
    "v_accvgpr_write_b32 a143, v75\n"
    "ds_read_b64_tr_b16 a[32:33], v20 offset:1024\n"
    "v_exp_f32 v27, v27\n"
    "ds_read_b64_tr_b16 a[34:35], v20 offset:5120\n"
    "ds_read_b64_tr_b16 a[36:37], v20 offset:9216\n"
    "v_pk_mul_f32 v[250:251], v[250:251], v[234:235]\n"
    "v_exp_f32 v28, v28\n"
    "ds_read_b64_tr_b16 a[38:39], v20 offset:13312\n"
    "v_accvgpr_read_b32 v64, a100\n"
    "v_exp_f32 v29, v29\n"
    "v_accvgpr_read_b32 v65, a101\n"
    "v_accvgpr_read_b32 v66, a102\n"
    "v_pk_mul_f32 v[252:253], v[252:253], v[236:237]\n"
    "v_exp_f32 v30, v30\n"
    "v_accvgpr_read_b32 v67, a103\n"
    "v_accvgpr_read_b32 v68, a116\n"
    "v_exp_f32 v31, v31\n"
    "v_accvgpr_read_b32 v69, a117\n"
    "v_accvgpr_read_b32 v70, a118\n"
    "v_pk_mul_f32 v[254:255], v[254:255], v[238:239]\n"
    "v_pk_add_f32 v[24:25], v[24:25], 1.0 op_sel_hi:[1,0]\n"
    "v_pk_add_f32 v[26:27], v[26:27], 1.0 op_sel_hi:[1,0]\n"
    "v_pk_add_f32 v[28:29], v[28:29], 1.0 op_sel_hi:[1,0]\n"
    "v_pk_add_f32 v[30:31], v[30:31], 1.0 op_sel_hi:[1,0]\n"
    "v_rcp_f32 v24, v24\n"
    "v_accvgpr_read_b32 v71, a119\n"
    "v_pk_mul_f32 v[64:65], v[64:65], s[92:93]\n"
    "v_rcp_f32 v25, v25\n"
    "v_pk_mul_f32 v[66:67], v[66:67], s[92:93]\n"
    "v_pk_mul_f32 v[68:69], v[68:69], s[92:93]\n"
    "v_rcp_f32 v26, v26\n"
    "v_pk_mul_f32 v[70:71], v[70:71], s[92:93]\n"
    "v_cvt_pk_bf16_f32 v72, v64, v65\n"
    "v_rcp_f32 v27, v27\n"
    "v_cvt_pk_bf16_f32 v73, v66, v67\n"
    "v_cvt_pk_bf16_f32 v74, v68, v69\n"
    "v_rcp_f32 v28, v28\n"
    "v_cvt_pk_bf16_f32 v75, v70, v71\n"
    "v_accvgpr_write_b32 a156, v72\n"
    "v_rcp_f32 v29, v29\n"
    "v_accvgpr_write_b32 a157, v73\n"
    "v_accvgpr_write_b32 a158, v74\n"
    "v_rcp_f32 v30, v30\n"
    "v_accvgpr_write_b32 a159, v75\n"
    "ds_read_b64_tr_b16 a[40:41], v21 offset:1024\n"
    "v_rcp_f32 v31, v31\n"
    "ds_read_b64_tr_b16 a[42:43], v21 offset:5120\n"
    "ds_read_b64_tr_b16 a[44:45], v21 offset:9216\n"
    "v_pk_mul_f32 v[248:249], v[248:249], v[24:25]\n"
    "v_pk_mul_f32 v[250:251], v[250:251], v[26:27]\n"
    "v_pk_mul_f32 v[252:253], v[252:253], v[28:29]\n"
    "v_pk_mul_f32 v[254:255], v[254:255], v[30:31]\n"
    "v_cvt_pk_bf16_f32 v232, v248, v249\n"
    "v_cvt_pk_bf16_f32 v233, v250, v251\n"
    "v_cvt_pk_bf16_f32 v236, v252, v253\n"
    "v_cvt_pk_bf16_f32 v237, v254, v255\n"
    "buffer_store_dwordx2 v[232:233], v2, s[56:59], s79 offen offset:0 sc1\n"
    "buffer_store_dwordx2 v[236:237], v2, s[56:59], s79 offen offset:1024 sc1\n"
    "v_mov_b32 v12, 0\n"
    "v_mov_b32 v13, 0\n"
    "v_mov_b32 v14, 0\n"
    "v_mov_b32 v15, 0\n"
    "ds_read_b64_tr_b16 a[46:47], v21 offset:13312\n"
    "v_accvgpr_read_b32 v64, a104\n"
    "v_accvgpr_read_b32 v65, a105\n"
    "v_accvgpr_read_b32 v66, a106\n"
    "v_accvgpr_read_b32 v67, a107\n"
    "v_accvgpr_read_b32 v68, a120\n"
    "v_mfma_f32_32x32x16_bf16 v[128:143], v[12:15], v[12:15], 0\n"
    "v_accvgpr_read_b32 v69, a121\n"
    "v_accvgpr_read_b32 v70, a122\n"
    "v_accvgpr_read_b32 v71, a123\n"
    "v_pk_mul_f32 v[64:65], v[64:65], s[92:93]\n"
    "v_pk_mul_f32 v[66:67], v[66:67], s[92:93]\n"
    "v_pk_mul_f32 v[68:69], v[68:69], s[92:93]\n"
    "v_mfma_f32_32x32x16_bf16 v[144:159], v[12:15], v[12:15], 0\n"
    "v_pk_mul_f32 v[70:71], v[70:71], s[92:93]\n"
    "v_cvt_pk_bf16_f32 v72, v64, v65\n"
    "v_cvt_pk_bf16_f32 v73, v66, v67\n"
    "v_cvt_pk_bf16_f32 v74, v68, v69\n"
    "v_cvt_pk_bf16_f32 v75, v70, v71\n"
    "v_accvgpr_write_b32 a172, v72\n"
    "v_mfma_f32_32x32x16_bf16 v[160:175], v[12:15], v[12:15], 0\n"
    "v_accvgpr_write_b32 a173, v73\n"
    "v_accvgpr_write_b32 a174, v74\n"
    "v_accvgpr_write_b32 a175, v75\n"
    "ds_read_b64_tr_b16 a[48:49], v20 offset:1536\n"
    "ds_read_b64_tr_b16 a[50:51], v20 offset:5632\n"
    "ds_read_b64_tr_b16 a[52:53], v20 offset:9728\n"
    "v_mfma_f32_32x32x16_bf16 v[176:191], v[12:15], v[12:15], 0\n"
    "ds_read_b64_tr_b16 a[54:55], v20 offset:13824\n"
    "v_accvgpr_read_b32 v64, a108\n"
    "v_accvgpr_read_b32 v65, a109\n"
    "v_accvgpr_read_b32 v66, a110\n"
    "v_accvgpr_read_b32 v67, a111\n"
    "v_accvgpr_read_b32 v68, a124\n"
    "v_mfma_f32_32x32x16_bf16 v[192:207], v[12:15], v[12:15], 0\n"
    "v_accvgpr_read_b32 v69, a125\n"
    "v_accvgpr_read_b32 v70, a126\n"
    "v_accvgpr_read_b32 v71, a127\n"
    "v_pk_mul_f32 v[64:65], v[64:65], s[92:93]\n"
    "v_pk_mul_f32 v[66:67], v[66:67], s[92:93]\n"
    "v_pk_mul_f32 v[68:69], v[68:69], s[92:93]\n"
    "v_mfma_f32_32x32x16_bf16 v[208:223], v[12:15], v[12:15], 0\n"
    "v_pk_mul_f32 v[70:71], v[70:71], s[92:93]\n"
    "v_cvt_pk_bf16_f32 v72, v64, v65\n"
    "v_cvt_pk_bf16_f32 v73, v66, v67\n"
    "v_cvt_pk_bf16_f32 v74, v68, v69\n"
    "v_cvt_pk_bf16_f32 v75, v70, v71\n"
    "v_accvgpr_write_b32 a188, v72\n"
    "v_mfma_f32_32x32x16_bf16 v[224:239], v[12:15], v[12:15], 0\n"
    "v_accvgpr_write_b32 a189, v73\n"
    "v_accvgpr_write_b32 a190, v74\n"
    "v_accvgpr_write_b32 a191, v75\n"
    "ds_read_b64_tr_b16 a[56:57], v21 offset:1536\n"
    "ds_read_b64_tr_b16 a[58:59], v21 offset:5632\n"
    "ds_read_b64_tr_b16 a[60:61], v21 offset:9728\n"
    "v_mfma_f32_32x32x16_bf16 v[240:255], v[12:15], v[12:15], 0\n"
    "ds_read_b64_tr_b16 a[62:63], v21 offset:13824\n"
    "v_mfma_f32_32x32x16_bf16 v[64:79], v[12:15], v[12:15], 0\n"
    "v_mov_b32 v60, 0x3f803f80\n"
    "v_mov_b32 v61, 0x3f803f80\n"
    "v_mov_b32 v62, 0x3f803f80\n"
    "v_mov_b32 v63, 0x3f803f80\n"
    "s_waitcnt lgkmcnt(15)\n"
    "v_mfma_f32_16x16x32_bf16 v[96:99], a[192:195], a[128:131], 0\n"
    "v_mfma_f32_16x16x32_bf16 v[96:99], a[196:199], a[132:135], v[96:99]\n"
    "v_mfma_f32_16x16x32_bf16 v[96:99], a[200:203], a[136:139], v[96:99]\n"
    "v_mfma_f32_16x16x32_bf16 v[96:99], a[204:207], a[140:143], v[96:99]\n"
    "v_mfma_f32_16x16x32_bf16 v[100:103], a[192:195], a[144:147], 0\n"
    "v_mfma_f32_16x16x32_bf16 v[100:103], a[196:199], a[148:151], v[100:103]\n"
    "v_mfma_f32_16x16x32_bf16 v[100:103], a[200:203], a[152:155], v[100:103]\n"
    "v_mfma_f32_16x16x32_bf16 v[100:103], a[204:207], a[156:159], v[100:103]\n"
    "v_mfma_f32_16x16x32_bf16 v[104:107], a[192:195], a[160:163], 0\n"
    "v_mfma_f32_16x16x32_bf16 v[104:107], a[196:199], a[164:167], v[104:107]\n"
    "v_mfma_f32_16x16x32_bf16 v[104:107], a[200:203], a[168:171], v[104:107]\n"
    "v_mfma_f32_16x16x32_bf16 v[104:107], a[204:207], a[172:175], v[104:107]\n"
    "v_mfma_f32_16x16x32_bf16 v[108:111], a[192:195], a[176:179], 0\n"
    "v_mfma_f32_16x16x32_bf16 v[108:111], a[196:199], a[180:183], v[108:111]\n"
    "v_mfma_f32_16x16x32_bf16 v[108:111], a[200:203], a[184:187], v[108:111]\n"
    "v_mfma_f32_16x16x32_bf16 v[108:111], a[204:207], a[188:191], v[108:111]\n"
    "s_lshl_b32 s86, s38, 2\n"
    "s_add_u32 s86, s86, s40\n"
    "s_lshr_b32 s83, s86, 7\n"
    "s_and_b32 s86, s86, 127\n"
    "s_lshl_b32 s84, s86, 16\n"
    "s_lshl_b32 s85, s86, 13\n"
    "s_lshl_b32 s87, s83, 8\n"
    "s_mov_b64 s[44:45], %7\n"
    "s_add_u32 s44, s44, s87\n"
    "s_addc_u32 s45, s45, 0\n"
    "s_and_b32 s45, s45, 0xffff\n"
    "s_sub_u32 s46, 0x800000, s87\n"
    "s_mov_b32 s47, 0x20000\n"
    "s_lshl_b32 s87, s83, 20\n"
    "s_add_u32 s87, s87, 0x2800000\n"
    "s_add_u32 s48, s34, s87\n"
    "s_addc_u32 s49, s35, 0\n"
    "s_and_b32 s49, s49, 0xffff\n"
    "s_mov_b32 s50, 0\n"
    "s_mov_b32 s51, 0x20000\n"
    "s_mov_b32 s92, s84\n"
    "s_sub_u32 s93, s85, 2048\n"
    "v_max3_f32 v112, v96, v97, v98\n"
    "v_max_f32 v112, v112, v99\n"
    "v_mov_b32 v113, v112\n"
    "v_max3_f32 v116, v100, v101, v102\n"
    "v_max_f32 v116, v116, v103\n"
    "v_mov_b32 v117, v116\n"
    "v_max3_f32 v120, v104, v105, v106\n"
    "v_max_f32 v120, v120, v107\n"
    "v_mov_b32 v121, v120\n"
    "v_max3_f32 v124, v108, v109, v110\n"
    "v_max_f32 v124, v124, v111\n"
    "v_mov_b32 v125, v124\n"
    "v_permlane32_swap_b32 v112, v113\n"
    "v_permlane32_swap_b32 v116, v117\n"
    "v_permlane32_swap_b32 v120, v121\n"
    "v_permlane32_swap_b32 v124, v125\n"
    "v_max_f32 v112, v112, v113\n"
    "v_mov_b32 v113, v112\n"
    "v_max_f32 v116, v116, v117\n"
    "v_mov_b32 v117, v116\n"
    "v_max_f32 v120, v120, v121\n"
    "v_mov_b32 v121, v120\n"
    "v_max_f32 v124, v124, v125\n"
    "v_mov_b32 v125, v124\n"
    "v_permlane16_swap_b32 v112, v113\n"
    "v_permlane16_swap_b32 v116, v117\n"
    "v_permlane16_swap_b32 v120, v121\n"
    "v_permlane16_swap_b32 v124, v125\n"
    "v_max_f32 v112, v112, v113\n"
    "v_max_f32 v116, v116, v117\n"
    "v_max_f32 v120, v120, v121\n"
    "v_max_f32 v124, v124, v125\n"
    "v_sub_f32 v80, 0, v112\n"
    "v_sub_f32 v81, 0, v112\n"
    "v_sub_f32 v82, 0, v112\n"
    "v_sub_f32 v83, 0, v112\n"
    "s_nop 1\n"
    "v_mfma_f32_16x16x32_bf16 v[96:99], a[192:195], a[128:131], v[80:83]\n"
    "v_sub_f32 v84, 0, v116\n"
    "v_sub_f32 v85, 0, v116\n"
    "v_mfma_f32_16x16x32_bf16 v[96:99], a[196:199], a[132:135], v[96:99]\n"
    "v_sub_f32 v86, 0, v116\n"
    "v_sub_f32 v87, 0, v116\n"
    "v_mfma_f32_16x16x32_bf16 v[96:99], a[200:203], a[136:139], v[96:99]\n"
    "v_sub_f32 v88, 0, v120\n"
    "v_sub_f32 v89, 0, v120\n"
    "v_mfma_f32_16x16x32_bf16 v[96:99], a[204:207], a[140:143], v[96:99]\n"
    "v_sub_f32 v90, 0, v120\n"
    "v_sub_f32 v91, 0, v120\n"
    "v_mfma_f32_16x16x32_bf16 v[100:103], a[208:211], a[128:131], v[80:83]\n"
    "v_sub_f32 v92, 0, v124\n"
    "v_sub_f32 v93, 0, v124\n"
    "v_mfma_f32_16x16x32_bf16 v[100:103], a[212:215], a[132:135], v[100:103]\n"
    "v_sub_f32 v94, 0, v124\n"
    "v_sub_f32 v95, 0, v124\n"
    "v_mfma_f32_16x16x32_bf16 v[100:103], a[216:219], a[136:139], v[100:103]\n"
    "v_mfma_f32_16x16x32_bf16 v[100:103], a[220:223], a[140:143], v[100:103]\n"
    "v_exp_f32 v96, v96\n"
    "v_mfma_f32_16x16x32_bf16 v[104:107], a[224:227], a[128:131], v[80:83]\n"
    "v_exp_f32 v97, v97\n"
    "v_mfma_f32_16x16x32_bf16 v[104:107], a[228:231], a[132:135], v[104:107]\n"
    "v_exp_f32 v98, v98\n"
    "v_mfma_f32_16x16x32_bf16 v[104:107], a[232:235], a[136:139], v[104:107]\n"
    "v_exp_f32 v99, v99\n"
    "v_mfma_f32_16x16x32_bf16 v[104:107], a[236:239], a[140:143], v[104:107]\n"
    "v_cvt_pk_bf16_f32 v96, v96, v97\n"
    "v_cvt_pk_bf16_f32 v97, v98, v99\n"
    "v_mfma_f32_16x16x32_bf16 v[108:111], a[240:243], a[128:131], v[80:83]\n"
    "v_exp_f32 v100, v100\n"
    "v_mfma_f32_16x16x32_bf16 v[108:111], a[244:247], a[132:135], v[108:111]\n"
    "v_exp_f32 v101, v101\n"
    "v_mfma_f32_16x16x32_bf16 v[108:111], a[248:251], a[136:139], v[108:111]\n"
    "v_exp_f32 v102, v102\n"
    "v_mfma_f32_16x16x32_bf16 v[108:111], a[252:255], a[140:143], v[108:111]\n"
    "v_exp_f32 v103, v103\n"
    "v_cvt_pk_bf16_f32 v98, v100, v101\n"
    "v_cvt_pk_bf16_f32 v99, v102, v103\n"
    "v_exp_f32 v104, v104\n"
    "v_exp_f32 v105, v105\n"
    "v_exp_f32 v106, v106\n"
    "v_exp_f32 v107, v107\n"
    "v_cvt_pk_bf16_f32 v104, v104, v105\n"
    "v_cvt_pk_bf16_f32 v105, v106, v107\n"
    "v_exp_f32 v108, v108\n"
    "s_mov_b32 s43, 0\n"
    "s_waitcnt lgkmcnt(0)\n"
    "s_mov_b32 s79, 0x3599\n"
    "s_nop 7\n"
    "s_nop 7\n"
    ".Lk2_att:\n"
    "v_mfma_f32_16x16x32_bf16 v[112:115], a[192:195], a[144:147], v[84:87]\n"
    "s_waitcnt vmcnt(20) lgkmcnt(0)\n"
    "s_barrier\n"
    "v_mfma_f32_16x16x32_bf16 v[112:115], a[196:199], a[148:151], v[112:115]\n"
    "v_exp_f32 v109, v109\n"
    "v_mfma_f32_16x16x32_bf16 v[112:115], a[200:203], a[152:155], v[112:115]\n"
    "v_exp_f32 v110, v110\n"
    "v_mfma_f32_16x16x32_bf16 v[112:115], a[204:207], a[156:159], v[112:115]\n"
    "s_add_u32 m0, s81, 0\n"
    "ds_read_b64_tr_b16 a[64:65], v20 offset:16384\n"
    "v_mfma_f32_16x16x32_bf16 v[116:119], a[208:211], a[144:147], v[84:87]\n"
    "buffer_load_dwordx4 v3, s[60:63], s80 offen offset:0 sc1 lds\n"
    "v_mfma_f32_16x16x32_bf16 v[116:119], a[212:215], a[148:151], v[116:119]\n"
    "v_exp_f32 v111, v111\n"
    "v_mfma_f32_16x16x32_bf16 v[116:119], a[216:219], a[152:155], v[116:119]\n"
    "ds_read_b64_tr_b16 a[66:67], v20 offset:20480\n"
    "v_cvt_pk_bf16_f32 v106, v108, v109\n"
    "v_mfma_f32_16x16x32_bf16 v[116:119], a[220:223], a[156:159], v[116:119]\n"
    "v_cvt_pk_bf16_f32 v107, v110, v111\n"
    "ds_read_b64_tr_b16 a[68:69], v20 offset:24576\n"
    "v_mfma_f32_16x16x32_bf16 v[120:123], a[224:227], a[144:147], v[84:87]\n"
    "v_exp_f32 v112, v112\n"
    "v_mfma_f32_16x16x32_bf16 v[120:123], a[228:231], a[148:151], v[120:123]\n"
    "v_exp_f32 v113, v113\n"
    "v_mfma_f32_16x16x32_bf16 v[120:123], a[232:235], a[152:155], v[120:123]\n"
    "ds_read_b64_tr_b16 a[70:71], v20 offset:28672\n"
    "v_mfma_f32_16x16x32_bf16 v[120:123], a[236:239], a[156:159], v[120:123]\n"
    "v_exp_f32 v114, v114\n"
    "v_mfma_f32_16x16x32_bf16 v[124:127], a[240:243], a[144:147], v[84:87]\n"
    "v_mfma_f32_16x16x32_bf16 v[124:127], a[244:247], a[148:151], v[124:127]\n"
    "v_exp_f32 v115, v115\n"
    "v_mfma_f32_16x16x32_bf16 v[124:127], a[248:251], a[152:155], v[124:127]\n"
    "buffer_load_dwordx4 v4, s[60:63], s80 offen offset:1024 sc1 lds\n"
    "v_mfma_f32_16x16x32_bf16 v[124:127], a[252:255], a[156:159], v[124:127]\n"
    "ds_read_b64_tr_b16 a[72:73], v21 offset:16384\n"
    "v_cvt_pk_bf16_f32 v112, v112, v113\n"
    "v_mfma_f32_16x16x32_bf16 v[128:131], a[0:3], v[96:99], v[128:131]\n"
    "v_cvt_pk_bf16_f32 v113, v114, v115\n"
    "v_mfma_f32_16x16x32_bf16 v[128:131], a[4:7], v[104:107], v[128:131]\n"
    "v_exp_f32 v116, v116\n"
    "v_mfma_f32_16x16x32_bf16 v[144:147], a[8:11], v[96:99], v[144:147]\n"
    "ds_read_b64_tr_b16 a[74:75], v21 offset:20480\n"
    "v_mfma_f32_16x16x32_bf16 v[144:147], a[12:15], v[104:107], v[144:147]\n"
    "v_exp_f32 v117, v117\n"
    "v_mfma_f32_16x16x32_bf16 v[160:163], a[16:19], v[96:99], v[160:163]\n"
    "v_exp_f32 v118, v118\n"
    "v_mfma_f32_16x16x32_bf16 v[160:163], a[20:23], v[104:107], v[160:163]\n"
    "ds_read_b64_tr_b16 a[76:77], v21 offset:24576\n"
    "v_mfma_f32_16x16x32_bf16 v[176:179], a[24:27], v[96:99], v[176:179]\n"
    "v_exp_f32 v119, v119\n"
    "v_mfma_f32_16x16x32_bf16 v[176:179], a[28:31], v[104:107], v[176:179]\n"
    "ds_read_b64_tr_b16 a[78:79], v21 offset:28672\n"
    "v_cvt_pk_bf16_f32 v114, v116, v117\n"
    "v_mfma_f32_16x16x32_bf16 v[192:195], a[32:35], v[96:99], v[192:195]\n"
    "buffer_load_dwordx4 v5, s[60:63], s80 offen offset:2048 sc1 lds\n"
    "v_mfma_f32_16x16x32_bf16 v[192:195], a[36:39], v[104:107], v[192:195]\n"
    "v_cvt_pk_bf16_f32 v115, v118, v119\n"
    "v_mfma_f32_16x16x32_bf16 v[208:211], a[40:43], v[96:99], v[208:211]\n"
    "v_exp_f32 v120, v120\n"
    "v_mfma_f32_16x16x32_bf16 v[208:211], a[44:47], v[104:107], v[208:211]\n"
    "ds_read_b64_tr_b16 a[80:81], v20 offset:16896\n"
    "v_mfma_f32_16x16x32_bf16 v[224:227], a[48:51], v[96:99], v[224:227]\n"
    "v_exp_f32 v121, v121\n"
    "v_mfma_f32_16x16x32_bf16 v[224:227], a[52:55], v[104:107], v[224:227]\n"
    "v_exp_f32 v122, v122\n"
    "v_mfma_f32_16x16x32_bf16 v[240:243], a[56:59], v[96:99], v[240:243]\n"
    "ds_read_b64_tr_b16 a[82:83], v20 offset:20992\n"
    "v_mfma_f32_16x16x32_bf16 v[240:243], a[60:63], v[104:107], v[240:243]\n"
    "v_exp_f32 v123, v123\n"
    "v_mfma_f32_16x16x32_bf16 v[64:67], v[60:63], v[96:99], v[64:67]\n"
    "v_cvt_pk_bf16_f32 v120, v120, v121\n"
    "ds_read_b64_tr_b16 a[84:85], v20 offset:25088\n"
    "v_mfma_f32_16x16x32_bf16 v[64:67], v[60:63], v[104:107], v[64:67]\n"
    "v_cvt_pk_bf16_f32 v121, v122, v123\n"
    "v_mfma_f32_16x16x32_bf16 v[96:99], a[192:195], a[160:163], v[88:91]\n"
    "v_exp_f32 v124, v124\n"
    "v_mfma_f32_16x16x32_bf16 v[96:99], a[196:199], a[164:167], v[96:99]\n"
    "v_exp_f32 v125, v125\n"
    "v_mfma_f32_16x16x32_bf16 v[96:99], a[200:203], a[168:171], v[96:99]\n"
    "ds_read_b64_tr_b16 a[86:87], v20 offset:29184\n"
    "v_mfma_f32_16x16x32_bf16 v[96:99], a[204:207], a[172:175], v[96:99]\n"
    "v_exp_f32 v126, v126\n"
    "v_mfma_f32_16x16x32_bf16 v[100:103], a[208:211], a[160:163], v[88:91]\n"
    "buffer_load_dwordx4 v6, s[60:63], s80 offen offset:3072 sc1 lds\n"
    "v_mfma_f32_16x16x32_bf16 v[100:103], a[212:215], a[164:167], v[100:103]\n"
    "ds_read_b64_tr_b16 a[88:89], v21 offset:16896\n"
    "v_mfma_f32_16x16x32_bf16 v[100:103], a[216:219], a[168:171], v[100:103]\n"
    "v_exp_f32 v127, v127\n"
    "v_mfma_f32_16x16x32_bf16 v[100:103], a[220:223], a[172:175], v[100:103]\n"
    "v_cvt_pk_bf16_f32 v122, v124, v125\n"
    "ds_read_b64_tr_b16 a[90:91], v21 offset:20992\n"
    "v_mfma_f32_16x16x32_bf16 v[104:107], a[224:227], a[160:163], v[88:91]\n"
    "v_cvt_pk_bf16_f32 v123, v126, v127\n"
    "v_mfma_f32_16x16x32_bf16 v[104:107], a[228:231], a[164:167], v[104:107]\n"
    "v_exp_f32 v96, v96\n"
    "v_mfma_f32_16x16x32_bf16 v[104:107], a[232:235], a[168:171], v[104:107]\n"
    "v_exp_f32 v97, v97\n"
    "v_mfma_f32_16x16x32_bf16 v[104:107], a[236:239], a[172:175], v[104:107]\n"
    "ds_read_b64_tr_b16 a[92:93], v21 offset:25088\n"
    "v_mfma_f32_16x16x32_bf16 v[108:111], a[240:243], a[160:163], v[88:91]\n"
    "v_exp_f32 v98, v98\n"
    "v_mfma_f32_16x16x32_bf16 v[108:111], a[244:247], a[164:167], v[108:111]\n"
    "s_add_u32 m0, s81, 65536\n"
    "ds_read_b64_tr_b16 a[94:95], v21 offset:29184\n"
    "v_mfma_f32_16x16x32_bf16 v[108:111], a[248:251], a[168:171], v[108:111]\n"
    "buffer_load_dwordx4 v7, s[64:67], s80 offen offset:0 sc1 lds\n"
    "v_mfma_f32_16x16x32_bf16 v[108:111], a[252:255], a[172:175], v[108:111]\n"
    "v_exp_f32 v99, v99\n"
    "v_mfma_f32_16x16x32_bf16 v[132:135], a[0:3], v[112:115], v[132:135]\n"
    "v_cvt_pk_bf16_f32 v96, v96, v97\n"
    "v_cvt_pk_bf16_f32 v97, v98, v99\n"
    "v_mfma_f32_16x16x32_bf16 v[132:135], a[4:7], v[120:123], v[132:135]\n"
    "ds_read_b64_tr_b16 a[96:97], v20 offset:17408\n"
    "v_mfma_f32_16x16x32_bf16 v[148:151], a[8:11], v[112:115], v[148:151]\n"
    "v_exp_f32 v100, v100\n"
    "v_mfma_f32_16x16x32_bf16 v[148:151], a[12:15], v[120:123], v[148:151]\n"
    "v_exp_f32 v101, v101\n"
    "v_mfma_f32_16x16x32_bf16 v[164:167], a[16:19], v[112:115], v[164:167]\n"
    "ds_read_b64_tr_b16 a[98:99], v20 offset:21504\n"
    "v_mfma_f32_16x16x32_bf16 v[164:167], a[20:23], v[120:123], v[164:167]\n"
    "v_exp_f32 v102, v102\n"
    "v_mfma_f32_16x16x32_bf16 v[180:183], a[24:27], v[112:115], v[180:183]\n"
    "v_exp_f32 v103, v103\n"
    "v_mfma_f32_16x16x32_bf16 v[180:183], a[28:31], v[120:123], v[180:183]\n"
    "s_add_u32 m0, s81, 66432\n"
    "ds_read_b64_tr_b16 a[100:101], v20 offset:25600\n"
    "v_mfma_f32_16x16x32_bf16 v[196:199], a[32:35], v[112:115], v[196:199]\n"
    "buffer_load_dwordx4 v9, s[64:67], s80 offen offset:128 sc1 lds\n"
    "v_mfma_f32_16x16x32_bf16 v[196:199], a[36:39], v[120:123], v[196:199]\n"
    "v_cvt_pk_bf16_f32 v98, v100, v101\n"
    "v_cvt_pk_bf16_f32 v99, v102, v103\n"
    "v_mfma_f32_16x16x32_bf16 v[212:215], a[40:43], v[112:115], v[212:215]\n"
    "ds_read_b64_tr_b16 a[102:103], v20 offset:29696\n"
    "v_mfma_f32_16x16x32_bf16 v[212:215], a[44:47], v[120:123], v[212:215]\n"
    "v_exp_f32 v104, v104\n"
    "v_mfma_f32_16x16x32_bf16 v[228:231], a[48:51], v[112:115], v[228:231]\n"
    "v_exp_f32 v105, v105\n"
    "v_mfma_f32_16x16x32_bf16 v[228:231], a[52:55], v[120:123], v[228:231]\n"
    "ds_read_b64_tr_b16 a[104:105], v21 offset:17408\n"
    "v_mfma_f32_16x16x32_bf16 v[244:247], a[56:59], v[112:115], v[244:247]\n"
    "v_exp_f32 v106, v106\n"
    "v_mfma_f32_16x16x32_bf16 v[244:247], a[60:63], v[120:123], v[244:247]\n"
    "v_exp_f32 v107, v107\n"
    "v_mfma_f32_16x16x32_bf16 v[68:71], v[60:63], v[112:115], v[68:71]\n"
    "ds_read_b64_tr_b16 a[106:107], v21 offset:21504\n"
    "v_cvt_pk_bf16_f32 v104, v104, v105\n"
    "v_mfma_f32_16x16x32_bf16 v[68:71], v[60:63], v[120:123], v[68:71]\n"
    "v_cvt_pk_bf16_f32 v105, v106, v107\n"
    "v_mfma_f32_16x16x32_bf16 v[112:115], a[192:195], a[176:179], v[92:95]\n"
    "v_exp_f32 v108, v108\n"
    "v_mfma_f32_16x16x32_bf16 v[112:115], a[196:199], a[180:183], v[112:115]\n"
    "v_exp_f32 v109, v109\n"
    "v_mfma_f32_16x16x32_bf16 v[112:115], a[200:203], a[184:187], v[112:115]\n"
    "ds_read_b128 a[192:195], v16 offset:16384\n"
    "v_mfma_f32_16x16x32_bf16 v[112:115], a[204:207], a[188:191], v[112:115]\n"
    "v_exp_f32 v110, v110\n"
    "v_mfma_f32_16x16x32_bf16 v[116:119], a[208:211], a[176:179], v[92:95]\n"
    "ds_read_b128 a[196:199], v17 offset:16384\n"
    "v_mfma_f32_16x16x32_bf16 v[116:119], a[212:215], a[180:183], v[116:119]\n"
    "v_exp_f32 v111, v111\n"
    "v_mfma_f32_16x16x32_bf16 v[116:119], a[216:219], a[184:187], v[116:119]\n"
    "ds_read_b128 a[200:203], v18 offset:16384\n"
    "ds_read_b128 a[204:207], v19 offset:16384\n"
    "v_mfma_f32_16x16x32_bf16 v[116:119], a[220:223], a[188:191], v[116:119]\n"
    "v_cvt_pk_bf16_f32 v106, v108, v109\n"
    "v_cvt_pk_bf16_f32 v107, v110, v111\n"
    "v_mfma_f32_16x16x32_bf16 v[120:123], a[224:227], a[176:179], v[92:95]\n"
    "ds_read_b128 a[208:211], v16 offset:20480\n"
    "v_mfma_f32_16x16x32_bf16 v[120:123], a[228:231], a[180:183], v[120:123]\n"
    "v_exp_f32 v112, v112\n"
    "v_mfma_f32_16x16x32_bf16 v[120:123], a[232:235], a[184:187], v[120:123]\n"
    "ds_read_b128 a[212:215], v17 offset:20480\n"
    "v_mfma_f32_16x16x32_bf16 v[120:123], a[236:239], a[188:191], v[120:123]\n"
    "v_exp_f32 v113, v113\n"
    "v_mfma_f32_16x16x32_bf16 v[124:127], a[240:243], a[176:179], v[92:95]\n"
    "ds_read_b128 a[216:219], v18 offset:20480\n"
    "v_mfma_f32_16x16x32_bf16 v[124:127], a[244:247], a[180:183], v[124:127]\n"
    "v_exp_f32 v114, v114\n"
    "v_mfma_f32_16x16x32_bf16 v[124:127], a[248:251], a[184:187], v[124:127]\n"
    "ds_read_b128 a[220:223], v19 offset:20480\n"
    "v_mfma_f32_16x16x32_bf16 v[124:127], a[252:255], a[188:191], v[124:127]\n"
    "v_exp_f32 v115, v115\n"
    "v_mfma_f32_16x16x32_bf16 v[136:139], a[0:3], v[96:99], v[136:139]\n"
    "ds_read_b128 a[224:227], v16 offset:24576\n"
    "v_cvt_pk_bf16_f32 v112, v112, v113\n"
    "v_mfma_f32_16x16x32_bf16 v[136:139], a[4:7], v[104:107], v[136:139]\n"
    "ds_read_b128 a[228:231], v17 offset:24576\n"
    "v_cvt_pk_bf16_f32 v113, v114, v115\n"
    "v_mfma_f32_16x16x32_bf16 v[152:155], a[8:11], v[96:99], v[152:155]\n"
    "v_exp_f32 v116, v116\n"
    "v_mfma_f32_16x16x32_bf16 v[152:155], a[12:15], v[104:107], v[152:155]\n"
    "ds_read_b128 a[232:235], v18 offset:24576\n"
    "ds_read_b128 a[236:239], v19 offset:24576\n"
    "v_mfma_f32_16x16x32_bf16 v[168:171], a[16:19], v[96:99], v[168:171]\n"
    "v_exp_f32 v117, v117\n"
    "v_mfma_f32_16x16x32_bf16 v[168:171], a[20:23], v[104:107], v[168:171]\n"
    "v_exp_f32 v118, v118\n"
    "v_mfma_f32_16x16x32_bf16 v[184:187], a[24:27], v[96:99], v[184:187]\n"
    "ds_read_b128 a[240:243], v16 offset:28672\n"
    "ds_read_b128 a[244:247], v17 offset:28672\n"
    "v_mfma_f32_16x16x32_bf16 v[184:187], a[28:31], v[104:107], v[184:187]\n"
    "v_exp_f32 v119, v119\n"
    "v_mfma_f32_16x16x32_bf16 v[200:203], a[32:35], v[96:99], v[200:203]\n"
    "ds_read_b128 a[248:251], v18 offset:28672\n"
    "v_cvt_pk_bf16_f32 v114, v116, v117\n"
    "v_mfma_f32_16x16x32_bf16 v[200:203], a[36:39], v[104:107], v[200:203]\n"
    "ds_read_b128 a[252:255], v19 offset:28672\n"
    "v_cvt_pk_bf16_f32 v115, v118, v119\n"
    "v_mfma_f32_16x16x32_bf16 v[216:219], a[40:43], v[96:99], v[216:219]\n"
    "v_exp_f32 v120, v120\n"
    "v_mfma_f32_16x16x32_bf16 v[216:219], a[44:47], v[104:107], v[216:219]\n"
    "s_add_u32 m0, s81, 65536\n"
    "v_mfma_f32_16x16x32_bf16 v[232:235], a[48:51], v[96:99], v[232:235]\n"
    "buffer_load_dwordx4 v7, s[64:67], s80 offen offset:2048 sc1 lds\n"
    "v_mfma_f32_16x16x32_bf16 v[232:235], a[52:55], v[104:107], v[232:235]\n"
    "v_exp_f32 v121, v121\n"
    "v_mfma_f32_16x16x32_bf16 v[248:251], a[56:59], v[96:99], v[248:251]\n"
    "v_exp_f32 v122, v122\n"
    "v_mfma_f32_16x16x32_bf16 v[248:251], a[60:63], v[104:107], v[248:251]\n"
    "v_exp_f32 v123, v123\n"
    "v_mfma_f32_16x16x32_bf16 v[72:75], v[60:63], v[96:99], v[72:75]\n"
    "v_cvt_pk_bf16_f32 v120, v120, v121\n"
    "v_cvt_pk_bf16_f32 v121, v122, v123\n"
    "v_mfma_f32_16x16x32_bf16 v[72:75], v[60:63], v[104:107], v[72:75]\n"
    "s_waitcnt lgkmcnt(0)\n"
    "v_exp_f32 v124, v124\n"
    "v_mfma_f32_16x16x32_bf16 v[96:99], a[192:195], a[128:131], v[80:83]\n"
    "ds_read_b64_tr_b16 a[108:109], v21 offset:25600\n"
    "v_mfma_f32_16x16x32_bf16 v[96:99], a[196:199], a[132:135], v[96:99]\n"
    "v_exp_f32 v125, v125\n"
    "v_mfma_f32_16x16x32_bf16 v[96:99], a[200:203], a[136:139], v[96:99]\n"
    "v_exp_f32 v126, v126\n"
    "v_mfma_f32_16x16x32_bf16 v[96:99], a[204:207], a[140:143], v[96:99]\n"
    "s_add_u32 m0, s81, 66432\n"
    "ds_read_b64_tr_b16 a[110:111], v21 offset:29696\n"
    "v_mfma_f32_16x16x32_bf16 v[100:103], a[208:211], a[128:131], v[80:83]\n"
    "buffer_load_dwordx4 v9, s[64:67], s80 offen offset:2176 sc1 lds\n"
    "v_mfma_f32_16x16x32_bf16 v[100:103], a[212:215], a[132:135], v[100:103]\n"
    "v_exp_f32 v127, v127\n"
    "v_mfma_f32_16x16x32_bf16 v[100:103], a[216:219], a[136:139], v[100:103]\n"
    "s_add_u32 s80, s80, 16384\n"
    "ds_read_b64_tr_b16 a[112:113], v20 offset:17920\n"
    "v_mfma_f32_16x16x32_bf16 v[100:103], a[220:223], a[140:143], v[100:103]\n"
    "v_cvt_pk_bf16_f32 v122, v124, v125\n"
    "v_cvt_pk_bf16_f32 v123, v126, v127\n"
    "v_mfma_f32_16x16x32_bf16 v[104:107], a[224:227], a[128:131], v[80:83]\n"
    "v_exp_f32 v96, v96\n"
    "v_mfma_f32_16x16x32_bf16 v[104:107], a[228:231], a[132:135], v[104:107]\n"
    "ds_read_b64_tr_b16 a[114:115], v20 offset:22016\n"
    "v_mfma_f32_16x16x32_bf16 v[104:107], a[232:235], a[136:139], v[104:107]\n"
    "v_exp_f32 v97, v97\n"
    "v_mfma_f32_16x16x32_bf16 v[104:107], a[236:239], a[140:143], v[104:107]\n"
    "v_exp_f32 v98, v98\n"
    "v_mfma_f32_16x16x32_bf16 v[108:111], a[240:243], a[128:131], v[80:83]\n"
    "ds_read_b64_tr_b16 a[116:117], v20 offset:26112\n"
    "v_mfma_f32_16x16x32_bf16 v[108:111], a[244:247], a[132:135], v[108:111]\n"
    "v_exp_f32 v99, v99\n"
    "v_mfma_f32_16x16x32_bf16 v[108:111], a[248:251], a[136:139], v[108:111]\n"
    "s_waitcnt vmcnt(18)\n"
    "buffer_store_dwordx4 v[24:27], v2, s[48:51], s93 offen offset:0 sc1\n"
    "v_mfma_f32_16x16x32_bf16 v[108:111], a[252:255], a[140:143], v[108:111]\n"
    "v_cvt_pk_bf16_f32 v96, v96, v97\n"
    "ds_read_b64_tr_b16 a[118:119], v20 offset:30208\n"
    "v_mfma_f32_16x16x32_bf16 v[140:143], a[0:3], v[112:115], v[140:143]\n"
    "v_cvt_pk_bf16_f32 v97, v98, v99\n"
    "v_mfma_f32_16x16x32_bf16 v[140:143], a[4:7], v[120:123], v[140:143]\n"
    "v_exp_f32 v100, v100\n"
    "v_mfma_f32_16x16x32_bf16 v[156:159], a[8:11], v[112:115], v[156:159]\n"
    "ds_read_b64_tr_b16 a[120:121], v21 offset:17920\n"
    "v_mfma_f32_16x16x32_bf16 v[156:159], a[12:15], v[120:123], v[156:159]\n"
    "v_exp_f32 v101, v101\n"
    "v_mfma_f32_16x16x32_bf16 v[172:175], a[16:19], v[112:115], v[172:175]\n"
    "v_exp_f32 v102, v102\n"
    "v_mfma_f32_16x16x32_bf16 v[172:175], a[20:23], v[120:123], v[172:175]\n"
    "ds_read_b64_tr_b16 a[122:123], v21 offset:22016\n"
    "v_mfma_f32_16x16x32_bf16 v[188:191], a[24:27], v[112:115], v[188:191]\n"
    "buffer_load_dwordx4 v[24:27], v8, s[44:47], s92 offen offset:0\n"
    "v_mfma_f32_16x16x32_bf16 v[188:191], a[28:31], v[120:123], v[188:191]\n"
    "v_exp_f32 v103, v103\n"
    "v_mfma_f32_16x16x32_bf16 v[204:207], a[32:35], v[112:115], v[204:207]\n"
    "ds_read_b64_tr_b16 a[124:125], v21 offset:26112\n"
    "v_cvt_pk_bf16_f32 v98, v100, v101\n"
    "v_mfma_f32_16x16x32_bf16 v[204:207], a[36:39], v[120:123], v[204:207]\n"
    "v_cvt_pk_bf16_f32 v99, v102, v103\n"
    "v_mfma_f32_16x16x32_bf16 v[220:223], a[40:43], v[112:115], v[220:223]\n"
    "v_exp_f32 v104, v104\n"
    "v_mfma_f32_16x16x32_bf16 v[220:223], a[44:47], v[120:123], v[220:223]\n"
    "ds_read_b64_tr_b16 a[126:127], v21 offset:30208\n"
    "v_mfma_f32_16x16x32_bf16 v[236:239], a[48:51], v[112:115], v[236:239]\n"
    "v_exp_f32 v105, v105\n"
    "v_mfma_f32_16x16x32_bf16 v[236:239], a[52:55], v[120:123], v[236:239]\n"
    "v_exp_f32 v106, v106\n"
    "v_mfma_f32_16x16x32_bf16 v[252:255], a[56:59], v[112:115], v[252:255]\n"
    "v_exp_f32 v107, v107\n"
    "v_mfma_f32_16x16x32_bf16 v[252:255], a[60:63], v[120:123], v[252:255]\n"
    "v_cvt_pk_bf16_f32 v104, v104, v105\n"
    "v_mfma_f32_16x16x32_bf16 v[76:79], v[60:63], v[112:115], v[76:79]\n"
    "v_cvt_pk_bf16_f32 v105, v106, v107\n"
    "v_mfma_f32_16x16x32_bf16 v[76:79], v[60:63], v[120:123], v[76:79]\n"
    "v_exp_f32 v108, v108\n"
    "v_mfma_f32_16x16x32_bf16 v[112:115], a[192:195], a[144:147], v[84:87]\n"
    "s_waitcnt vmcnt(20) lgkmcnt(0)\n"
    "s_barrier\n"
    "v_mfma_f32_16x16x32_bf16 v[112:115], a[196:199], a[148:151], v[112:115]\n"
    "v_xor_b32 v20, 0x8000, v20\n"
    "v_xor_b32 v21, 0x8000, v21\n"
    "v_mfma_f32_16x16x32_bf16 v[112:115], a[200:203], a[152:155], v[112:115]\n"
    "v_exp_f32 v109, v109\n"
    "v_mfma_f32_16x16x32_bf16 v[112:115], a[204:207], a[156:159], v[112:115]\n"
    "s_add_u32 m0, s81, 16384\n"
    "v_mfma_f32_16x16x32_bf16 v[116:119], a[208:211], a[144:147], v[84:87]\n"
    "buffer_load_dwordx4 v3, s[60:63], s80 offen offset:0 sc1 lds\n"
    "v_mfma_f32_16x16x32_bf16 v[116:119], a[212:215], a[148:151], v[116:119]\n"
    "v_exp_f32 v110, v110\n"
    "ds_read_b64_tr_b16 a[0:1], v20 offset:0\n"
    "v_mfma_f32_16x16x32_bf16 v[116:119], a[216:219], a[152:155], v[116:119]\n"
    "v_exp_f32 v111, v111\n"
    "v_mfma_f32_16x16x32_bf16 v[116:119], a[220:223], a[156:159], v[116:119]\n"
    "ds_read_b64_tr_b16 a[2:3], v20 offset:4096\n"
    "v_cvt_pk_bf16_f32 v106, v108, v109\n"
    "v_mfma_f32_16x16x32_bf16 v[120:123], a[224:227], a[144:147], v[84:87]\n"
    "v_cvt_pk_bf16_f32 v107, v110, v111\n"
    "ds_read_b64_tr_b16 a[4:5], v20 offset:8192\n"
    "v_mfma_f32_16x16x32_bf16 v[120:123], a[228:231], a[148:151], v[120:123]\n"
    "v_exp_f32 v112, v112\n"
    "v_mfma_f32_16x16x32_bf16 v[120:123], a[232:235], a[152:155], v[120:123]\n"
    "v_exp_f32 v113, v113\n"
    "v_mfma_f32_16x16x32_bf16 v[120:123], a[236:239], a[156:159], v[120:123]\n"
    "ds_read_b64_tr_b16 a[6:7], v20 offset:12288\n"
    "v_mfma_f32_16x16x32_bf16 v[124:127], a[240:243], a[144:147], v[84:87]\n"
    "v_exp_f32 v114, v114\n"
    "v_mfma_f32_16x16x32_bf16 v[124:127], a[244:247], a[148:151], v[124:127]\n"
    "v_exp_f32 v115, v115\n"
    "v_mfma_f32_16x16x32_bf16 v[124:127], a[248:251], a[152:155], v[124:127]\n"
    "buffer_load_dwordx4 v4, s[60:63], s80 offen offset:1024 sc1 lds\n"
    "v_mfma_f32_16x16x32_bf16 v[124:127], a[252:255], a[156:159], v[124:127]\n"
    "ds_read_b64_tr_b16 a[8:9], v21 offset:0\n"
    "v_cvt_pk_bf16_f32 v112, v112, v113\n"
    "v_mfma_f32_16x16x32_bf16 v[128:131], a[64:67], v[96:99], v[128:131]\n"
    "v_cvt_pk_bf16_f32 v113, v114, v115\n"
    "v_mfma_f32_16x16x32_bf16 v[128:131], a[68:71], v[104:107], v[128:131]\n"
    "v_exp_f32 v116, v116\n"
    "v_mfma_f32_16x16x32_bf16 v[144:147], a[72:75], v[96:99], v[144:147]\n"
    "ds_read_b64_tr_b16 a[10:11], v21 offset:4096\n"
    "v_mfma_f32_16x16x32_bf16 v[144:147], a[76:79], v[104:107], v[144:147]\n"
    "v_exp_f32 v117, v117\n"
    "v_mfma_f32_16x16x32_bf16 v[160:163], a[80:83], v[96:99], v[160:163]\n"
    "v_exp_f32 v118, v118\n"
    "v_mfma_f32_16x16x32_bf16 v[160:163], a[84:87], v[104:107], v[160:163]\n"
    "ds_read_b64_tr_b16 a[12:13], v21 offset:8192\n"
    "v_mfma_f32_16x16x32_bf16 v[176:179], a[88:91], v[96:99], v[176:179]\n"
    "v_exp_f32 v119, v119\n"
    "v_mfma_f32_16x16x32_bf16 v[176:179], a[92:95], v[104:107], v[176:179]\n"
    "ds_read_b64_tr_b16 a[14:15], v21 offset:12288\n"
    "v_cvt_pk_bf16_f32 v114, v116, v117\n"
    "v_mfma_f32_16x16x32_bf16 v[192:195], a[96:99], v[96:99], v[192:195]\n"
    "buffer_load_dwordx4 v5, s[60:63], s80 offen offset:2048 sc1 lds\n"
    "v_mfma_f32_16x16x32_bf16 v[192:195], a[100:103], v[104:107], v[192:195]\n"
    "v_cvt_pk_bf16_f32 v115, v118, v119\n"
    "v_mfma_f32_16x16x32_bf16 v[208:211], a[104:107], v[96:99], v[208:211]\n"
    "v_exp_f32 v120, v120\n"
    "v_mfma_f32_16x16x32_bf16 v[208:211], a[108:111], v[104:107], v[208:211]\n"
    "ds_read_b64_tr_b16 a[16:17], v20 offset:512\n"
    "v_mfma_f32_16x16x32_bf16 v[224:227], a[112:115], v[96:99], v[224:227]\n"
    "v_exp_f32 v121, v121\n"
    "v_mfma_f32_16x16x32_bf16 v[224:227], a[116:119], v[104:107], v[224:227]\n"
    "v_exp_f32 v122, v122\n"
    "v_mfma_f32_16x16x32_bf16 v[240:243], a[120:123], v[96:99], v[240:243]\n"
    "ds_read_b64_tr_b16 a[18:19], v20 offset:4608\n"
    "v_mfma_f32_16x16x32_bf16 v[240:243], a[124:127], v[104:107], v[240:243]\n"
    "v_exp_f32 v123, v123\n"
    "v_mfma_f32_16x16x32_bf16 v[64:67], v[60:63], v[96:99], v[64:67]\n"
    "v_cvt_pk_bf16_f32 v120, v120, v121\n"
    "ds_read_b64_tr_b16 a[20:21], v20 offset:8704\n"
    "v_mfma_f32_16x16x32_bf16 v[64:67], v[60:63], v[104:107], v[64:67]\n"
    "v_cvt_pk_bf16_f32 v121, v122, v123\n"
    "v_mfma_f32_16x16x32_bf16 v[96:99], a[192:195], a[160:163], v[88:91]\n"
    "v_exp_f32 v124, v124\n"
    "v_mfma_f32_16x16x32_bf16 v[96:99], a[196:199], a[164:167], v[96:99]\n"
    "v_exp_f32 v125, v125\n"
    "v_mfma_f32_16x16x32_bf16 v[96:99], a[200:203], a[168:171], v[96:99]\n"
    "ds_read_b64_tr_b16 a[22:23], v20 offset:12800\n"
    "v_mfma_f32_16x16x32_bf16 v[96:99], a[204:207], a[172:175], v[96:99]\n"
    "v_exp_f32 v126, v126\n"
    "v_mfma_f32_16x16x32_bf16 v[100:103], a[208:211], a[160:163], v[88:91]\n"
    "buffer_load_dwordx4 v6, s[60:63], s80 offen offset:3072 sc1 lds\n"
    "v_mfma_f32_16x16x32_bf16 v[100:103], a[212:215], a[164:167], v[100:103]\n"
    "ds_read_b64_tr_b16 a[24:25], v21 offset:512\n"
    "v_mfma_f32_16x16x32_bf16 v[100:103], a[216:219], a[168:171], v[100:103]\n"
    "v_exp_f32 v127, v127\n"
    "v_mfma_f32_16x16x32_bf16 v[100:103], a[220:223], a[172:175], v[100:103]\n"
    "v_cvt_pk_bf16_f32 v122, v124, v125\n"
    "ds_read_b64_tr_b16 a[26:27], v21 offset:4608\n"
    "v_mfma_f32_16x16x32_bf16 v[104:107], a[224:227], a[160:163], v[88:91]\n"
    "v_cvt_pk_bf16_f32 v123, v126, v127\n"
    "v_mfma_f32_16x16x32_bf16 v[104:107], a[228:231], a[164:167], v[104:107]\n"
    "v_exp_f32 v96, v96\n"
    "v_mfma_f32_16x16x32_bf16 v[104:107], a[232:235], a[168:171], v[104:107]\n"
    "v_exp_f32 v97, v97\n"
    "v_mfma_f32_16x16x32_bf16 v[104:107], a[236:239], a[172:175], v[104:107]\n"
    "ds_read_b64_tr_b16 a[28:29], v21 offset:8704\n"
    "v_mfma_f32_16x16x32_bf16 v[108:111], a[240:243], a[160:163], v[88:91]\n"
    "v_exp_f32 v98, v98\n"
    "v_mfma_f32_16x16x32_bf16 v[108:111], a[244:247], a[164:167], v[108:111]\n"
    "s_add_u32 m0, s81, 81920\n"
    "ds_read_b64_tr_b16 a[30:31], v21 offset:12800\n"
    "v_mfma_f32_16x16x32_bf16 v[108:111], a[248:251], a[168:171], v[108:111]\n"
    "buffer_load_dwordx4 v7, s[64:67], s80 offen offset:0 sc1 lds\n"
    "v_mfma_f32_16x16x32_bf16 v[108:111], a[252:255], a[172:175], v[108:111]\n"
    "v_exp_f32 v99, v99\n"
    "v_mfma_f32_16x16x32_bf16 v[132:135], a[64:67], v[112:115], v[132:135]\n"
    "v_cvt_pk_bf16_f32 v96, v96, v97\n"
    "v_cvt_pk_bf16_f32 v97, v98, v99\n"
    "v_mfma_f32_16x16x32_bf16 v[132:135], a[68:71], v[120:123], v[132:135]\n"
    "ds_read_b64_tr_b16 a[32:33], v20 offset:1024\n"
    "v_mfma_f32_16x16x32_bf16 v[148:151], a[72:75], v[112:115], v[148:151]\n"
    "v_exp_f32 v100, v100\n"
    "v_mfma_f32_16x16x32_bf16 v[148:151], a[76:79], v[120:123], v[148:151]\n"
    "v_exp_f32 v101, v101\n"
    "v_mfma_f32_16x16x32_bf16 v[164:167], a[80:83], v[112:115], v[164:167]\n"
    "ds_read_b64_tr_b16 a[34:35], v20 offset:5120\n"
    "v_mfma_f32_16x16x32_bf16 v[164:167], a[84:87], v[120:123], v[164:167]\n"
    "v_exp_f32 v102, v102\n"
    "v_mfma_f32_16x16x32_bf16 v[180:183], a[88:91], v[112:115], v[180:183]\n"
    "v_exp_f32 v103, v103\n"
    "v_mfma_f32_16x16x32_bf16 v[180:183], a[92:95], v[120:123], v[180:183]\n"
    "s_add_u32 m0, s81, 82816\n"
    "ds_read_b64_tr_b16 a[36:37], v20 offset:9216\n"
    "v_mfma_f32_16x16x32_bf16 v[196:199], a[96:99], v[112:115], v[196:199]\n"
    "buffer_load_dwordx4 v9, s[64:67], s80 offen offset:128 sc1 lds\n"
    "v_mfma_f32_16x16x32_bf16 v[196:199], a[100:103], v[120:123], v[196:199]\n"
    "v_cvt_pk_bf16_f32 v98, v100, v101\n"
    "v_cvt_pk_bf16_f32 v99, v102, v103\n"
    "v_mfma_f32_16x16x32_bf16 v[212:215], a[104:107], v[112:115], v[212:215]\n"
    "ds_read_b64_tr_b16 a[38:39], v20 offset:13312\n"
    "v_mfma_f32_16x16x32_bf16 v[212:215], a[108:111], v[120:123], v[212:215]\n"
    "v_exp_f32 v104, v104\n"
    "v_mfma_f32_16x16x32_bf16 v[228:231], a[112:115], v[112:115], v[228:231]\n"
    "v_exp_f32 v105, v105\n"
    "v_mfma_f32_16x16x32_bf16 v[228:231], a[116:119], v[120:123], v[228:231]\n"
    "ds_read_b64_tr_b16 a[40:41], v21 offset:1024\n"
    "v_mfma_f32_16x16x32_bf16 v[244:247], a[120:123], v[112:115], v[244:247]\n"
    "v_exp_f32 v106, v106\n"
    "v_mfma_f32_16x16x32_bf16 v[244:247], a[124:127], v[120:123], v[244:247]\n"
    "v_exp_f32 v107, v107\n"
    "v_mfma_f32_16x16x32_bf16 v[68:71], v[60:63], v[112:115], v[68:71]\n"
    "ds_read_b64_tr_b16 a[42:43], v21 offset:5120\n"
    "v_xor_b32 v16, 0x8000, v16\n"
    "v_xor_b32 v17, 0x8000, v17\n"
    "v_xor_b32 v18, 0x8000, v18\n"
    "v_xor_b32 v19, 0x8000, v19\n"
    "v_mfma_f32_16x16x32_bf16 v[68:71], v[60:63], v[120:123], v[68:71]\n"
    "v_cvt_pk_bf16_f32 v104, v104, v105\n"
    "v_cvt_pk_bf16_f32 v105, v106, v107\n"
    "v_mfma_f32_16x16x32_bf16 v[112:115], a[192:195], a[176:179], v[92:95]\n"
    "v_exp_f32 v108, v108\n"
    "v_mfma_f32_16x16x32_bf16 v[112:115], a[196:199], a[180:183], v[112:115]\n"
    "v_exp_f32 v109, v109\n"
    "v_mfma_f32_16x16x32_bf16 v[112:115], a[200:203], a[184:187], v[112:115]\n"
    "ds_read_b128 a[192:195], v16 offset:0\n"
    "v_mfma_f32_16x16x32_bf16 v[112:115], a[204:207], a[188:191], v[112:115]\n"
    "v_exp_f32 v110, v110\n"
    "v_mfma_f32_16x16x32_bf16 v[116:119], a[208:211], a[176:179], v[92:95]\n"
    "ds_read_b128 a[196:199], v17 offset:0\n"
    "v_mfma_f32_16x16x32_bf16 v[116:119], a[212:215], a[180:183], v[116:119]\n"
    "v_exp_f32 v111, v111\n"
    "v_mfma_f32_16x16x32_bf16 v[116:119], a[216:219], a[184:187], v[116:119]\n"
    "ds_read_b128 a[200:203], v18 offset:0\n"
    "ds_read_b128 a[204:207], v19 offset:0\n"
    "v_mfma_f32_16x16x32_bf16 v[116:119], a[220:223], a[188:191], v[116:119]\n"
    "v_cvt_pk_bf16_f32 v106, v108, v109\n"
    "v_cvt_pk_bf16_f32 v107, v110, v111\n"
    "v_mfma_f32_16x16x32_bf16 v[120:123], a[224:227], a[176:179], v[92:95]\n"
    "ds_read_b128 a[208:211], v16 offset:4096\n"
    "v_mfma_f32_16x16x32_bf16 v[120:123], a[228:231], a[180:183], v[120:123]\n"
    "v_exp_f32 v112, v112\n"
    "v_mfma_f32_16x16x32_bf16 v[120:123], a[232:235], a[184:187], v[120:123]\n"
    "ds_read_b128 a[212:215], v17 offset:4096\n"
    "v_mfma_f32_16x16x32_bf16 v[120:123], a[236:239], a[188:191], v[120:123]\n"
    "v_exp_f32 v113, v113\n"
    "v_mfma_f32_16x16x32_bf16 v[124:127], a[240:243], a[176:179], v[92:95]\n"
    "ds_read_b128 a[216:219], v18 offset:4096\n"
    "v_mfma_f32_16x16x32_bf16 v[124:127], a[244:247], a[180:183], v[124:127]\n"
    "v_exp_f32 v114, v114\n"
    "v_mfma_f32_16x16x32_bf16 v[124:127], a[248:251], a[184:187], v[124:127]\n"
    "ds_read_b128 a[220:223], v19 offset:4096\n"
    "v_mfma_f32_16x16x32_bf16 v[124:127], a[252:255], a[188:191], v[124:127]\n"
    "v_exp_f32 v115, v115\n"
    "v_mfma_f32_16x16x32_bf16 v[136:139], a[64:67], v[96:99], v[136:139]\n"
    "ds_read_b128 a[224:227], v16 offset:8192\n"
    "v_cvt_pk_bf16_f32 v112, v112, v113\n"
    "v_mfma_f32_16x16x32_bf16 v[136:139], a[68:71], v[104:107], v[136:139]\n"
    "ds_read_b128 a[228:231], v17 offset:8192\n"
    "v_cvt_pk_bf16_f32 v113, v114, v115\n"
    "v_mfma_f32_16x16x32_bf16 v[152:155], a[72:75], v[96:99], v[152:155]\n"
    "v_exp_f32 v116, v116\n"
    "v_mfma_f32_16x16x32_bf16 v[152:155], a[76:79], v[104:107], v[152:155]\n"
    "ds_read_b128 a[232:235], v18 offset:8192\n"
    "ds_read_b128 a[236:239], v19 offset:8192\n"
    "v_mfma_f32_16x16x32_bf16 v[168:171], a[80:83], v[96:99], v[168:171]\n"
    "v_exp_f32 v117, v117\n"
    "v_mfma_f32_16x16x32_bf16 v[168:171], a[84:87], v[104:107], v[168:171]\n"
    "v_exp_f32 v118, v118\n"
    "v_mfma_f32_16x16x32_bf16 v[184:187], a[88:91], v[96:99], v[184:187]\n"
    "ds_read_b128 a[240:243], v16 offset:12288\n"
    "ds_read_b128 a[244:247], v17 offset:12288\n"
    "v_mfma_f32_16x16x32_bf16 v[184:187], a[92:95], v[104:107], v[184:187]\n"
    "v_exp_f32 v119, v119\n"
    "v_mfma_f32_16x16x32_bf16 v[200:203], a[96:99], v[96:99], v[200:203]\n"
    "ds_read_b128 a[248:251], v18 offset:12288\n"
    "v_cvt_pk_bf16_f32 v114, v116, v117\n"
    "v_mfma_f32_16x16x32_bf16 v[200:203], a[100:103], v[104:107], v[200:203]\n"
    "ds_read_b128 a[252:255], v19 offset:12288\n"
    "v_cvt_pk_bf16_f32 v115, v118, v119\n"
    "v_mfma_f32_16x16x32_bf16 v[216:219], a[104:107], v[96:99], v[216:219]\n"
    "v_exp_f32 v120, v120\n"
    "v_mfma_f32_16x16x32_bf16 v[216:219], a[108:111], v[104:107], v[216:219]\n"
    "s_add_u32 m0, s81, 81920\n"
    "v_mfma_f32_16x16x32_bf16 v[232:235], a[112:115], v[96:99], v[232:235]\n"
    "buffer_load_dwordx4 v7, s[64:67], s80 offen offset:2048 sc1 lds\n"
    "v_mfma_f32_16x16x32_bf16 v[232:235], a[116:119], v[104:107], v[232:235]\n"
    "v_exp_f32 v121, v121\n"
    "v_mfma_f32_16x16x32_bf16 v[248:251], a[120:123], v[96:99], v[248:251]\n"
    "v_exp_f32 v122, v122\n"
    "v_mfma_f32_16x16x32_bf16 v[248:251], a[124:127], v[104:107], v[248:251]\n"
    "v_exp_f32 v123, v123\n"
    "v_mfma_f32_16x16x32_bf16 v[72:75], v[60:63], v[96:99], v[72:75]\n"
    "v_cvt_pk_bf16_f32 v120, v120, v121\n"
    "v_cvt_pk_bf16_f32 v121, v122, v123\n"
    "v_mfma_f32_16x16x32_bf16 v[72:75], v[60:63], v[104:107], v[72:75]\n"
    "s_waitcnt lgkmcnt(0)\n"
    "v_exp_f32 v124, v124\n"
    "v_mfma_f32_16x16x32_bf16 v[96:99], a[192:195], a[128:131], v[80:83]\n"
    "ds_read_b64_tr_b16 a[44:45], v21 offset:9216\n"
    "v_mfma_f32_16x16x32_bf16 v[96:99], a[196:199], a[132:135], v[96:99]\n"
    "v_exp_f32 v125, v125\n"
    "v_mfma_f32_16x16x32_bf16 v[96:99], a[200:203], a[136:139], v[96:99]\n"
    "v_exp_f32 v126, v126\n"
    "v_mfma_f32_16x16x32_bf16 v[96:99], a[204:207], a[140:143], v[96:99]\n"
    "s_add_u32 m0, s81, 82816\n"
    "ds_read_b64_tr_b16 a[46:47], v21 offset:13312\n"
    "v_mfma_f32_16x16x32_bf16 v[100:103], a[208:211], a[128:131], v[80:83]\n"
    "buffer_load_dwordx4 v9, s[64:67], s80 offen offset:2176 sc1 lds\n"
    "v_mfma_f32_16x16x32_bf16 v[100:103], a[212:215], a[132:135], v[100:103]\n"
    "v_exp_f32 v127, v127\n"
    "v_mfma_f32_16x16x32_bf16 v[100:103], a[216:219], a[136:139], v[100:103]\n"
    "s_add_u32 s80, s80, 16384\n"
    "ds_read_b64_tr_b16 a[48:49], v20 offset:1536\n"
    "v_mfma_f32_16x16x32_bf16 v[100:103], a[220:223], a[140:143], v[100:103]\n"
    "v_cvt_pk_bf16_f32 v122, v124, v125\n"
    "v_cvt_pk_bf16_f32 v123, v126, v127\n"
    "v_mfma_f32_16x16x32_bf16 v[104:107], a[224:227], a[128:131], v[80:83]\n"
    "v_exp_f32 v96, v96\n"
    "v_mfma_f32_16x16x32_bf16 v[104:107], a[228:231], a[132:135], v[104:107]\n"
    "ds_read_b64_tr_b16 a[50:51], v20 offset:5632\n"
    "v_mfma_f32_16x16x32_bf16 v[104:107], a[232:235], a[136:139], v[104:107]\n"
    "v_exp_f32 v97, v97\n"
    "v_mfma_f32_16x16x32_bf16 v[104:107], a[236:239], a[140:143], v[104:107]\n"
    "v_exp_f32 v98, v98\n"
    "v_mfma_f32_16x16x32_bf16 v[108:111], a[240:243], a[128:131], v[80:83]\n"
    "ds_read_b64_tr_b16 a[52:53], v20 offset:9728\n"
    "v_mfma_f32_16x16x32_bf16 v[108:111], a[244:247], a[132:135], v[108:111]\n"
    "v_exp_f32 v99, v99\n"
    "v_mfma_f32_16x16x32_bf16 v[108:111], a[248:251], a[136:139], v[108:111]\n"
    "s_waitcnt vmcnt(18)\n"
    "buffer_store_dwordx4 v[28:31], v2, s[48:51], s93 offen offset:1024 sc1\n"
    "v_mfma_f32_16x16x32_bf16 v[108:111], a[252:255], a[140:143], v[108:111]\n"
    "v_cvt_pk_bf16_f32 v96, v96, v97\n"
    "ds_read_b64_tr_b16 a[54:55], v20 offset:13824\n"
    "v_mfma_f32_16x16x32_bf16 v[140:143], a[64:67], v[112:115], v[140:143]\n"
    "v_cvt_pk_bf16_f32 v97, v98, v99\n"
    "v_mfma_f32_16x16x32_bf16 v[140:143], a[68:71], v[120:123], v[140:143]\n"
    "v_exp_f32 v100, v100\n"
    "v_mfma_f32_16x16x32_bf16 v[156:159], a[72:75], v[112:115], v[156:159]\n"
    "ds_read_b64_tr_b16 a[56:57], v21 offset:1536\n"
    "v_mfma_f32_16x16x32_bf16 v[156:159], a[76:79], v[120:123], v[156:159]\n"
    "v_exp_f32 v101, v101\n"
    "v_mfma_f32_16x16x32_bf16 v[172:175], a[80:83], v[112:115], v[172:175]\n"
    "v_exp_f32 v102, v102\n"
    "v_mfma_f32_16x16x32_bf16 v[172:175], a[84:87], v[120:123], v[172:175]\n"
    "ds_read_b64_tr_b16 a[58:59], v21 offset:5632\n"
    "v_mfma_f32_16x16x32_bf16 v[188:191], a[88:91], v[112:115], v[188:191]\n"
    "buffer_load_dwordx4 v[28:31], v8, s[44:47], s92 offen offset:128\n"
    "v_mfma_f32_16x16x32_bf16 v[188:191], a[92:95], v[120:123], v[188:191]\n"
    "v_exp_f32 v103, v103\n"
    "v_mfma_f32_16x16x32_bf16 v[204:207], a[96:99], v[112:115], v[204:207]\n"
    "s_add_u32 s92, s92, 16384\n"
    "ds_read_b64_tr_b16 a[60:61], v21 offset:9728\n"
    "v_mfma_f32_16x16x32_bf16 v[204:207], a[100:103], v[120:123], v[204:207]\n"
    "v_cvt_pk_bf16_f32 v98, v100, v101\n"
    "v_cvt_pk_bf16_f32 v99, v102, v103\n"
    "v_mfma_f32_16x16x32_bf16 v[220:223], a[104:107], v[112:115], v[220:223]\n"
    "s_add_u32 s93, s93, 2048\n"
    "v_mfma_f32_16x16x32_bf16 v[220:223], a[108:111], v[120:123], v[220:223]\n"
    "v_exp_f32 v104, v104\n"
    "v_mfma_f32_16x16x32_bf16 v[236:239], a[112:115], v[112:115], v[236:239]\n"
    "ds_read_b64_tr_b16 a[62:63], v21 offset:13824\n"
    "s_xor_b32 s81, s81, 0x8000\n"
    "v_mfma_f32_16x16x32_bf16 v[236:239], a[116:119], v[120:123], v[236:239]\n"
    "v_exp_f32 v105, v105\n"
    "v_mfma_f32_16x16x32_bf16 v[252:255], a[120:123], v[112:115], v[252:255]\n"
    "s_bitcmp1_b32 s79, s43\n"
    "s_cbranch_scc1 .Lk2_ev\n"
    ".Lk2_ev_ret:\n"
    "v_exp_f32 v106, v106\n"
    "v_mfma_f32_16x16x32_bf16 v[252:255], a[124:127], v[120:123], v[252:255]\n"
    "v_exp_f32 v107, v107\n"
    "v_mfma_f32_16x16x32_bf16 v[76:79], v[60:63], v[112:115], v[76:79]\n"
    "s_add_u32 s43, s43, 1\n"
    "s_cmp_lt_u32 s43, 15\n"
    "v_cvt_pk_bf16_f32 v104, v104, v105\n"
    "v_cvt_pk_bf16_f32 v105, v106, v107\n"
    "v_mfma_f32_16x16x32_bf16 v[76:79], v[60:63], v[120:123], v[76:79]\n"
    "v_exp_f32 v108, v108\n"
    "s_cbranch_scc1 .Lk2_att\n"
    ".Lk2_oepi:\n"
    "v_mfma_f32_16x16x32_bf16 v[112:115], a[192:195], a[144:147], v[84:87]\n"
    "s_waitcnt vmcnt(20) lgkmcnt(0)\n"
    "s_barrier\n"
    "v_mfma_f32_16x16x32_bf16 v[112:115], a[196:199], a[148:151], v[112:115]\n"
    "v_exp_f32 v109, v109\n"
    "v_mfma_f32_16x16x32_bf16 v[112:115], a[200:203], a[152:155], v[112:115]\n"
    "v_exp_f32 v110, v110\n"
    "v_mfma_f32_16x16x32_bf16 v[112:115], a[204:207], a[156:159], v[112:115]\n"
    "s_add_u32 m0, s81, 0\n"
    "ds_read_b64_tr_b16 a[64:65], v20 offset:16384\n"
    "v_mfma_f32_16x16x32_bf16 v[116:119], a[208:211], a[144:147], v[84:87]\n"
    "buffer_load_dwordx4 v3, s[60:63], s80 offen offset:0 sc1 lds\n"
    "v_mfma_f32_16x16x32_bf16 v[116:119], a[212:215], a[148:151], v[116:119]\n"
    "v_exp_f32 v111, v111\n"
    "v_mfma_f32_16x16x32_bf16 v[116:119], a[216:219], a[152:155], v[116:119]\n"
    "ds_read_b64_tr_b16 a[66:67], v20 offset:20480\n"
    "v_cvt_pk_bf16_f32 v106, v108, v109\n"
    "v_mfma_f32_16x16x32_bf16 v[116:119], a[220:223], a[156:159], v[116:119]\n"
    "v_cvt_pk_bf16_f32 v107, v110, v111\n"
    "ds_read_b64_tr_b16 a[68:69], v20 offset:24576\n"
    "v_mfma_f32_16x16x32_bf16 v[120:123], a[224:227], a[144:147], v[84:87]\n"
    "v_exp_f32 v112, v112\n"
    "v_mfma_f32_16x16x32_bf16 v[120:123], a[228:231], a[148:151], v[120:123]\n"
    "v_exp_f32 v113, v113\n"
    "v_mfma_f32_16x16x32_bf16 v[120:123], a[232:235], a[152:155], v[120:123]\n"
    "ds_read_b64_tr_b16 a[70:71], v20 offset:28672\n"
    "v_mfma_f32_16x16x32_bf16 v[120:123], a[236:239], a[156:159], v[120:123]\n"
    "v_exp_f32 v114, v114\n"
    "v_mfma_f32_16x16x32_bf16 v[124:127], a[240:243], a[144:147], v[84:87]\n"
    "v_mfma_f32_16x16x32_bf16 v[124:127], a[244:247], a[148:151], v[124:127]\n"
    "v_exp_f32 v115, v115\n"
    "v_mfma_f32_16x16x32_bf16 v[124:127], a[248:251], a[152:155], v[124:127]\n"
    "buffer_load_dwordx4 v4, s[60:63], s80 offen offset:1024 sc1 lds\n"
    "v_mfma_f32_16x16x32_bf16 v[124:127], a[252:255], a[156:159], v[124:127]\n"
    "ds_read_b64_tr_b16 a[72:73], v21 offset:16384\n"
    "v_cvt_pk_bf16_f32 v112, v112, v113\n"
    "v_mfma_f32_16x16x32_bf16 v[128:131], a[0:3], v[96:99], v[128:131]\n"
    "v_cvt_pk_bf16_f32 v113, v114, v115\n"
    "v_mfma_f32_16x16x32_bf16 v[128:131], a[4:7], v[104:107], v[128:131]\n"
    "v_exp_f32 v116, v116\n"
    "v_mfma_f32_16x16x32_bf16 v[144:147], a[8:11], v[96:99], v[144:147]\n"
    "ds_read_b64_tr_b16 a[74:75], v21 offset:20480\n"
    "v_mfma_f32_16x16x32_bf16 v[144:147], a[12:15], v[104:107], v[144:147]\n"
    "v_exp_f32 v117, v117\n"
    "v_mfma_f32_16x16x32_bf16 v[160:163], a[16:19], v[96:99], v[160:163]\n"
    "v_exp_f32 v118, v118\n"
    "v_mfma_f32_16x16x32_bf16 v[160:163], a[20:23], v[104:107], v[160:163]\n"
    "ds_read_b64_tr_b16 a[76:77], v21 offset:24576\n"
    "v_mfma_f32_16x16x32_bf16 v[176:179], a[24:27], v[96:99], v[176:179]\n"
    "v_exp_f32 v119, v119\n"
    "v_mfma_f32_16x16x32_bf16 v[176:179], a[28:31], v[104:107], v[176:179]\n"
    "ds_read_b64_tr_b16 a[78:79], v21 offset:28672\n"
    "v_cvt_pk_bf16_f32 v114, v116, v117\n"
    "v_mfma_f32_16x16x32_bf16 v[192:195], a[32:35], v[96:99], v[192:195]\n"
    "buffer_load_dwordx4 v5, s[60:63], s80 offen offset:2048 sc1 lds\n"
    "v_mfma_f32_16x16x32_bf16 v[192:195], a[36:39], v[104:107], v[192:195]\n"
    "v_cvt_pk_bf16_f32 v115, v118, v119\n"
    "v_mfma_f32_16x16x32_bf16 v[208:211], a[40:43], v[96:99], v[208:211]\n"
    "v_exp_f32 v120, v120\n"
    "v_mfma_f32_16x16x32_bf16 v[208:211], a[44:47], v[104:107], v[208:211]\n"
    "ds_read_b64_tr_b16 a[80:81], v20 offset:16896\n"
    "v_mfma_f32_16x16x32_bf16 v[224:227], a[48:51], v[96:99], v[224:227]\n"
    "v_exp_f32 v121, v121\n"
    "v_mfma_f32_16x16x32_bf16 v[224:227], a[52:55], v[104:107], v[224:227]\n"
    "v_exp_f32 v122, v122\n"
    "v_mfma_f32_16x16x32_bf16 v[240:243], a[56:59], v[96:99], v[240:243]\n"
    "ds_read_b64_tr_b16 a[82:83], v20 offset:20992\n"
    "v_mfma_f32_16x16x32_bf16 v[240:243], a[60:63], v[104:107], v[240:243]\n"
    "v_exp_f32 v123, v123\n"
    "v_mfma_f32_16x16x32_bf16 v[64:67], v[60:63], v[96:99], v[64:67]\n"
    "v_cvt_pk_bf16_f32 v120, v120, v121\n"
    "ds_read_b64_tr_b16 a[84:85], v20 offset:25088\n"
    "v_mfma_f32_16x16x32_bf16 v[64:67], v[60:63], v[104:107], v[64:67]\n"
    "v_cvt_pk_bf16_f32 v121, v122, v123\n"
    "v_mfma_f32_16x16x32_bf16 v[96:99], a[192:195], a[160:163], v[88:91]\n"
    "v_exp_f32 v124, v124\n"
    "v_mfma_f32_16x16x32_bf16 v[96:99], a[196:199], a[164:167], v[96:99]\n"
    "v_exp_f32 v125, v125\n"
    "v_mfma_f32_16x16x32_bf16 v[96:99], a[200:203], a[168:171], v[96:99]\n"
    "ds_read_b64_tr_b16 a[86:87], v20 offset:29184\n"
    "v_mfma_f32_16x16x32_bf16 v[96:99], a[204:207], a[172:175], v[96:99]\n"
    "v_exp_f32 v126, v126\n"
    "v_mfma_f32_16x16x32_bf16 v[100:103], a[208:211], a[160:163], v[88:91]\n"
    "buffer_load_dwordx4 v6, s[60:63], s80 offen offset:3072 sc1 lds\n"
    "v_mfma_f32_16x16x32_bf16 v[100:103], a[212:215], a[164:167], v[100:103]\n"
    "ds_read_b64_tr_b16 a[88:89], v21 offset:16896\n"
    "v_mfma_f32_16x16x32_bf16 v[100:103], a[216:219], a[168:171], v[100:103]\n"
    "v_exp_f32 v127, v127\n"
    "v_mfma_f32_16x16x32_bf16 v[100:103], a[220:223], a[172:175], v[100:103]\n"
    "v_cvt_pk_bf16_f32 v122, v124, v125\n"
    "ds_read_b64_tr_b16 a[90:91], v21 offset:20992\n"
    "v_mfma_f32_16x16x32_bf16 v[104:107], a[224:227], a[160:163], v[88:91]\n"
    "v_cvt_pk_bf16_f32 v123, v126, v127\n"
    "v_mfma_f32_16x16x32_bf16 v[104:107], a[228:231], a[164:167], v[104:107]\n"
    "v_exp_f32 v96, v96\n"
    "v_mfma_f32_16x16x32_bf16 v[104:107], a[232:235], a[168:171], v[104:107]\n"
    "v_exp_f32 v97, v97\n"
    "v_mfma_f32_16x16x32_bf16 v[104:107], a[236:239], a[172:175], v[104:107]\n"
    "ds_read_b64_tr_b16 a[92:93], v21 offset:25088\n"
    "v_mfma_f32_16x16x32_bf16 v[108:111], a[240:243], a[160:163], v[88:91]\n"
    "v_exp_f32 v98, v98\n"
    "v_mfma_f32_16x16x32_bf16 v[108:111], a[244:247], a[164:167], v[108:111]\n"
    "ds_read_b64_tr_b16 a[94:95], v21 offset:29184\n"
    "v_mfma_f32_16x16x32_bf16 v[108:111], a[248:251], a[168:171], v[108:111]\n"
    "v_exp_f32 v99, v99\n"
    "v_mfma_f32_16x16x32_bf16 v[108:111], a[252:255], a[172:175], v[108:111]\n"
    "v_cvt_pk_bf16_f32 v96, v96, v97\n"
    "v_cvt_pk_bf16_f32 v97, v98, v99\n"
    "v_mfma_f32_16x16x32_bf16 v[132:135], a[0:3], v[112:115], v[132:135]\n"
    "ds_read_b64_tr_b16 a[96:97], v20 offset:17408\n"
    "v_mfma_f32_16x16x32_bf16 v[132:135], a[4:7], v[120:123], v[132:135]\n"
    "v_exp_f32 v100, v100\n"
    "v_mfma_f32_16x16x32_bf16 v[148:151], a[8:11], v[112:115], v[148:151]\n"
    "v_exp_f32 v101, v101\n"
    "v_mfma_f32_16x16x32_bf16 v[148:151], a[12:15], v[120:123], v[148:151]\n"
    "ds_read_b64_tr_b16 a[98:99], v20 offset:21504\n"
    "v_mfma_f32_16x16x32_bf16 v[164:167], a[16:19], v[112:115], v[164:167]\n"
    "v_exp_f32 v102, v102\n"
    "v_mfma_f32_16x16x32_bf16 v[164:167], a[20:23], v[120:123], v[164:167]\n"
    "v_exp_f32 v103, v103\n"
    "v_mfma_f32_16x16x32_bf16 v[180:183], a[24:27], v[112:115], v[180:183]\n"
    "ds_read_b64_tr_b16 a[100:101], v20 offset:25600\n"
    "v_mfma_f32_16x16x32_bf16 v[180:183], a[28:31], v[120:123], v[180:183]\n"
    "v_cvt_pk_bf16_f32 v98, v100, v101\n"
    "v_mfma_f32_16x16x32_bf16 v[196:199], a[32:35], v[112:115], v[196:199]\n"
    "v_cvt_pk_bf16_f32 v99, v102, v103\n"
    "ds_read_b64_tr_b16 a[102:103], v20 offset:29696\n"
    "v_mfma_f32_16x16x32_bf16 v[196:199], a[36:39], v[120:123], v[196:199]\n"
    "v_exp_f32 v104, v104\n"
    "v_mfma_f32_16x16x32_bf16 v[212:215], a[40:43], v[112:115], v[212:215]\n"
    "v_exp_f32 v105, v105\n"
    "v_mfma_f32_16x16x32_bf16 v[212:215], a[44:47], v[120:123], v[212:215]\n"
    "ds_read_b64_tr_b16 a[104:105], v21 offset:17408\n"
    "v_mfma_f32_16x16x32_bf16 v[228:231], a[48:51], v[112:115], v[228:231]\n"
    "v_exp_f32 v106, v106\n"
    "v_mfma_f32_16x16x32_bf16 v[228:231], a[52:55], v[120:123], v[228:231]\n"
    "v_mfma_f32_16x16x32_bf16 v[244:247], a[56:59], v[112:115], v[244:247]\n"
    "v_exp_f32 v107, v107\n"
    "v_mfma_f32_16x16x32_bf16 v[244:247], a[60:63], v[120:123], v[244:247]\n"
    "ds_read_b64_tr_b16 a[106:107], v21 offset:21504\n"
    "v_cvt_pk_bf16_f32 v104, v104, v105\n"
    "v_mfma_f32_16x16x32_bf16 v[68:71], v[60:63], v[112:115], v[68:71]\n"
    "v_cvt_pk_bf16_f32 v105, v106, v107\n"
    "v_mfma_f32_16x16x32_bf16 v[68:71], v[60:63], v[120:123], v[68:71]\n"
    "v_exp_f32 v108, v108\n"
    "v_mfma_f32_16x16x32_bf16 v[112:115], a[192:195], a[176:179], v[92:95]\n"
    "v_mfma_f32_16x16x32_bf16 v[112:115], a[196:199], a[180:183], v[112:115]\n"
    "v_exp_f32 v109, v109\n"
    "v_mfma_f32_16x16x32_bf16 v[112:115], a[200:203], a[184:187], v[112:115]\n"
    "ds_read_b128 a[192:195], v16 offset:16384\n"
    "v_mfma_f32_16x16x32_bf16 v[112:115], a[204:207], a[188:191], v[112:115]\n"
    "v_exp_f32 v110, v110\n"
    "v_mfma_f32_16x16x32_bf16 v[116:119], a[208:211], a[176:179], v[92:95]\n"
    "ds_read_b128 a[196:199], v17 offset:16384\n"
    "v_mfma_f32_16x16x32_bf16 v[116:119], a[212:215], a[180:183], v[116:119]\n"
    "v_exp_f32 v111, v111\n"
    "v_mfma_f32_16x16x32_bf16 v[116:119], a[216:219], a[184:187], v[116:119]\n"
    "ds_read_b128 a[200:203], v18 offset:16384\n"
    "ds_read_b128 a[204:207], v19 offset:16384\n"
    "v_mfma_f32_16x16x32_bf16 v[116:119], a[220:223], a[188:191], v[116:119]\n"
    "v_cvt_pk_bf16_f32 v106, v108, v109\n"
    "v_cvt_pk_bf16_f32 v107, v110, v111\n"
    "v_mfma_f32_16x16x32_bf16 v[120:123], a[224:227], a[176:179], v[92:95]\n"
    "ds_read_b128 a[208:211], v16 offset:20480\n"
    "v_mfma_f32_16x16x32_bf16 v[120:123], a[228:231], a[180:183], v[120:123]\n"
    "v_exp_f32 v112, v112\n"
    "v_mfma_f32_16x16x32_bf16 v[120:123], a[232:235], a[184:187], v[120:123]\n"
    "ds_read_b128 a[212:215], v17 offset:20480\n"
    "v_mfma_f32_16x16x32_bf16 v[120:123], a[236:239], a[188:191], v[120:123]\n"
    "v_exp_f32 v113, v113\n"
    "v_mfma_f32_16x16x32_bf16 v[124:127], a[240:243], a[176:179], v[92:95]\n"
    "ds_read_b128 a[216:219], v18 offset:20480\n"
    "v_mfma_f32_16x16x32_bf16 v[124:127], a[244:247], a[180:183], v[124:127]\n"
    "v_exp_f32 v114, v114\n"
    "v_mfma_f32_16x16x32_bf16 v[124:127], a[248:251], a[184:187], v[124:127]\n"
    "ds_read_b128 a[220:223], v19 offset:20480\n"
    "v_mfma_f32_16x16x32_bf16 v[124:127], a[252:255], a[188:191], v[124:127]\n"
    "v_exp_f32 v115, v115\n"
    "v_mfma_f32_16x16x32_bf16 v[136:139], a[0:3], v[96:99], v[136:139]\n"
    "ds_read_b128 a[224:227], v16 offset:24576\n"
    "v_cvt_pk_bf16_f32 v112, v112, v113\n"
    "v_mfma_f32_16x16x32_bf16 v[136:139], a[4:7], v[104:107], v[136:139]\n"
    "ds_read_b128 a[228:231], v17 offset:24576\n"
    "v_cvt_pk_bf16_f32 v113, v114, v115\n"
    "v_mfma_f32_16x16x32_bf16 v[152:155], a[8:11], v[96:99], v[152:155]\n"
    "v_exp_f32 v116, v116\n"
    "v_mfma_f32_16x16x32_bf16 v[152:155], a[12:15], v[104:107], v[152:155]\n"
    "ds_read_b128 a[232:235], v18 offset:24576\n"
    "ds_read_b128 a[236:239], v19 offset:24576\n"
    "v_mfma_f32_16x16x32_bf16 v[168:171], a[16:19], v[96:99], v[168:171]\n"
    "v_exp_f32 v117, v117\n"
    "v_mfma_f32_16x16x32_bf16 v[168:171], a[20:23], v[104:107], v[168:171]\n"
    "v_exp_f32 v118, v118\n"
    "v_mfma_f32_16x16x32_bf16 v[184:187], a[24:27], v[96:99], v[184:187]\n"
    "ds_read_b128 a[240:243], v16 offset:28672\n"
    "ds_read_b128 a[244:247], v17 offset:28672\n"
    "v_mfma_f32_16x16x32_bf16 v[184:187], a[28:31], v[104:107], v[184:187]\n"
    "v_exp_f32 v119, v119\n"
    "v_mfma_f32_16x16x32_bf16 v[200:203], a[32:35], v[96:99], v[200:203]\n"
    "ds_read_b128 a[248:251], v18 offset:28672\n"
    "v_cvt_pk_bf16_f32 v114, v116, v117\n"
    "v_mfma_f32_16x16x32_bf16 v[200:203], a[36:39], v[104:107], v[200:203]\n"
    "ds_read_b128 a[252:255], v19 offset:28672\n"
    "v_cvt_pk_bf16_f32 v115, v118, v119\n"
    "v_mfma_f32_16x16x32_bf16 v[216:219], a[40:43], v[96:99], v[216:219]\n"
    "v_exp_f32 v120, v120\n"
    "v_mfma_f32_16x16x32_bf16 v[216:219], a[44:47], v[104:107], v[216:219]\n"
    "v_exp_f32 v121, v121\n"
    "v_mfma_f32_16x16x32_bf16 v[232:235], a[48:51], v[96:99], v[232:235]\n"
    "v_exp_f32 v122, v122\n"
    "v_mfma_f32_16x16x32_bf16 v[232:235], a[52:55], v[104:107], v[232:235]\n"
    "v_mfma_f32_16x16x32_bf16 v[248:251], a[56:59], v[96:99], v[248:251]\n"
    "v_exp_f32 v123, v123\n"
    "v_mfma_f32_16x16x32_bf16 v[248:251], a[60:63], v[104:107], v[248:251]\n"
    "v_cvt_pk_bf16_f32 v120, v120, v121\n"
    "v_mfma_f32_16x16x32_bf16 v[72:75], v[60:63], v[96:99], v[72:75]\n"
    "v_cvt_pk_bf16_f32 v121, v122, v123\n"
    "v_mfma_f32_16x16x32_bf16 v[72:75], v[60:63], v[104:107], v[72:75]\n"
    "s_waitcnt lgkmcnt(0)\n"
    "v_exp_f32 v124, v124\n"
    "v_mfma_f32_16x16x32_bf16 v[96:99], a[192:195], a[128:131], v[80:83]\n"
    "ds_read_b64_tr_b16 a[108:109], v21 offset:25600\n"
    "v_mfma_f32_16x16x32_bf16 v[96:99], a[196:199], a[132:135], v[96:99]\n"
    "v_exp_f32 v125, v125\n"
    "v_mfma_f32_16x16x32_bf16 v[96:99], a[200:203], a[136:139], v[96:99]\n"
    "v_exp_f32 v126, v126\n"
    "v_mfma_f32_16x16x32_bf16 v[96:99], a[204:207], a[140:143], v[96:99]\n"
    "ds_read_b64_tr_b16 a[110:111], v21 offset:29696\n"
    "v_mfma_f32_16x16x32_bf16 v[100:103], a[208:211], a[128:131], v[80:83]\n"
    "v_exp_f32 v127, v127\n"
    "v_mfma_f32_16x16x32_bf16 v[100:103], a[212:215], a[132:135], v[100:103]\n"
    "ds_read_b64_tr_b16 a[112:113], v20 offset:17920\n"
    "v_mfma_f32_16x16x32_bf16 v[100:103], a[216:219], a[136:139], v[100:103]\n"
    "s_add_u32 s80, s80, 16384\n"
    "v_cvt_pk_bf16_f32 v122, v124, v125\n"
    "v_mfma_f32_16x16x32_bf16 v[100:103], a[220:223], a[140:143], v[100:103]\n"
    "v_cvt_pk_bf16_f32 v123, v126, v127\n"
    "v_mfma_f32_16x16x32_bf16 v[104:107], a[224:227], a[128:131], v[80:83]\n"
    "v_exp_f32 v96, v96\n"
    "v_mfma_f32_16x16x32_bf16 v[104:107], a[228:231], a[132:135], v[104:107]\n"
    "ds_read_b64_tr_b16 a[114:115], v20 offset:22016\n"
    "v_mfma_f32_16x16x32_bf16 v[104:107], a[232:235], a[136:139], v[104:107]\n"
    "v_exp_f32 v97, v97\n"
    "v_mfma_f32_16x16x32_bf16 v[104:107], a[236:239], a[140:143], v[104:107]\n"
    "v_exp_f32 v98, v98\n"
    "v_mfma_f32_16x16x32_bf16 v[108:111], a[240:243], a[128:131], v[80:83]\n"
    "ds_read_b64_tr_b16 a[116:117], v20 offset:26112\n"
    "v_mfma_f32_16x16x32_bf16 v[108:111], a[244:247], a[132:135], v[108:111]\n"
    "v_exp_f32 v99, v99\n"
    "v_mfma_f32_16x16x32_bf16 v[108:111], a[248:251], a[136:139], v[108:111]\n"
    "v_cvt_pk_bf16_f32 v96, v96, v97\n"
    "ds_read_b64_tr_b16 a[118:119], v20 offset:30208\n"
    "v_mfma_f32_16x16x32_bf16 v[108:111], a[252:255], a[140:143], v[108:111]\n"
    "v_cvt_pk_bf16_f32 v97, v98, v99\n"
    "v_mfma_f32_16x16x32_bf16 v[140:143], a[0:3], v[112:115], v[140:143]\n"
    "v_exp_f32 v100, v100\n"
    "v_mfma_f32_16x16x32_bf16 v[140:143], a[4:7], v[120:123], v[140:143]\n"
    "ds_read_b64_tr_b16 a[120:121], v21 offset:17920\n"
    "v_mfma_f32_16x16x32_bf16 v[156:159], a[8:11], v[112:115], v[156:159]\n"
    "v_exp_f32 v101, v101\n"
    "v_mfma_f32_16x16x32_bf16 v[156:159], a[12:15], v[120:123], v[156:159]\n"
    "v_exp_f32 v102, v102\n"
    "v_mfma_f32_16x16x32_bf16 v[172:175], a[16:19], v[112:115], v[172:175]\n"
    "ds_read_b64_tr_b16 a[122:123], v21 offset:22016\n"
    "v_mfma_f32_16x16x32_bf16 v[172:175], a[20:23], v[120:123], v[172:175]\n"
    "v_exp_f32 v103, v103\n"
    "v_mfma_f32_16x16x32_bf16 v[188:191], a[24:27], v[112:115], v[188:191]\n"
    "ds_read_b64_tr_b16 a[124:125], v21 offset:26112\n"
    "v_mfma_f32_16x16x32_bf16 v[188:191], a[28:31], v[120:123], v[188:191]\n"
    "v_cvt_pk_bf16_f32 v98, v100, v101\n"
    "v_mfma_f32_16x16x32_bf16 v[204:207], a[32:35], v[112:115], v[204:207]\n"
    "v_cvt_pk_bf16_f32 v99, v102, v103\n"
    "v_mfma_f32_16x16x32_bf16 v[204:207], a[36:39], v[120:123], v[204:207]\n"
    "v_exp_f32 v104, v104\n"
    "v_mfma_f32_16x16x32_bf16 v[220:223], a[40:43], v[112:115], v[220:223]\n"
    "ds_read_b64_tr_b16 a[126:127], v21 offset:30208\n"
    "v_mfma_f32_16x16x32_bf16 v[220:223], a[44:47], v[120:123], v[220:223]\n"
    "v_exp_f32 v105, v105\n"
    "v_mfma_f32_16x16x32_bf16 v[236:239], a[48:51], v[112:115], v[236:239]\n"
    "v_exp_f32 v106, v106\n"
    "v_mfma_f32_16x16x32_bf16 v[236:239], a[52:55], v[120:123], v[236:239]\n"
    "v_mfma_f32_16x16x32_bf16 v[252:255], a[56:59], v[112:115], v[252:255]\n"
    "v_exp_f32 v107, v107\n"
    "v_mfma_f32_16x16x32_bf16 v[252:255], a[60:63], v[120:123], v[252:255]\n"
    "v_cvt_pk_bf16_f32 v104, v104, v105\n"
    "v_mfma_f32_16x16x32_bf16 v[76:79], v[60:63], v[112:115], v[76:79]\n"
    "v_cvt_pk_bf16_f32 v105, v106, v107\n"
    "v_mfma_f32_16x16x32_bf16 v[76:79], v[60:63], v[120:123], v[76:79]\n"
    "v_exp_f32 v108, v108\n"
    "v_mfma_f32_16x16x32_bf16 v[112:115], a[192:195], a[144:147], v[84:87]\n"
    "s_waitcnt lgkmcnt(0)\n"
    "s_barrier\n"
    "v_mfma_f32_16x16x32_bf16 v[112:115], a[196:199], a[148:151], v[112:115]\n"
    "v_exp_f32 v109, v109\n"
    "v_mfma_f32_16x16x32_bf16 v[112:115], a[200:203], a[152:155], v[112:115]\n"
    "s_and_b32 s79, s36, 1\n"
    "s_lshl_b32 s79, s79, 2\n"
    "s_bfe_u32 s85, s36, 0x20003\n"
    "v_mfma_f32_16x16x32_bf16 v[112:115], a[204:207], a[156:159], v[112:115]\n"
    "s_add_u32 m0, s81, 16384\n"
    "s_add_u32 s85, s85, s79\n"
    "s_bfe_u32 s84, s36, 0x20001\n"
    "v_mfma_f32_16x16x32_bf16 v[116:119], a[208:211], a[144:147], v[84:87]\n"
    "buffer_load_dwordx4 v3, s[60:63], s80 offen offset:0 sc1 lds\n"
    "s_lshl_b32 s66, s85, 2\n"
    "s_add_u32 s66, s66, s40\n"
    "s_lshl_b32 s65, s66, 3\n"
    "v_mfma_f32_16x16x32_bf16 v[116:119], a[212:215], a[148:151], v[116:119]\n"
    "v_exp_f32 v110, v110\n"
    "s_add_u32 s65, s65, s84\n"
    "s_mul_i32 s82, s65, 0xe000\n"
    "v_mfma_f32_16x16x32_bf16 v[116:119], a[216:219], a[152:155], v[116:119]\n"
    "s_add_u32 s82, s82, 0x1400000\n"
    "s_add_u32 s44, s34, s82\n"
    "s_addc_u32 s45, s35, 0\n"
    "s_and_b32 s45, s45, 0xffff\n"
    "v_mfma_f32_16x16x32_bf16 v[116:119], a[220:223], a[156:159], v[116:119]\n"
    "v_exp_f32 v111, v111\n"
    "s_mov_b32 s46, 0x46000\n"
    "s_mov_b32 s47, 0x20000\n"
    "v_mfma_f32_16x16x32_bf16 v[120:123], a[224:227], a[144:147], v[84:87]\n"
    "s_mov_b32 s90, 0x0\n"
    "s_mov_b32 s91, 0x1000\n"
    "s_mov_b32 s92, 0x38000\n"
    "v_mfma_f32_16x16x32_bf16 v[120:123], a[228:231], a[148:151], v[120:123]\n"
    "v_cvt_pk_bf16_f32 v106, v108, v109\n"
    "s_mov_b32 s93, 0x39000\n"
    "s_lshl_b32 s76, s41, 13\n"
    "v_mfma_f32_16x16x32_bf16 v[120:123], a[232:235], a[152:155], v[120:123]\n"
    "v_cvt_pk_bf16_f32 v107, v110, v111\n"
    "s_add_u32 s77, s76, 0x40000\n"
    "s_add_u32 s86, s76, 4096\n"
    "s_add_u32 s87, s77, 4096\n"
    "v_mfma_f32_16x16x32_bf16 v[120:123], a[236:239], a[156:159], v[120:123]\n"
    "v_exp_f32 v112, v112\n"
    "s_mov_b32 s89, 0x5f800000\n"
    "s_cmp_lt_u32 s84, 2\n"
    "s_cselect_b32 s37, 8, 8\n"
    "s_cselect_b32 s79, 0, 0\n"
    "v_mfma_f32_16x16x32_bf16 v[124:127], a[240:243], a[144:147], v[84:87]\n"
    "v_exp_f32 v113, v113\n"
    "s_mul_i32 s82, s84, s37\n"
    "s_add_u32 s79, s79, s82\n"
    "s_lshl_b32 s82, s66, 6\n"
    "v_mfma_f32_16x16x32_bf16 v[124:127], a[244:247], a[148:151], v[124:127]\n"
    "s_add_u32 s82, s82, s79\n"
    "s_lshl_b32 s82, s82, 13\n"
    "v_mfma_f32_16x16x32_bf16 v[124:127], a[248:251], a[152:155], v[124:127]\n"
    "buffer_load_dwordx4 v4, s[60:63], s80 offen offset:1024 sc1 lds\n"
    "v_exp_f32 v114, v114\n"
    "s_add_u32 s82, s82, 0x400000\n"
    "s_add_u32 s54, s37, 32\n"
    "s_add_u32 s52, s34, s82\n"
    "s_addc_u32 s53, s35, 0\n"
    "v_mfma_f32_16x16x32_bf16 v[124:127], a[252:255], a[156:159], v[124:127]\n"
    "s_and_b32 s53, s53, 0xffff\n"
    "s_lshl_b32 s54, s54, 13\n"
    "v_mfma_f32_16x16x32_bf16 v[128:131], a[64:67], v[96:99], v[128:131]\n"
    "v_exp_f32 v115, v115\n"
    "s_mov_b32 s55, 0x20000\n"
    "s_lshr_b32 s82, s36, 5\n"
    "s_lshl_b32 s82, s82, 5\n"
    "v_mfma_f32_16x16x32_bf16 v[128:131], a[68:71], v[104:107], v[128:131]\n"
    "v_cvt_pk_bf16_f32 v112, v112, v113\n"
    "s_add_u32 s82, s82, s79\n"
    "s_lshl_b32 s82, s82, 15\n"
    "v_mfma_f32_16x16x32_bf16 v[144:147], a[72:75], v[96:99], v[144:147]\n"
    "v_cvt_pk_bf16_f32 v113, v114, v115\n"
    "s_add_u32 s82, s82, 0x27ffc00\n"
    "s_lshl_b32 s58, s37, 15\n"
    "s_add_u32 s56, s34, s82\n"
    "s_addc_u32 s57, s35, 0\n"
    "v_mfma_f32_16x16x32_bf16 v[144:147], a[76:79], v[104:107], v[144:147]\n"
    "v_exp_f32 v116, v116\n"
    "s_and_b32 s57, s57, 0xffff\n"
    "s_add_u32 s58, s58, 1024\n"
    "v_mfma_f32_16x16x32_bf16 v[160:163], a[80:83], v[96:99], v[160:163]\n"
    "s_mov_b32 s59, 0x20000\n"
    "s_sub_u32 s37, -6, s37\n"
    "v_mfma_f32_16x16x32_bf16 v[160:163], a[84:87], v[104:107], v[160:163]\n"
    "v_exp_f32 v117, v117\n"
    "s_bfe_u32 s84, s36, 0x20001\n"
    "s_lshr_b32 s65, s36, 5\n"
    "s_lshl_b32 s65, s65, 2\n"
    "v_mfma_f32_16x16x32_bf16 v[176:179], a[88:91], v[96:99], v[176:179]\n"
    "v_exp_f32 v118, v118\n"
    "s_add_u32 s65, s65, s84\n"
    "s_mul_i32 s66, s65, 0x38000\n"
    "v_mfma_f32_16x16x32_bf16 v[176:179], a[92:95], v[104:107], v[176:179]\n"
    "s_add_u32 s66, s66, 0x2fffc00\n"
    "s_add_u32 s48, s34, s66\n"
    "s_addc_u32 s49, s35, 0\n"
    "s_and_b32 s49, s49, 0xffff\n"
    "v_mfma_f32_16x16x32_bf16 v[192:195], a[96:99], v[96:99], v[192:195]\n"
    "buffer_load_dwordx4 v5, s[60:63], s80 offen offset:2048 sc1 lds\n"
    "v_exp_f32 v119, v119\n"
    "s_mov_b32 s50, 0x38400\n"
    "s_mov_b32 s51, 0x20000\n"
    "v_mfma_f32_16x16x32_bf16 v[192:195], a[100:103], v[104:107], v[192:195]\n"
    "s_lshr_b32 s94, s36, 5\n"
    "s_mov_b32 s67, 0x10000\n"
    "s_lshl_b32 s65, s85, 3\n"
    "v_mfma_f32_16x16x32_bf16 v[208:211], a[104:107], v[96:99], v[208:211]\n"
    "v_cvt_pk_bf16_f32 v114, v116, v117\n"
    "s_add_u32 s65, s65, s94\n"
    "s_lshl_b32 s83, s65, 10\n"
    "v_mfma_f32_16x16x32_bf16 v[208:211], a[108:111], v[104:107], v[208:211]\n"
    "v_cvt_pk_bf16_f32 v115, v118, v119\n"
    "s_lshl_b32 s66, s40, 6\n"
    "s_add_u32 s83, s83, s66\n"
    "s_add_u32 s83, s83, 0x2600000\n"
    "v_mfma_f32_16x16x32_bf16 v[224:227], a[112:115], v[96:99], v[224:227]\n"
    "v_exp_f32 v120, v120\n"
    "s_mul_i32 s65, s65, 0x30000\n"
    "s_lshl_b32 s66, s40, 12\n"
    "v_mfma_f32_16x16x32_bf16 v[224:227], a[116:119], v[104:107], v[224:227]\n"
    "v_exp_f32 v121, v121\n"
    "s_add_u32 s65, s65, s66\n"
    "s_add_u32 s65, s65, 0x4000000\n"
    "s_add_u32 s72, s34, s65\n"
    "s_addc_u32 s73, s35, 0\n"
    "v_mfma_f32_16x16x32_bf16 v[240:243], a[120:123], v[96:99], v[240:243]\n"
    "s_and_b32 s73, s73, 0xffff\n"
    "s_mov_b32 s74, 0x1030000\n"
    "v_mfma_f32_16x16x32_bf16 v[240:243], a[124:127], v[104:107], v[240:243]\n"
    "v_exp_f32 v122, v122\n"
    "s_mov_b32 s75, 0x20000\n"
    "v_lshrrev_b32 v16, 5, v1\n"
    "v_lshlrev_b32 v16, 11, v16\n"
    "v_mfma_f32_16x16x32_bf16 v[64:67], v[60:63], v[96:99], v[64:67]\n"
    "v_bfe_u32 v17, v1, 2, 3\n"
    "v_lshl_add_u32 v16, v17, 6, v16\n"
    "v_mfma_f32_16x16x32_bf16 v[64:67], v[60:63], v[104:107], v[64:67]\n"
    "v_exp_f32 v123, v123\n"
    "v_and_b32 v17, 3, v1\n"
    "v_lshl_add_u32 v16, v17, 3, v16\n"
    "v_add_u32 v16, s39, v16\n"
    "v_mfma_f32_16x16x32_bf16 v[96:99], a[192:195], a[160:163], v[88:91]\n"
    "v_cvt_pk_bf16_f32 v120, v120, v121\n"
    "v_bfe_u32 v17, v1, 4, 1\n"
    "v_lshl_add_u32 v22, v17, 5, v16\n"
    "v_mfma_f32_16x16x32_bf16 v[96:99], a[196:199], a[164:167], v[96:99]\n"
    "v_cvt_pk_bf16_f32 v121, v122, v123\n"
    "v_xor_b32 v23, 32, v22\n"
    "s_waitcnt vmcnt(17)\n"
    "v_add_u32 v24, 0x10000, v22\n"
    "v_add_u32 v25, 0x10000, v23\n"
    "v_mfma_f32_16x16x32_bf16 v[96:99], a[200:203], a[168:171], v[96:99]\n"
    "v_exp_f32 v124, v124\n"
    "s_waitcnt vmcnt(7)\n"
    "v_mov_b32 v28, 1\n"
    "s_cmp_eq_u32 s40, 0\n"
    "s_cselect_b32 s95, 1, 99\n"
    "v_mfma_f32_16x16x32_bf16 v[96:99], a[204:207], a[172:175], v[96:99]\n"
    "s_lshl_b32 s78, s40, 13\n"
    "s_mov_b32 s43, 0\n"
    "v_mfma_f32_16x16x32_bf16 v[100:103], a[208:211], a[160:163], v[88:91]\n"
    "buffer_load_dwordx4 v6, s[60:63], s80 offen offset:3072 sc1 lds\n"
    "v_exp_f32 v125, v125\n"
    "v_mfma_f32_16x16x32_bf16 v[100:103], a[212:215], a[164:167], v[100:103]\n"
    "v_exp_f32 v126, v126\n"
    "v_mfma_f32_16x16x32_bf16 v[100:103], a[216:219], a[168:171], v[100:103]\n"
    "v_exp_f32 v127, v127\n"
    "v_mfma_f32_16x16x32_bf16 v[100:103], a[220:223], a[172:175], v[100:103]\n"
    "buffer_load_dwordx4 a[0:3], v2, s[44:47], s90 offen offset:0 sc1\n"
    "v_mfma_f32_16x16x32_bf16 v[104:107], a[224:227], a[160:163], v[88:91]\n"
    "v_cvt_pk_bf16_f32 v122, v124, v125\n"
    "v_mfma_f32_16x16x32_bf16 v[104:107], a[228:231], a[164:167], v[104:107]\n"
    "buffer_load_dwordx4 a[4:7], v2, s[44:47], s90 offen offset:1024 sc1\n"
    "v_mfma_f32_16x16x32_bf16 v[104:107], a[232:235], a[168:171], v[104:107]\n"
    "v_cvt_pk_bf16_f32 v123, v126, v127\n"
    "v_lshrrev_b32 v16, 5, v1\n"
    "v_add3_u32 v16, s84, v16, 1\n"
    "v_and_b32 v17, 31, v1\n"
    "v_lshlrev_b32 v17, 4, v17\n"
    "v_add_u32 v17, s78, v17\n"
    "v_and_b32 v3, 3, v16\n"
    "v_lshl_add_u32 v3, v3, 9, v17\n"
    "v_add_u32 v3, 1024, v3\n"
    "v_add_u32 v4, 2, v16\n"
    "v_and_b32 v4, 3, v4\n"
    "v_lshl_add_u32 v4, v4, 9, v17\n"
    "v_add_u32 v5, 4096, v3\n"
    "v_add_u32 v6, 4096, v4\n"
    "s_add_u32 s78, s78, s39\n"
    "v_mfma_f32_16x16x32_bf16 v[104:107], a[236:239], a[172:175], v[104:107]\n"
    "v_exp_f32 v96, v96\n"
    "v_mfma_f32_16x16x32_bf16 v[108:111], a[240:243], a[160:163], v[88:91]\n"
    "buffer_load_dwordx4 a[16:19], v2, s[44:47], s90 offen offset:2048 sc1\n"
    "v_exp_f32 v97, v97\n"
    "v_rcp_f32 v80, v64\n"
    "s_nop 0\n"
    "v_mul_f32 v128, v80, v128\n"
    "v_mfma_f32_16x16x32_bf16 v[108:111], a[244:247], a[164:167], v[108:111]\n"
    "v_mul_f32 v129, v80, v129\n"
    "v_mul_f32 v130, v80, v130\n"
    "v_mfma_f32_16x16x32_bf16 v[108:111], a[248:251], a[168:171], v[108:111]\n"
    "buffer_load_dwordx4 a[20:23], v2, s[44:47], s90 offen offset:3072 sc1\n"
    "v_exp_f32 v98, v98\n"
    "v_mul_f32 v131, v80, v131\n"
    "v_mul_f32 v144, v80, v144\n"
    "v_mfma_f32_16x16x32_bf16 v[108:111], a[252:255], a[172:175], v[108:111]\n"
    "v_mul_f32 v145, v80, v145\n"
    "v_mul_f32 v146, v80, v146\n"
    "v_mfma_f32_16x16x32_bf16 v[132:135], a[64:67], v[112:115], v[132:135]\n"
    "buffer_load_dwordx4 a[32:35], v2, s[44:47], s91 offen offset:0 sc1\n"
    "v_exp_f32 v99, v99\n"
    "v_mul_f32 v147, v80, v147\n"
    "v_cvt_pk_bf16_f32 v32, v128, v129\n"
    "v_mfma_f32_16x16x32_bf16 v[132:135], a[68:71], v[120:123], v[132:135]\n"
    "v_cvt_pk_bf16_f32 v96, v96, v97\n"
    "v_cvt_pk_bf16_f32 v33, v130, v131\n"
    "v_cvt_pk_bf16_f32 v34, v144, v145\n"
    "v_mfma_f32_16x16x32_bf16 v[148:151], a[72:75], v[112:115], v[148:151]\n"
    "buffer_load_dwordx4 a[36:39], v2, s[44:47], s91 offen offset:1024 sc1\n"
    "v_cvt_pk_bf16_f32 v97, v98, v99\n"
    "v_cvt_pk_bf16_f32 v35, v146, v147\n"
    "s_nop 1\n"
    "v_mfma_f32_16x16x32_bf16 v[56:59], v[60:63], v[32:35], 0\n"
    "v_mfma_f32_16x16x32_bf16 v[148:151], a[76:79], v[120:123], v[148:151]\n"
    "v_exp_f32 v100, v100\n"
    "buffer_store_dwordx4 v[32:35], v2, s[68:71], s76 offen offset:0 sc1\n"
    "v_mul_f32 v160, v80, v160\n"
    "v_mfma_f32_16x16x32_bf16 v[164:167], a[80:83], v[112:115], v[164:167]\n"
    "buffer_load_dwordx4 a[48:51], v2, s[44:47], s91 offen offset:2048 sc1\n"
    "v_mul_f32 v161, v80, v161\n"
    "v_mul_f32 v162, v80, v162\n"
    "v_mfma_f32_16x16x32_bf16 v[164:167], a[84:87], v[120:123], v[164:167]\n"
    "v_exp_f32 v101, v101\n"
    "v_mul_f32 v163, v80, v163\n"
    "v_mfma_f32_16x16x32_bf16 v[180:183], a[88:91], v[112:115], v[180:183]\n"
    "buffer_load_dwordx4 a[52:55], v2, s[44:47], s91 offen offset:3072 sc1\n"
    "v_exp_f32 v102, v102\n"
    "v_mul_f32 v176, v80, v176\n"
    "v_mul_f32 v177, v80, v177\n"
    "v_mfma_f32_16x16x32_bf16 v[180:183], a[92:95], v[120:123], v[180:183]\n"
    "v_mul_f32 v178, v80, v178\n"
    "v_mul_f32 v179, v80, v179\n"
    "v_mfma_f32_16x16x32_bf16 v[196:199], a[96:99], v[112:115], v[196:199]\n"
    "buffer_load_dwordx4 a[8:11], v2, s[44:47], s92 offen offset:0 sc1\n"
    "v_exp_f32 v103, v103\n"
    "v_cvt_pk_bf16_f32 v36, v160, v161\n"
    "v_cvt_pk_bf16_f32 v37, v162, v163\n"
    "v_mfma_f32_16x16x32_bf16 v[196:199], a[100:103], v[120:123], v[196:199]\n"
    "v_cvt_pk_bf16_f32 v38, v176, v177\n"
    "v_cvt_pk_bf16_f32 v39, v178, v179\n"
    "v_mfma_f32_16x16x32_bf16 v[212:215], a[104:107], v[112:115], v[212:215]\n"
    "buffer_load_dwordx4 a[12:15], v2, s[44:47], s92 offen offset:1024 sc1\n"
    "v_cvt_pk_bf16_f32 v98, v100, v101\n"
    "v_mfma_f32_16x16x32_bf16 v[56:59], v[60:63], v[36:39], v[56:59]\n"
    "buffer_store_dwordx4 v[36:39], v2, s[68:71], s76 offen offset:2048 sc1\n"
    "v_mfma_f32_16x16x32_bf16 v[212:215], a[108:111], v[120:123], v[212:215]\n"
    "v_cvt_pk_bf16_f32 v99, v102, v103\n"
    "v_mul_f32 v192, v80, v192\n"
    "v_mul_f32 v193, v80, v193\n"
    "v_mfma_f32_16x16x32_bf16 v[228:231], a[112:115], v[112:115], v[228:231]\n"
    "buffer_load_dwordx4 a[24:27], v2, s[44:47], s92 offen offset:2048 sc1\n"
    "v_exp_f32 v104, v104\n"
    "v_mul_f32 v194, v80, v194\n"
    "v_mul_f32 v195, v80, v195\n"
    "v_mfma_f32_16x16x32_bf16 v[228:231], a[116:119], v[120:123], v[228:231]\n"
    "v_exp_f32 v105, v105\n"
    "v_mul_f32 v208, v80, v208\n"
    "v_mul_f32 v209, v80, v209\n"
    "v_mfma_f32_16x16x32_bf16 v[244:247], a[120:123], v[112:115], v[244:247]\n"
    "buffer_load_dwordx4 a[28:31], v2, s[44:47], s92 offen offset:3072 sc1\n"
    "v_mul_f32 v210, v80, v210\n"
    "v_mul_f32 v211, v80, v211\n"
    "v_mfma_f32_16x16x32_bf16 v[244:247], a[124:127], v[120:123], v[244:247]\n"
    "v_exp_f32 v106, v106\n"
    "v_cvt_pk_bf16_f32 v32, v192, v193\n"
    "v_mfma_f32_16x16x32_bf16 v[68:71], v[60:63], v[112:115], v[68:71]\n"
    "buffer_load_dwordx4 a[40:43], v2, s[44:47], s93 offen offset:0 sc1\n"
    "v_cvt_pk_bf16_f32 v33, v194, v195\n"
    "v_cvt_pk_bf16_f32 v34, v208, v209\n"
    "v_mfma_f32_16x16x32_bf16 v[68:71], v[60:63], v[120:123], v[68:71]\n"
    "v_exp_f32 v107, v107\n"
    "v_cvt_pk_bf16_f32 v35, v210, v211\n"
    "s_nop 1\n"
    "v_mfma_f32_16x16x32_bf16 v[56:59], v[60:63], v[32:35], v[56:59]\n"
    "v_mfma_f32_16x16x32_bf16 v[112:115], a[192:195], a[176:179], v[92:95]\n"
    "buffer_load_dwordx4 a[44:47], v2, s[44:47], s93 offen offset:1024 sc1\n"
    "v_cvt_pk_bf16_f32 v104, v104, v105\n"
    "buffer_store_dwordx4 v[32:35], v2, s[68:71], s86 offen offset:0 sc1\n"
    "v_mul_f32 v224, v80, v224\n"
    "v_mfma_f32_16x16x32_bf16 v[112:115], a[196:199], a[180:183], v[112:115]\n"
    "v_cvt_pk_bf16_f32 v105, v106, v107\n"
    "v_mul_f32 v225, v80, v225\n"
    "v_mul_f32 v226, v80, v226\n"
    "v_mfma_f32_16x16x32_bf16 v[112:115], a[200:203], a[184:187], v[112:115]\n"
    "buffer_load_dwordx4 a[56:59], v2, s[44:47], s93 offen offset:2048 sc1\n"
    "v_exp_f32 v108, v108\n"
    "v_mul_f32 v227, v80, v227\n"
    "v_mul_f32 v240, v80, v240\n"
    "v_mfma_f32_16x16x32_bf16 v[112:115], a[204:207], a[188:191], v[112:115]\n"
    "v_mul_f32 v241, v80, v241\n"
    "v_mul_f32 v242, v80, v242\n"
    "v_mfma_f32_16x16x32_bf16 v[116:119], a[208:211], a[176:179], v[92:95]\n"
    "buffer_load_dwordx4 a[60:63], v2, s[44:47], s93 offen offset:3072 sc1\n"
    "v_exp_f32 v109, v109\n"
    "v_mul_f32 v243, v80, v243\n"
    "v_cvt_pk_bf16_f32 v36, v224, v225\n"
    "v_mfma_f32_16x16x32_bf16 v[116:119], a[212:215], a[180:183], v[116:119]\n"
    "v_exp_f32 v110, v110\n"
    "v_cvt_pk_bf16_f32 v37, v226, v227\n"
    "v_cvt_pk_bf16_f32 v38, v240, v241\n"
    "v_mfma_f32_16x16x32_bf16 v[116:119], a[216:219], a[184:187], v[116:119]\n"
    "v_cvt_pk_bf16_f32 v39, v242, v243\n"
    "s_nop 1\n"
    "v_mfma_f32_16x16x32_bf16 v[56:59], v[60:63], v[36:39], v[56:59]\n"
    "v_mfma_f32_16x16x32_bf16 v[116:119], a[220:223], a[188:191], v[116:119]\n"
    "v_exp_f32 v111, v111\n"
    "buffer_store_dwordx4 v[36:39], v2, s[68:71], s86 offen offset:2048 sc1\n"
    "v_mfma_f32_16x16x32_bf16 v[120:123], a[224:227], a[176:179], v[92:95]\n"
    "v_cvt_pk_bf16_f32 v106, v108, v109\n"
    "v_cvt_pk_bf16_f32 v107, v110, v111\n"
    "v_mfma_f32_16x16x32_bf16 v[120:123], a[228:231], a[180:183], v[120:123]\n"
    "v_exp_f32 v112, v112\n"
    "v_mfma_f32_16x16x32_bf16 v[120:123], a[232:235], a[184:187], v[120:123]\n"
    "v_exp_f32 v113, v113\n"
    "v_mfma_f32_16x16x32_bf16 v[120:123], a[236:239], a[188:191], v[120:123]\n"
    "v_rcp_f32 v84, v68\n"
    "v_mfma_f32_16x16x32_bf16 v[124:127], a[240:243], a[176:179], v[92:95]\n"
    "v_mul_f32 v132, v84, v132\n"
    "v_mul_f32 v133, v84, v133\n"
    "v_mfma_f32_16x16x32_bf16 v[124:127], a[244:247], a[180:183], v[124:127]\n"
    "v_mul_f32 v134, v84, v134\n"
    "v_mfma_f32_16x16x32_bf16 v[124:127], a[248:251], a[184:187], v[124:127]\n"
    "v_exp_f32 v114, v114\n"
    "v_mul_f32 v135, v84, v135\n"
    "v_mul_f32 v148, v84, v148\n"
    "v_mfma_f32_16x16x32_bf16 v[124:127], a[252:255], a[188:191], v[124:127]\n"
    "v_mul_f32 v149, v84, v149\n"
    "v_mul_f32 v150, v84, v150\n"
    "v_mfma_f32_16x16x32_bf16 v[136:139], a[64:67], v[96:99], v[136:139]\n"
    "s_add_u32 s90, s90, 8192\n"
    "s_add_u32 s91, s91, 8192\n"
    "s_add_u32 s92, s92, 8192\n"
    "s_add_u32 s93, s93, 8192\n"
    "v_exp_f32 v115, v115\n"
    "v_mul_f32 v151, v84, v151\n"
    "v_cvt_pk_bf16_f32 v40, v132, v133\n"
    "v_mfma_f32_16x16x32_bf16 v[136:139], a[68:71], v[104:107], v[136:139]\n"
    "v_cvt_pk_bf16_f32 v112, v112, v113\n"
    "v_cvt_pk_bf16_f32 v41, v134, v135\n"
    "v_cvt_pk_bf16_f32 v42, v148, v149\n"
    "v_mfma_f32_16x16x32_bf16 v[152:155], a[72:75], v[96:99], v[152:155]\n"
    "v_cvt_pk_bf16_f32 v113, v114, v115\n"
    "v_cvt_pk_bf16_f32 v43, v150, v151\n"
    "s_nop 1\n"
    "v_mfma_f32_16x16x32_bf16 v[56:59], v[60:63], v[40:43], v[56:59]\n"
    "v_mfma_f32_16x16x32_bf16 v[152:155], a[76:79], v[104:107], v[152:155]\n"
    "v_exp_f32 v116, v116\n"
    "buffer_store_dwordx4 v[40:43], v2, s[68:71], s76 offen offset:1024 sc1\n"
    "v_mul_f32 v164, v84, v164\n"
    "v_mfma_f32_16x16x32_bf16 v[168:171], a[80:83], v[96:99], v[168:171]\n"
    "buffer_load_dwordx4 a[192:195], v2, s[44:47], s90 offen offset:0 sc1\n"
    "v_mul_f32 v165, v84, v165\n"
    "v_mul_f32 v166, v84, v166\n"
    "v_mfma_f32_16x16x32_bf16 v[168:171], a[84:87], v[104:107], v[168:171]\n"
    "v_exp_f32 v117, v117\n"
    "v_mul_f32 v167, v84, v167\n"
    "v_mfma_f32_16x16x32_bf16 v[184:187], a[88:91], v[96:99], v[184:187]\n"
    "v_exp_f32 v118, v118\n"
    "v_mul_f32 v180, v84, v180\n"
    "v_mul_f32 v181, v84, v181\n"
    "v_mfma_f32_16x16x32_bf16 v[184:187], a[92:95], v[104:107], v[184:187]\n"
    "buffer_load_dwordx4 a[196:199], v2, s[44:47], s90 offen offset:1024 sc1\n"
    "v_mul_f32 v182, v84, v182\n"
    "v_mul_f32 v183, v84, v183\n"
    "v_mfma_f32_16x16x32_bf16 v[200:203], a[96:99], v[96:99], v[200:203]\n"
    "v_exp_f32 v119, v119\n"
    "v_cvt_pk_bf16_f32 v44, v164, v165\n"
    "v_cvt_pk_bf16_f32 v45, v166, v167\n"
    "v_mfma_f32_16x16x32_bf16 v[200:203], a[100:103], v[104:107], v[200:203]\n"
    "v_cvt_pk_bf16_f32 v46, v180, v181\n"
    "v_cvt_pk_bf16_f32 v47, v182, v183\n"
    "v_mfma_f32_16x16x32_bf16 v[216:219], a[104:107], v[96:99], v[216:219]\n"
    "buffer_load_dwordx4 a[208:211], v2, s[44:47], s90 offen offset:2048 sc1\n"
    "v_cvt_pk_bf16_f32 v114, v116, v117\n"
    "v_mfma_f32_16x16x32_bf16 v[56:59], v[60:63], v[44:47], v[56:59]\n"
    "buffer_store_dwordx4 v[44:47], v2, s[68:71], s76 offen offset:3072 sc1\n"
    "v_mfma_f32_16x16x32_bf16 v[216:219], a[108:111], v[104:107], v[216:219]\n"
    "v_cvt_pk_bf16_f32 v115, v118, v119\n"
    "v_mul_f32 v196, v84, v196\n"
    "v_mul_f32 v197, v84, v197\n"
    "v_mfma_f32_16x16x32_bf16 v[232:235], a[112:115], v[96:99], v[232:235]\n"
    "v_exp_f32 v120, v120\n"
    "v_mul_f32 v198, v84, v198\n"
    "v_mul_f32 v199, v84, v199\n"
    "v_mfma_f32_16x16x32_bf16 v[232:235], a[116:119], v[104:107], v[232:235]\n"
    "buffer_load_dwordx4 a[212:215], v2, s[44:47], s90 offen offset:3072 sc1\n"
    "v_exp_f32 v121, v121\n"
    "v_mul_f32 v212, v84, v212\n"
    "v_mul_f32 v213, v84, v213\n"
    "v_mfma_f32_16x16x32_bf16 v[248:251], a[120:123], v[96:99], v[248:251]\n"
    "v_mul_f32 v214, v84, v214\n"
    "v_mul_f32 v215, v84, v215\n"
    "v_mfma_f32_16x16x32_bf16 v[248:251], a[124:127], v[104:107], v[248:251]\n"
    "v_exp_f32 v122, v122\n"
    "v_cvt_pk_bf16_f32 v40, v196, v197\n"
    "v_mfma_f32_16x16x32_bf16 v[72:75], v[60:63], v[96:99], v[72:75]\n"
    "buffer_load_dwordx4 a[224:227], v2, s[44:47], s91 offen offset:0 sc1\n"
    "v_exp_f32 v123, v123\n"
    "v_cvt_pk_bf16_f32 v120, v120, v121\n"
    "v_cvt_pk_bf16_f32 v121, v122, v123\n"
    "v_exp_f32 v124, v124\n"
    "v_exp_f32 v125, v125\n"
    "v_exp_f32 v126, v126\n"
    "v_exp_f32 v127, v127\n"
    "v_cvt_pk_bf16_f32 v122, v124, v125\n"
    "v_cvt_pk_bf16_f32 v123, v126, v127\n"
    "v_cvt_pk_bf16_f32 v41, v198, v199\n"
    "v_cvt_pk_bf16_f32 v42, v212, v213\n"
    "v_mfma_f32_16x16x32_bf16 v[72:75], v[60:63], v[104:107], v[72:75]\n"
    "v_cvt_pk_bf16_f32 v43, v214, v215\n"
    "s_nop 1\n"
    "v_mfma_f32_16x16x32_bf16 v[56:59], v[60:63], v[40:43], v[56:59]\n"
    "v_mfma_f32_16x16x32_bf16 v[140:143], a[64:67], v[112:115], v[140:143]\n"
    "buffer_store_dwordx4 v[40:43], v2, s[68:71], s86 offen offset:1024 sc1\n"
    "v_mul_f32 v228, v84, v228\n"
    "v_mfma_f32_16x16x32_bf16 v[140:143], a[68:71], v[120:123], v[140:143]\n"
    "buffer_load_dwordx4 a[228:231], v2, s[44:47], s91 offen offset:1024 sc1\n"
    "v_mul_f32 v229, v84, v229\n"
    "v_mul_f32 v230, v84, v230\n"
    "v_mfma_f32_16x16x32_bf16 v[156:159], a[72:75], v[112:115], v[156:159]\n"
    "v_mul_f32 v231, v84, v231\n"
    "v_mul_f32 v244, v84, v244\n"
    "v_mfma_f32_16x16x32_bf16 v[156:159], a[76:79], v[120:123], v[156:159]\n"
    "v_mul_f32 v245, v84, v245\n"
    "v_mul_f32 v246, v84, v246\n"
    "v_mfma_f32_16x16x32_bf16 v[172:175], a[80:83], v[112:115], v[172:175]\n"
    "buffer_load_dwordx4 a[240:243], v2, s[44:47], s91 offen offset:2048 sc1\n"
    "v_mul_f32 v247, v84, v247\n"
    "v_cvt_pk_bf16_f32 v44, v228, v229\n"
    "v_mfma_f32_16x16x32_bf16 v[172:175], a[84:87], v[120:123], v[172:175]\n"
    "v_cvt_pk_bf16_f32 v45, v230, v231\n"
    "v_cvt_pk_bf16_f32 v46, v244, v245\n"
    "v_mfma_f32_16x16x32_bf16 v[188:191], a[88:91], v[112:115], v[188:191]\n"
    "v_cvt_pk_bf16_f32 v47, v246, v247\n"
    "s_nop 1\n"
    "v_mfma_f32_16x16x32_bf16 v[56:59], v[60:63], v[44:47], v[56:59]\n"
    "v_mfma_f32_16x16x32_bf16 v[188:191], a[92:95], v[120:123], v[188:191]\n"
    "buffer_load_dwordx4 a[244:247], v2, s[44:47], s91 offen offset:3072 sc1\n"
    "buffer_store_dwordx4 v[44:47], v2, s[68:71], s86 offen offset:3072 sc1\n"
    "v_mfma_f32_16x16x32_bf16 v[204:207], a[96:99], v[112:115], v[204:207]\n"
    "buffer_load_dwordx4 a[200:203], v2, s[44:47], s92 offen offset:0 sc1\n"
    "v_mfma_f32_16x16x32_bf16 v[204:207], a[100:103], v[120:123], v[204:207]\n"
    "v_rcp_f32 v88, v72\n"
    "v_mfma_f32_16x16x32_bf16 v[220:223], a[104:107], v[112:115], v[220:223]\n"
    "v_pk_mul_f32 v[136:137], v[136:137], v[88:89] op_sel_hi:[1,0]\n"
    "v_pk_mul_f32 v[138:139], v[138:139], v[88:89] op_sel_hi:[1,0]\n"
    "v_mfma_f32_16x16x32_bf16 v[220:223], a[108:111], v[120:123], v[220:223]\n"
    "buffer_load_dwordx4 a[204:207], v2, s[44:47], s92 offen offset:1024 sc1\n"
    "v_mfma_f32_16x16x32_bf16 v[236:239], a[112:115], v[112:115], v[236:239]\n"
    "v_pk_mul_f32 v[152:153], v[152:153], v[88:89] op_sel_hi:[1,0]\n"
    "v_pk_mul_f32 v[154:155], v[154:155], v[88:89] op_sel_hi:[1,0]\n"
    "v_mfma_f32_16x16x32_bf16 v[236:239], a[116:119], v[120:123], v[236:239]\n"
    "v_cvt_pk_bf16_f32 v32, v136, v137\n"
    "v_cvt_pk_bf16_f32 v33, v138, v139\n"
    "v_mfma_f32_16x16x32_bf16 v[252:255], a[120:123], v[112:115], v[252:255]\n"
    "buffer_load_dwordx4 a[216:219], v2, s[44:47], s92 offen offset:2048 sc1\n"
    "v_mfma_f32_16x16x32_bf16 v[252:255], a[124:127], v[120:123], v[252:255]\n"
    "v_cvt_pk_bf16_f32 v34, v152, v153\n"
    "v_cvt_pk_bf16_f32 v35, v154, v155\n"
    "v_mfma_f32_16x16x32_bf16 v[76:79], v[60:63], v[112:115], v[76:79]\n"
    "s_nop 0\n"
    "v_mfma_f32_16x16x32_bf16 v[56:59], v[60:63], v[32:35], v[56:59]\n"
    "v_mfma_f32_16x16x32_bf16 v[76:79], v[60:63], v[120:123], v[76:79]\n"
    "buffer_load_dwordx4 a[220:223], v2, s[44:47], s92 offen offset:3072 sc1\n"
    "buffer_store_dwordx4 v[32:35], v2, s[68:71], s77 offen offset:0 sc1\n"
    "v_pk_mul_f32 v[168:169], v[168:169], v[88:89] op_sel_hi:[1,0]\n"
    "v_pk_mul_f32 v[170:171], v[170:171], v[88:89] op_sel_hi:[1,0]\n"
    "v_pk_mul_f32 v[184:185], v[184:185], v[88:89] op_sel_hi:[1,0]\n"
    "v_pk_mul_f32 v[186:187], v[186:187], v[88:89] op_sel_hi:[1,0]\n"
    "v_cvt_pk_bf16_f32 v36, v168, v169\n"
    "v_cvt_pk_bf16_f32 v37, v170, v171\n"
    "v_cvt_pk_bf16_f32 v38, v184, v185\n"
    "v_cvt_pk_bf16_f32 v39, v186, v187\n"
    "s_nop 1\n"
    "v_mfma_f32_16x16x32_bf16 v[56:59], v[60:63], v[36:39], v[56:59]\n"
    "buffer_store_dwordx4 v[36:39], v2, s[68:71], s77 offen offset:2048 sc1\n"
    "v_pk_mul_f32 v[200:201], v[200:201], v[88:89] op_sel_hi:[1,0]\n"
    "v_pk_mul_f32 v[202:203], v[202:203], v[88:89] op_sel_hi:[1,0]\n"
    "v_pk_mul_f32 v[216:217], v[216:217], v[88:89] op_sel_hi:[1,0]\n"
    "v_pk_mul_f32 v[218:219], v[218:219], v[88:89] op_sel_hi:[1,0]\n"
    "v_cvt_pk_bf16_f32 v32, v200, v201\n"
    "v_cvt_pk_bf16_f32 v33, v202, v203\n"
    "v_cvt_pk_bf16_f32 v34, v216, v217\n"
    "v_cvt_pk_bf16_f32 v35, v218, v219\n"
    "s_nop 1\n"
    "v_mfma_f32_16x16x32_bf16 v[56:59], v[60:63], v[32:35], v[56:59]\n"
    "buffer_store_dwordx4 v[32:35], v2, s[68:71], s87 offen offset:0 sc1\n"
    "v_pk_mul_f32 v[232:233], v[232:233], v[88:89] op_sel_hi:[1,0]\n"
    "v_pk_mul_f32 v[234:235], v[234:235], v[88:89] op_sel_hi:[1,0]\n"
    "v_pk_mul_f32 v[248:249], v[248:249], v[88:89] op_sel_hi:[1,0]\n"
    "v_pk_mul_f32 v[250:251], v[250:251], v[88:89] op_sel_hi:[1,0]\n"
    "v_cvt_pk_bf16_f32 v36, v232, v233\n"
    "v_cvt_pk_bf16_f32 v37, v234, v235\n"
    "v_cvt_pk_bf16_f32 v38, v248, v249\n"
    "v_cvt_pk_bf16_f32 v39, v250, v251\n"
    "s_nop 1\n"
    "v_mfma_f32_16x16x32_bf16 v[56:59], v[60:63], v[36:39], v[56:59]\n"
    "buffer_store_dwordx4 v[36:39], v2, s[68:71], s87 offen offset:2048 sc1\n"
    "v_add_f32 v12, v64, v68\n"
    "v_add_f32 v12, v12, v72\n"
    "v_rcp_f32 v92, v76\n"
    "s_nop 0\n"
    "v_pk_mul_f32 v[140:141], v[140:141], v[92:93] op_sel_hi:[1,0]\n"
    "v_pk_mul_f32 v[142:143], v[142:143], v[92:93] op_sel_hi:[1,0]\n"
    "v_pk_mul_f32 v[156:157], v[156:157], v[92:93] op_sel_hi:[1,0]\n"
    "v_pk_mul_f32 v[158:159], v[158:159], v[92:93] op_sel_hi:[1,0]\n"
    "v_cvt_pk_bf16_f32 v40, v140, v141\n"
    "v_cvt_pk_bf16_f32 v41, v142, v143\n"
    "v_cvt_pk_bf16_f32 v42, v156, v157\n"
    "v_cvt_pk_bf16_f32 v43, v158, v159\n"
    "s_nop 1\n"
    "v_mfma_f32_16x16x32_bf16 v[56:59], v[60:63], v[40:43], v[56:59]\n"
    "buffer_store_dwordx4 v[40:43], v2, s[68:71], s77 offen offset:1024 sc1\n"
    "v_pk_mul_f32 v[172:173], v[172:173], v[92:93] op_sel_hi:[1,0]\n"
    "v_pk_mul_f32 v[174:175], v[174:175], v[92:93] op_sel_hi:[1,0]\n"
    "v_pk_mul_f32 v[188:189], v[188:189], v[92:93] op_sel_hi:[1,0]\n"
    "v_pk_mul_f32 v[190:191], v[190:191], v[92:93] op_sel_hi:[1,0]\n"
    "v_cvt_pk_bf16_f32 v44, v172, v173\n"
    "v_cvt_pk_bf16_f32 v45, v174, v175\n"
    "v_cvt_pk_bf16_f32 v46, v188, v189\n"
    "v_cvt_pk_bf16_f32 v47, v190, v191\n"
    "s_nop 1\n"
    "v_mfma_f32_16x16x32_bf16 v[56:59], v[60:63], v[44:47], v[56:59]\n"
    "buffer_store_dwordx4 v[44:47], v2, s[68:71], s77 offen offset:3072 sc1\n"
    "v_pk_mul_f32 v[204:205], v[204:205], v[92:93] op_sel_hi:[1,0]\n"
    "v_pk_mul_f32 v[206:207], v[206:207], v[92:93] op_sel_hi:[1,0]\n"
    "v_pk_mul_f32 v[220:221], v[220:221], v[92:93] op_sel_hi:[1,0]\n"
    "v_pk_mul_f32 v[222:223], v[222:223], v[92:93] op_sel_hi:[1,0]\n"
    "v_cvt_pk_bf16_f32 v40, v204, v205\n"
    "v_cvt_pk_bf16_f32 v41, v206, v207\n"
    "v_cvt_pk_bf16_f32 v42, v220, v221\n"
    "v_cvt_pk_bf16_f32 v43, v222, v223\n"
    "s_nop 1\n"
    "v_mfma_f32_16x16x32_bf16 v[56:59], v[60:63], v[40:43], v[56:59]\n"
    "buffer_store_dwordx4 v[40:43], v2, s[68:71], s87 offen offset:1024 sc1\n"
    "v_pk_mul_f32 v[236:237], v[236:237], v[92:93] op_sel_hi:[1,0]\n"
    "v_pk_mul_f32 v[238:239], v[238:239], v[92:93] op_sel_hi:[1,0]\n"
    "v_pk_mul_f32 v[252:253], v[252:253], v[92:93] op_sel_hi:[1,0]\n"
    "v_pk_mul_f32 v[254:255], v[254:255], v[92:93] op_sel_hi:[1,0]\n"
    "v_cvt_pk_bf16_f32 v44, v236, v237\n"
    "v_cvt_pk_bf16_f32 v45, v238, v239\n"
    "v_cvt_pk_bf16_f32 v46, v252, v253\n"
    "v_cvt_pk_bf16_f32 v47, v254, v255\n"
    "s_nop 1\n"
    "v_mfma_f32_16x16x32_bf16 v[56:59], v[60:63], v[44:47], v[56:59]\n"
    "buffer_store_dwordx4 v[44:47], v2, s[68:71], s87 offen offset:3072 sc1\n"
    "v_add_f32 v12, v12, v76\n"
    "s_nop 5\n"
    "v_fma_f32 v12, v56, 0, v12\n"
    "v_cmp_ngt_f32 vcc, s89, v12\n"
    "v_mov_b32 v64, 0\n"
    "v_mov_b32 v65, 0\n"
    "v_mov_b32 v66, 0\n"
    "v_mov_b32 v67, 0\n"
    "s_cbranch_vccnz .Lk2_slow\n"
    ".Lk2_odone:\n"
    "s_nop 0\n"
    "v_mfma_f32_32x32x16_bf16 v[128:143], v[64:67], v[64:67], 0\n"
    "buffer_load_dwordx4 a[232:235], v2, s[44:47], s93 offen offset:0 sc1\n"
    "v_mfma_f32_32x32x16_bf16 v[144:159], v[64:67], v[64:67], 0\n"
    "buffer_load_dwordx4 a[236:239], v2, s[44:47], s93 offen offset:1024 sc1\n"
    "v_mfma_f32_32x32x16_bf16 v[160:175], v[64:67], v[64:67], 0\n"
    "buffer_load_dwordx4 a[248:251], v2, s[44:47], s93 offen offset:2048 sc1\n"
    "v_mfma_f32_32x32x16_bf16 v[176:191], v[64:67], v[64:67], 0\n"
    "buffer_load_dwordx4 a[252:255], v2, s[44:47], s93 offen offset:3072 sc1\n"
    "v_mfma_f32_32x32x16_bf16 v[192:207], v[64:67], v[64:67], 0\n"
    "v_mfma_f32_32x32x16_bf16 v[208:223], v[64:67], v[64:67], 0\n"
    "v_mfma_f32_32x32x16_bf16 v[224:239], v[64:67], v[64:67], 0\n"
    "v_mfma_f32_32x32x16_bf16 v[240:255], v[64:67], v[64:67], 0\n"
    "s_add_u32 s90, s90, 8192\n"
    "s_add_u32 s91, s91, 8192\n"
    "s_add_u32 s92, s92, 8192\n"
    "s_add_u32 s93, s93, 8192\n"
    "s_add_u32 s65, s84, 1\n"
    "s_and_b32 s65, s65, 3\n"
    "s_mul_i32 s65, s65, 0xc000\n"
    "s_add_u32 s87, s65, 0x0\n"
    "s_add_u32 s65, s84, 2\n"
    "s_and_b32 s65, s65, 3\n"
    "s_mul_i32 s65, s65, 0xc000\n"
    "s_add_u32 s88, s65, 0x4000\n"
    "s_add_u32 s65, s84, 3\n"
    "s_and_b32 s65, s65, 3\n"
    "s_mul_i32 s65, s65, 0xc000\n"
    "s_add_u32 s89, s65, 0x8000\n"
    "s_mul_i32 s86, s84, 0xc000\n"
    "s_waitcnt vmcnt(29)\n"
    "s_barrier\n"
    "ds_read_b64_tr_b16 a[128:129], v22 offset:0\n"
    "ds_read_b64_tr_b16 a[130:131], v22 offset:4096\n"
    "ds_read_b64_tr_b16 a[132:133], v22 offset:8192\n"
    "ds_read_b64_tr_b16 a[134:135], v22 offset:12288\n"
    "ds_read_b64_tr_b16 a[136:137], v22 offset:16384\n"
    "ds_read_b64_tr_b16 a[138:139], v22 offset:20480\n"
    "ds_read_b64_tr_b16 a[140:141], v22 offset:24576\n"
    "ds_read_b64_tr_b16 a[142:143], v22 offset:28672\n"
    "ds_read_b64_tr_b16 a[144:145], v23 offset:0\n"
    "ds_read_b64_tr_b16 a[146:147], v23 offset:4096\n"
    "ds_read_b64_tr_b16 a[148:149], v23 offset:8192\n"
    "ds_read_b64_tr_b16 a[150:151], v23 offset:12288\n"
    "ds_read_b64_tr_b16 a[152:153], v23 offset:16384\n"
    "ds_read_b64_tr_b16 a[154:155], v23 offset:20480\n"
    "ds_read_b64_tr_b16 a[156:157], v23 offset:24576\n"
    "ds_read_b64_tr_b16 a[158:159], v23 offset:28672\n"
    "s_waitcnt lgkmcnt(0)\n"
    "s_nop 7\n"
    "s_nop 7\n"
    ".Lk3_loop:\n"
    "v_mfma_f32_16x16x32_bf16 v[128:131], a[128:131], a[0:3], v[128:131]\n"
    "ds_read_b64_tr_b16 a[160:161], v22 offset:512\n"
    "s_add_u32 m0, s78, 0x10000\n"
    "v_mfma_f32_16x16x32_bf16 v[128:131], a[132:135], a[16:19], v[128:131]\n"
    "buffer_load_dwordx4 v3, s[48:51], s67 offen offset:0 sc1 lds\n"
    "v_mfma_f32_16x16x32_bf16 v[128:131], a[136:139], a[32:35], v[128:131]\n"
    "ds_read_b64_tr_b16 a[162:163], v22 offset:4608\n"
    "v_mfma_f32_16x16x32_bf16 v[128:131], a[140:143], a[48:51], v[128:131]\n"
    "v_mfma_f32_16x16x32_bf16 v[132:135], a[128:131], a[4:7], v[132:135]\n"
    "ds_read_b64_tr_b16 a[164:165], v22 offset:8704\n"
    "v_mfma_f32_16x16x32_bf16 v[132:135], a[132:135], a[20:23], v[132:135]\n"
    "v_mfma_f32_16x16x32_bf16 v[132:135], a[136:139], a[36:39], v[132:135]\n"
    "ds_read_b64_tr_b16 a[166:167], v22 offset:12800\n"
    "buffer_load_dwordx4 a[64:67], v2, s[44:47], s90 offen offset:0 sc1\n"
    "v_mfma_f32_16x16x32_bf16 v[132:135], a[140:143], a[52:55], v[132:135]\n"
    "v_mfma_f32_16x16x32_bf16 v[136:139], a[128:131], a[8:11], v[136:139]\n"
    "ds_read_b64_tr_b16 a[168:169], v22 offset:16896\n"
    "v_mfma_f32_16x16x32_bf16 v[136:139], a[132:135], a[24:27], v[136:139]\n"
    "v_mfma_f32_16x16x32_bf16 v[136:139], a[136:139], a[40:43], v[136:139]\n"
    "ds_read_b64_tr_b16 a[170:171], v22 offset:20992\n"
    "v_mfma_f32_16x16x32_bf16 v[136:139], a[140:143], a[56:59], v[136:139]\n"
    "buffer_load_dwordx4 a[68:71], v2, s[44:47], s90 offen offset:1024 sc1\n"
    "v_mfma_f32_16x16x32_bf16 v[140:143], a[128:131], a[12:15], v[140:143]\n"
    "ds_read_b64_tr_b16 a[172:173], v22 offset:25088\n"
    "v_mfma_f32_16x16x32_bf16 v[140:143], a[132:135], a[28:31], v[140:143]\n"
    "v_mfma_f32_16x16x32_bf16 v[140:143], a[136:139], a[44:47], v[140:143]\n"
    "ds_read_b64_tr_b16 a[174:175], v22 offset:29184\n"
    "v_mfma_f32_16x16x32_bf16 v[140:143], a[140:143], a[60:63], v[140:143]\n"
    "s_waitcnt lgkmcnt(8)\n"
    "v_mfma_f32_16x16x32_bf16 v[144:147], a[144:147], a[0:3], v[144:147]\n"
    "ds_read_b64_tr_b16 a[176:177], v23 offset:512\n"
    "buffer_load_dwordx4 v4, s[48:51], s67 offen offset:1024 sc1 lds\n"
    "v_mfma_f32_16x16x32_bf16 v[144:147], a[148:151], a[16:19], v[144:147]\n"
    "v_mfma_f32_16x16x32_bf16 v[144:147], a[152:155], a[32:35], v[144:147]\n"
    "ds_read_b64_tr_b16 a[178:179], v23 offset:4608\n"
    "v_mfma_f32_16x16x32_bf16 v[144:147], a[156:159], a[48:51], v[144:147]\n"
    "v_mfma_f32_16x16x32_bf16 v[148:151], a[144:147], a[4:7], v[148:151]\n"
    "ds_read_b64_tr_b16 a[180:181], v23 offset:8704\n"
    "v_mfma_f32_16x16x32_bf16 v[148:151], a[148:151], a[20:23], v[148:151]\n"
    "buffer_load_dwordx4 a[80:83], v2, s[44:47], s90 offen offset:2048 sc1\n"
    "v_mfma_f32_16x16x32_bf16 v[148:151], a[152:155], a[36:39], v[148:151]\n"
    "ds_read_b64_tr_b16 a[182:183], v23 offset:12800\n"
    "v_mfma_f32_16x16x32_bf16 v[148:151], a[156:159], a[52:55], v[148:151]\n"
    "v_mfma_f32_16x16x32_bf16 v[152:155], a[144:147], a[8:11], v[152:155]\n"
    "ds_read_b64_tr_b16 a[184:185], v23 offset:16896\n"
    "v_mfma_f32_16x16x32_bf16 v[152:155], a[148:151], a[24:27], v[152:155]\n"
    "v_mfma_f32_16x16x32_bf16 v[152:155], a[152:155], a[40:43], v[152:155]\n"
    "ds_read_b64_tr_b16 a[186:187], v23 offset:20992\n"
    "buffer_load_dwordx4 a[84:87], v2, s[44:47], s90 offen offset:3072 sc1\n"
    "v_mfma_f32_16x16x32_bf16 v[152:155], a[156:159], a[56:59], v[152:155]\n"
    "v_mfma_f32_16x16x32_bf16 v[156:159], a[144:147], a[12:15], v[156:159]\n"
    "ds_read_b64_tr_b16 a[188:189], v23 offset:25088\n"
    "v_mfma_f32_16x16x32_bf16 v[156:159], a[148:151], a[28:31], v[156:159]\n"
    "v_mfma_f32_16x16x32_bf16 v[156:159], a[152:155], a[44:47], v[156:159]\n"
    "ds_read_b64_tr_b16 a[190:191], v23 offset:29184\n"
    "v_mfma_f32_16x16x32_bf16 v[156:159], a[156:159], a[60:63], v[156:159]\n"
    "s_waitcnt lgkmcnt(8)\n"
    "buffer_load_dwordx4 v3, s[48:51], s67 offen offset:2048 sc1 lds\n"
    "v_mfma_f32_16x16x32_bf16 v[160:163], a[160:163], a[0:3], v[160:163]\n"
    "ds_read_b64_tr_b16 a[128:129], v22 offset:1024\n"
    "v_mfma_f32_16x16x32_bf16 v[160:163], a[164:167], a[16:19], v[160:163]\n"
    "v_mfma_f32_16x16x32_bf16 v[160:163], a[168:171], a[32:35], v[160:163]\n"
    "ds_read_b64_tr_b16 a[130:131], v22 offset:5120\n"
    "v_mfma_f32_16x16x32_bf16 v[160:163], a[172:175], a[48:51], v[160:163]\n"
    "v_mfma_f32_16x16x32_bf16 v[164:167], a[160:163], a[4:7], v[164:167]\n"
    "ds_read_b64_tr_b16 a[132:133], v22 offset:9216\n"
    "buffer_load_dwordx4 a[96:99], v2, s[44:47], s91 offen offset:0 sc1\n"
    "v_mfma_f32_16x16x32_bf16 v[164:167], a[164:167], a[20:23], v[164:167]\n"
    "v_mfma_f32_16x16x32_bf16 v[164:167], a[168:171], a[36:39], v[164:167]\n"
    "ds_read_b64_tr_b16 a[134:135], v22 offset:13312\n"
    "v_mfma_f32_16x16x32_bf16 v[164:167], a[172:175], a[52:55], v[164:167]\n"
    "v_mfma_f32_16x16x32_bf16 v[168:171], a[160:163], a[8:11], v[168:171]\n"
    "ds_read_b64_tr_b16 a[136:137], v22 offset:17408\n"
    "v_mfma_f32_16x16x32_bf16 v[168:171], a[164:167], a[24:27], v[168:171]\n"
    "buffer_load_dwordx4 a[100:103], v2, s[44:47], s91 offen offset:1024 sc1\n"
    "v_mfma_f32_16x16x32_bf16 v[168:171], a[168:171], a[40:43], v[168:171]\n"
    "ds_read_b64_tr_b16 a[138:139], v22 offset:21504\n"
    "v_mfma_f32_16x16x32_bf16 v[168:171], a[172:175], a[56:59], v[168:171]\n"
    "v_mfma_f32_16x16x32_bf16 v[172:175], a[160:163], a[12:15], v[172:175]\n"
    "ds_read_b64_tr_b16 a[140:141], v22 offset:25600\n"
    "v_mfma_f32_16x16x32_bf16 v[172:175], a[164:167], a[28:31], v[172:175]\n"
    "v_mfma_f32_16x16x32_bf16 v[172:175], a[168:171], a[44:47], v[172:175]\n"
    "ds_read_b64_tr_b16 a[142:143], v22 offset:29696\n"
    "buffer_load_dwordx4 v4, s[48:51], s67 offen offset:3072 sc1 lds\n"
    "v_mfma_f32_16x16x32_bf16 v[172:175], a[172:175], a[60:63], v[172:175]\n"
    "s_waitcnt lgkmcnt(8)\n"
    "v_mfma_f32_16x16x32_bf16 v[176:179], a[176:179], a[0:3], v[176:179]\n"
    "ds_read_b64_tr_b16 a[144:145], v23 offset:1024\n"
    "v_mfma_f32_16x16x32_bf16 v[176:179], a[180:183], a[16:19], v[176:179]\n"
    "v_mfma_f32_16x16x32_bf16 v[176:179], a[184:187], a[32:35], v[176:179]\n"
    "ds_read_b64_tr_b16 a[146:147], v23 offset:5120\n"
    "v_mfma_f32_16x16x32_bf16 v[176:179], a[188:191], a[48:51], v[176:179]\n"
    "buffer_load_dwordx4 a[112:115], v2, s[44:47], s91 offen offset:2048 sc1\n"
    "v_mfma_f32_16x16x32_bf16 v[180:183], a[176:179], a[4:7], v[180:183]\n"
    "ds_read_b64_tr_b16 a[148:149], v23 offset:9216\n"
    "v_mfma_f32_16x16x32_bf16 v[180:183], a[180:183], a[20:23], v[180:183]\n"
    "v_mfma_f32_16x16x32_bf16 v[180:183], a[184:187], a[36:39], v[180:183]\n"
    "ds_read_b64_tr_b16 a[150:151], v23 offset:13312\n"
    "v_mfma_f32_16x16x32_bf16 v[180:183], a[188:191], a[52:55], v[180:183]\n"
    "v_mfma_f32_16x16x32_bf16 v[184:187], a[176:179], a[8:11], v[184:187]\n"
    "ds_read_b64_tr_b16 a[152:153], v23 offset:17408\n"
    "buffer_load_dwordx4 a[116:119], v2, s[44:47], s91 offen offset:3072 sc1\n"
    "v_mfma_f32_16x16x32_bf16 v[184:187], a[180:183], a[24:27], v[184:187]\n"
    "v_mfma_f32_16x16x32_bf16 v[184:187], a[184:187], a[40:43], v[184:187]\n"
    "ds_read_b64_tr_b16 a[154:155], v23 offset:21504\n"
    "v_mfma_f32_16x16x32_bf16 v[184:187], a[188:191], a[56:59], v[184:187]\n"
    "s_add_u32 m0, s78, 0x11000\n"
    "v_mfma_f32_16x16x32_bf16 v[188:191], a[176:179], a[12:15], v[188:191]\n"
    "ds_read_b64_tr_b16 a[156:157], v23 offset:25600\n"
    "v_mfma_f32_16x16x32_bf16 v[188:191], a[180:183], a[28:31], v[188:191]\n"
    "buffer_load_dwordx4 v5, s[48:51], s67 offen offset:0 sc1 lds\n"
    "v_mfma_f32_16x16x32_bf16 v[188:191], a[184:187], a[44:47], v[188:191]\n"
    "ds_read_b64_tr_b16 a[158:159], v23 offset:29696\n"
    "v_mfma_f32_16x16x32_bf16 v[188:191], a[188:191], a[60:63], v[188:191]\n"
    "s_waitcnt lgkmcnt(8)\n"
    "v_mfma_f32_16x16x32_bf16 v[192:195], a[128:131], a[0:3], v[192:195]\n"
    "ds_read_b64_tr_b16 a[160:161], v22 offset:1536\n"
    "v_mfma_f32_16x16x32_bf16 v[192:195], a[132:135], a[16:19], v[192:195]\n"
    "v_mfma_f32_16x16x32_bf16 v[192:195], a[136:139], a[32:35], v[192:195]\n"
    "ds_read_b64_tr_b16 a[162:163], v22 offset:5632\n"
    "buffer_load_dwordx4 a[72:75], v2, s[44:47], s92 offen offset:0 sc1\n"
    "v_mfma_f32_16x16x32_bf16 v[192:195], a[140:143], a[48:51], v[192:195]\n"
    "v_mfma_f32_16x16x32_bf16 v[196:199], a[128:131], a[4:7], v[196:199]\n"
    "ds_read_b64_tr_b16 a[164:165], v22 offset:9728\n"
    "v_mfma_f32_16x16x32_bf16 v[196:199], a[132:135], a[20:23], v[196:199]\n"
    "v_mfma_f32_16x16x32_bf16 v[196:199], a[136:139], a[36:39], v[196:199]\n"
    "ds_read_b64_tr_b16 a[166:167], v22 offset:13824\n"
    "v_mfma_f32_16x16x32_bf16 v[196:199], a[140:143], a[52:55], v[196:199]\n"
    "buffer_load_dwordx4 a[76:79], v2, s[44:47], s92 offen offset:1024 sc1\n"
    "v_mfma_f32_16x16x32_bf16 v[200:203], a[128:131], a[8:11], v[200:203]\n"
    "ds_read_b64_tr_b16 a[168:169], v22 offset:17920\n"
    "v_mfma_f32_16x16x32_bf16 v[200:203], a[132:135], a[24:27], v[200:203]\n"
    "v_mfma_f32_16x16x32_bf16 v[200:203], a[136:139], a[40:43], v[200:203]\n"
    "ds_read_b64_tr_b16 a[170:171], v22 offset:22016\n"
    "v_mfma_f32_16x16x32_bf16 v[200:203], a[140:143], a[56:59], v[200:203]\n"
    "v_mfma_f32_16x16x32_bf16 v[204:207], a[128:131], a[12:15], v[204:207]\n"
    "ds_read_b64_tr_b16 a[172:173], v22 offset:26112\n"
    "buffer_load_dwordx4 v6, s[48:51], s67 offen offset:1024 sc1 lds\n"
    "v_mfma_f32_16x16x32_bf16 v[204:207], a[132:135], a[28:31], v[204:207]\n"
    "v_mfma_f32_16x16x32_bf16 v[204:207], a[136:139], a[44:47], v[204:207]\n"
    "ds_read_b64_tr_b16 a[174:175], v22 offset:30208\n"
    "v_mfma_f32_16x16x32_bf16 v[204:207], a[140:143], a[60:63], v[204:207]\n"
    "s_waitcnt lgkmcnt(8)\n"
    "v_mfma_f32_16x16x32_bf16 v[208:211], a[144:147], a[0:3], v[208:211]\n"
    "ds_read_b64_tr_b16 a[176:177], v23 offset:1536\n"
    "v_mfma_f32_16x16x32_bf16 v[208:211], a[148:151], a[16:19], v[208:211]\n"
    "buffer_load_dwordx4 a[88:91], v2, s[44:47], s92 offen offset:2048 sc1\n"
    "v_mfma_f32_16x16x32_bf16 v[208:211], a[152:155], a[32:35], v[208:211]\n"
    "ds_read_b64_tr_b16 a[178:179], v23 offset:5632\n"
    "v_mfma_f32_16x16x32_bf16 v[208:211], a[156:159], a[48:51], v[208:211]\n"
    "v_mfma_f32_16x16x32_bf16 v[212:215], a[144:147], a[4:7], v[212:215]\n"
    "ds_read_b64_tr_b16 a[180:181], v23 offset:9728\n"
    "v_mfma_f32_16x16x32_bf16 v[212:215], a[148:151], a[20:23], v[212:215]\n"
    "v_mfma_f32_16x16x32_bf16 v[212:215], a[152:155], a[36:39], v[212:215]\n"
    "ds_read_b64_tr_b16 a[182:183], v23 offset:13824\n"
    "buffer_load_dwordx4 a[92:95], v2, s[44:47], s92 offen offset:3072 sc1\n"
    "v_mfma_f32_16x16x32_bf16 v[212:215], a[156:159], a[52:55], v[212:215]\n"
    "v_mfma_f32_16x16x32_bf16 v[216:219], a[144:147], a[8:11], v[216:219]\n"
    "ds_read_b64_tr_b16 a[184:185], v23 offset:17920\n"
    "v_mfma_f32_16x16x32_bf16 v[216:219], a[148:151], a[24:27], v[216:219]\n"
    "v_mfma_f32_16x16x32_bf16 v[216:219], a[152:155], a[40:43], v[216:219]\n"
    "ds_read_b64_tr_b16 a[186:187], v23 offset:22016\n"
    "v_mfma_f32_16x16x32_bf16 v[216:219], a[156:159], a[56:59], v[216:219]\n"
    "buffer_load_dwordx4 v5, s[48:51], s67 offen offset:2048 sc1 lds\n"
    "v_mfma_f32_16x16x32_bf16 v[220:223], a[144:147], a[12:15], v[220:223]\n"
    "ds_read_b64_tr_b16 a[188:189], v23 offset:26112\n"
    "v_mfma_f32_16x16x32_bf16 v[220:223], a[148:151], a[28:31], v[220:223]\n"
    "v_mfma_f32_16x16x32_bf16 v[220:223], a[152:155], a[44:47], v[220:223]\n"
    "ds_read_b64_tr_b16 a[190:191], v23 offset:30208\n"
    "v_mfma_f32_16x16x32_bf16 v[220:223], a[156:159], a[60:63], v[220:223]\n"
    "s_waitcnt lgkmcnt(8)\n"
    "v_mfma_f32_16x16x32_bf16 v[224:227], a[160:163], a[0:3], v[224:227]\n"
    "buffer_load_dwordx4 a[104:107], v2, s[44:47], s93 offen offset:0 sc1\n"
    "v_mfma_f32_16x16x32_bf16 v[224:227], a[164:167], a[16:19], v[224:227]\n"
    "v_mfma_f32_16x16x32_bf16 v[224:227], a[168:171], a[32:35], v[224:227]\n"
    "v_mfma_f32_16x16x32_bf16 v[224:227], a[172:175], a[48:51], v[224:227]\n"
    "v_mfma_f32_16x16x32_bf16 v[228:231], a[160:163], a[4:7], v[228:231]\n"
    "s_waitcnt vmcnt(22)\n"
    "s_waitcnt lgkmcnt(0)\n"
    "s_barrier\n"
    "v_mfma_f32_16x16x32_bf16 v[228:231], a[164:167], a[20:23], v[228:231]\n"
    "ds_read_b64_tr_b16 a[128:129], v22 offset:32768\n"
    "buffer_load_dwordx4 a[108:111], v2, s[44:47], s93 offen offset:1024 sc1\n"
    "v_mfma_f32_16x16x32_bf16 v[228:231], a[168:171], a[36:39], v[228:231]\n"
    "ds_read_b64_tr_b16 a[130:131], v22 offset:36864\n"
    "v_mfma_f32_16x16x32_bf16 v[228:231], a[172:175], a[52:55], v[228:231]\n"
    "v_mfma_f32_16x16x32_bf16 v[232:235], a[160:163], a[8:11], v[232:235]\n"
    "ds_read_b64_tr_b16 a[132:133], v22 offset:40960\n"
    "v_mfma_f32_16x16x32_bf16 v[232:235], a[164:167], a[24:27], v[232:235]\n"
    "ds_read_b64_tr_b16 a[134:135], v22 offset:45056\n"
    "v_mfma_f32_16x16x32_bf16 v[232:235], a[168:171], a[40:43], v[232:235]\n"
    "buffer_load_dwordx4 v6, s[48:51], s67 offen offset:3072 sc1 lds\n"
    "v_mfma_f32_16x16x32_bf16 v[232:235], a[172:175], a[56:59], v[232:235]\n"
    "ds_read_b64_tr_b16 a[136:137], v22 offset:49152\n"
    "v_mfma_f32_16x16x32_bf16 v[236:239], a[160:163], a[12:15], v[236:239]\n"
    "ds_read_b64_tr_b16 a[138:139], v22 offset:53248\n"
    "v_mfma_f32_16x16x32_bf16 v[236:239], a[164:167], a[28:31], v[236:239]\n"
    "v_mfma_f32_16x16x32_bf16 v[236:239], a[168:171], a[44:47], v[236:239]\n"
    "ds_read_b64_tr_b16 a[140:141], v22 offset:57344\n"
    "v_mfma_f32_16x16x32_bf16 v[236:239], a[172:175], a[60:63], v[236:239]\n"
    "ds_read_b64_tr_b16 a[142:143], v22 offset:61440\n"
    "buffer_load_dwordx4 a[120:123], v2, s[44:47], s93 offen offset:2048 sc1\n"
    "v_mfma_f32_16x16x32_bf16 v[240:243], a[176:179], a[0:3], v[240:243]\n"
    "v_mfma_f32_16x16x32_bf16 v[240:243], a[180:183], a[16:19], v[240:243]\n"
    "ds_read_b64_tr_b16 a[144:145], v23 offset:32768\n"
    "v_mfma_f32_16x16x32_bf16 v[240:243], a[184:187], a[32:35], v[240:243]\n"
    "ds_read_b64_tr_b16 a[146:147], v23 offset:36864\n"
    "v_mfma_f32_16x16x32_bf16 v[240:243], a[188:191], a[48:51], v[240:243]\n"
    "v_mfma_f32_16x16x32_bf16 v[244:247], a[176:179], a[4:7], v[244:247]\n"
    "ds_read_b64_tr_b16 a[148:149], v23 offset:40960\n"
    "buffer_load_dwordx4 a[124:127], v2, s[44:47], s93 offen offset:3072 sc1\n"
    "v_mfma_f32_16x16x32_bf16 v[244:247], a[180:183], a[20:23], v[244:247]\n"
    "ds_read_b64_tr_b16 a[150:151], v23 offset:45056\n"
    "v_mfma_f32_16x16x32_bf16 v[244:247], a[184:187], a[36:39], v[244:247]\n"
    "v_mfma_f32_16x16x32_bf16 v[244:247], a[188:191], a[52:55], v[244:247]\n"
    "ds_read_b64_tr_b16 a[152:153], v23 offset:49152\n"
    "v_mfma_f32_16x16x32_bf16 v[248:251], a[176:179], a[8:11], v[248:251]\n"
    "ds_read_b64_tr_b16 a[154:155], v23 offset:53248\n"
    "v_mfma_f32_16x16x32_bf16 v[248:251], a[180:183], a[24:27], v[248:251]\n"
    "s_add_u32 s67, s67, 32768\n"
    "v_mfma_f32_16x16x32_bf16 v[248:251], a[184:187], a[40:43], v[248:251]\n"
    "ds_read_b64_tr_b16 a[156:157], v23 offset:57344\n"
    "s_add_u32 s90, s90, 8192\n"
    "v_mfma_f32_16x16x32_bf16 v[248:251], a[188:191], a[56:59], v[248:251]\n"
    "ds_read_b64_tr_b16 a[158:159], v23 offset:61440\n"
    "s_add_u32 s91, s91, 8192\n"
    "v_mfma_f32_16x16x32_bf16 v[252:255], a[176:179], a[12:15], v[252:255]\n"
    "s_add_u32 s92, s92, 8192\n"
    "v_mfma_f32_16x16x32_bf16 v[252:255], a[180:183], a[28:31], v[252:255]\n"
    "s_add_u32 s93, s93, 8192\n"
    "v_mfma_f32_16x16x32_bf16 v[252:255], a[184:187], a[44:47], v[252:255]\n"
    "v_mfma_f32_16x16x32_bf16 v[252:255], a[188:191], a[60:63], v[252:255]\n"
    "s_waitcnt lgkmcnt(8)\n"
    "s_waitcnt vmcnt(24)\n"
    "s_add_u32 s37, s37, 1\n"
    "s_cbranch_scc1 .Lk3_fin\n"
    "s_branch .Lk3_ev0\n"
    ".Lk3_ev0_ret:\n"
    "v_mfma_f32_16x16x32_bf16 v[128:131], a[128:131], a[192:195], v[128:131]\n"
    "ds_read_b64_tr_b16 a[160:161], v22 offset:33280\n"
    "s_add_u32 m0, s78, 0x0\n"
    "v_mfma_f32_16x16x32_bf16 v[128:131], a[132:135], a[208:211], v[128:131]\n"
    "buffer_load_dwordx4 v3, s[48:51], s67 offen offset:0 sc1 lds\n"
    "v_mfma_f32_16x16x32_bf16 v[128:131], a[136:139], a[224:227], v[128:131]\n"
    "ds_read_b64_tr_b16 a[162:163], v22 offset:37376\n"
    "v_mfma_f32_16x16x32_bf16 v[128:131], a[140:143], a[240:243], v[128:131]\n"
    "v_mfma_f32_16x16x32_bf16 v[132:135], a[128:131], a[196:199], v[132:135]\n"
    "ds_read_b64_tr_b16 a[164:165], v22 offset:41472\n"
    "v_mfma_f32_16x16x32_bf16 v[132:135], a[132:135], a[212:215], v[132:135]\n"
    "v_mfma_f32_16x16x32_bf16 v[132:135], a[136:139], a[228:231], v[132:135]\n"
    "ds_read_b64_tr_b16 a[166:167], v22 offset:45568\n"
    "buffer_load_dwordx4 a[0:3], v2, s[44:47], s90 offen offset:0 sc1\n"
    "v_mfma_f32_16x16x32_bf16 v[132:135], a[140:143], a[244:247], v[132:135]\n"
    "v_mfma_f32_16x16x32_bf16 v[136:139], a[128:131], a[200:203], v[136:139]\n"
    "ds_read_b64_tr_b16 a[168:169], v22 offset:49664\n"
    "v_mfma_f32_16x16x32_bf16 v[136:139], a[132:135], a[216:219], v[136:139]\n"
    "v_mfma_f32_16x16x32_bf16 v[136:139], a[136:139], a[232:235], v[136:139]\n"
    "ds_read_b64_tr_b16 a[170:171], v22 offset:53760\n"
    "v_mfma_f32_16x16x32_bf16 v[136:139], a[140:143], a[248:251], v[136:139]\n"
    "buffer_load_dwordx4 a[4:7], v2, s[44:47], s90 offen offset:1024 sc1\n"
    "v_mfma_f32_16x16x32_bf16 v[140:143], a[128:131], a[204:207], v[140:143]\n"
    "ds_read_b64_tr_b16 a[172:173], v22 offset:57856\n"
    "v_mfma_f32_16x16x32_bf16 v[140:143], a[132:135], a[220:223], v[140:143]\n"
    "v_mfma_f32_16x16x32_bf16 v[140:143], a[136:139], a[236:239], v[140:143]\n"
    "ds_read_b64_tr_b16 a[174:175], v22 offset:61952\n"
    "v_mfma_f32_16x16x32_bf16 v[140:143], a[140:143], a[252:255], v[140:143]\n"
    "s_waitcnt lgkmcnt(8)\n"
    "v_mfma_f32_16x16x32_bf16 v[144:147], a[144:147], a[192:195], v[144:147]\n"
    "ds_read_b64_tr_b16 a[176:177], v23 offset:33280\n"
    "buffer_load_dwordx4 v4, s[48:51], s67 offen offset:1024 sc1 lds\n"
    "v_mfma_f32_16x16x32_bf16 v[144:147], a[148:151], a[208:211], v[144:147]\n"
    "v_mfma_f32_16x16x32_bf16 v[144:147], a[152:155], a[224:227], v[144:147]\n"
    "ds_read_b64_tr_b16 a[178:179], v23 offset:37376\n"
    "v_mfma_f32_16x16x32_bf16 v[144:147], a[156:159], a[240:243], v[144:147]\n"
    "v_mfma_f32_16x16x32_bf16 v[148:151], a[144:147], a[196:199], v[148:151]\n"
    "ds_read_b64_tr_b16 a[180:181], v23 offset:41472\n"
    "v_mfma_f32_16x16x32_bf16 v[148:151], a[148:151], a[212:215], v[148:151]\n"
    "buffer_load_dwordx4 a[16:19], v2, s[44:47], s90 offen offset:2048 sc1\n"
    "v_mfma_f32_16x16x32_bf16 v[148:151], a[152:155], a[228:231], v[148:151]\n"
    "ds_read_b64_tr_b16 a[182:183], v23 offset:45568\n"
    "v_mfma_f32_16x16x32_bf16 v[148:151], a[156:159], a[244:247], v[148:151]\n"
    "v_mfma_f32_16x16x32_bf16 v[152:155], a[144:147], a[200:203], v[152:155]\n"
    "ds_read_b64_tr_b16 a[184:185], v23 offset:49664\n"
    "v_mfma_f32_16x16x32_bf16 v[152:155], a[148:151], a[216:219], v[152:155]\n"
    "v_mfma_f32_16x16x32_bf16 v[152:155], a[152:155], a[232:235], v[152:155]\n"
    "ds_read_b64_tr_b16 a[186:187], v23 offset:53760\n"
    "buffer_load_dwordx4 a[20:23], v2, s[44:47], s90 offen offset:3072 sc1\n"
    "v_mfma_f32_16x16x32_bf16 v[152:155], a[156:159], a[248:251], v[152:155]\n"
    "v_mfma_f32_16x16x32_bf16 v[156:159], a[144:147], a[204:207], v[156:159]\n"
    "ds_read_b64_tr_b16 a[188:189], v23 offset:57856\n"
    "v_mfma_f32_16x16x32_bf16 v[156:159], a[148:151], a[220:223], v[156:159]\n"
    "v_mfma_f32_16x16x32_bf16 v[156:159], a[152:155], a[236:239], v[156:159]\n"
    "ds_read_b64_tr_b16 a[190:191], v23 offset:61952\n"
    "v_mfma_f32_16x16x32_bf16 v[156:159], a[156:159], a[252:255], v[156:159]\n"
    "s_waitcnt lgkmcnt(8)\n"
    "buffer_load_dwordx4 v3, s[48:51], s67 offen offset:2048 sc1 lds\n"
    "v_mfma_f32_16x16x32_bf16 v[160:163], a[160:163], a[192:195], v[160:163]\n"
    "ds_read_b64_tr_b16 a[128:129], v22 offset:33792\n"
    "v_mfma_f32_16x16x32_bf16 v[160:163], a[164:167], a[208:211], v[160:163]\n"
    "v_mfma_f32_16x16x32_bf16 v[160:163], a[168:171], a[224:227], v[160:163]\n"
    "ds_read_b64_tr_b16 a[130:131], v22 offset:37888\n"
    "v_mfma_f32_16x16x32_bf16 v[160:163], a[172:175], a[240:243], v[160:163]\n"
    "v_mfma_f32_16x16x32_bf16 v[164:167], a[160:163], a[196:199], v[164:167]\n"
    "ds_read_b64_tr_b16 a[132:133], v22 offset:41984\n"
    "buffer_load_dwordx4 a[32:35], v2, s[44:47], s91 offen offset:0 sc1\n"
    "v_mfma_f32_16x16x32_bf16 v[164:167], a[164:167], a[212:215], v[164:167]\n"
    "v_mfma_f32_16x16x32_bf16 v[164:167], a[168:171], a[228:231], v[164:167]\n"
    "ds_read_b64_tr_b16 a[134:135], v22 offset:46080\n"
    "v_mfma_f32_16x16x32_bf16 v[164:167], a[172:175], a[244:247], v[164:167]\n"
    "v_mfma_f32_16x16x32_bf16 v[168:171], a[160:163], a[200:203], v[168:171]\n"
    "ds_read_b64_tr_b16 a[136:137], v22 offset:50176\n"
    "v_mfma_f32_16x16x32_bf16 v[168:171], a[164:167], a[216:219], v[168:171]\n"
    "buffer_load_dwordx4 a[36:39], v2, s[44:47], s91 offen offset:1024 sc1\n"
    "v_mfma_f32_16x16x32_bf16 v[168:171], a[168:171], a[232:235], v[168:171]\n"
    "ds_read_b64_tr_b16 a[138:139], v22 offset:54272\n"
    "v_mfma_f32_16x16x32_bf16 v[168:171], a[172:175], a[248:251], v[168:171]\n"
    "v_mfma_f32_16x16x32_bf16 v[172:175], a[160:163], a[204:207], v[172:175]\n"
    "ds_read_b64_tr_b16 a[140:141], v22 offset:58368\n"
    "v_mfma_f32_16x16x32_bf16 v[172:175], a[164:167], a[220:223], v[172:175]\n"
    "v_mfma_f32_16x16x32_bf16 v[172:175], a[168:171], a[236:239], v[172:175]\n"
    "ds_read_b64_tr_b16 a[142:143], v22 offset:62464\n"
    "buffer_load_dwordx4 v4, s[48:51], s67 offen offset:3072 sc1 lds\n"
    "v_mfma_f32_16x16x32_bf16 v[172:175], a[172:175], a[252:255], v[172:175]\n"
    "s_waitcnt lgkmcnt(8)\n"
    "v_mfma_f32_16x16x32_bf16 v[176:179], a[176:179], a[192:195], v[176:179]\n"
    "ds_read_b64_tr_b16 a[144:145], v23 offset:33792\n"
    "v_mfma_f32_16x16x32_bf16 v[176:179], a[180:183], a[208:211], v[176:179]\n"
    "v_mfma_f32_16x16x32_bf16 v[176:179], a[184:187], a[224:227], v[176:179]\n"
    "ds_read_b64_tr_b16 a[146:147], v23 offset:37888\n"
    "v_mfma_f32_16x16x32_bf16 v[176:179], a[188:191], a[240:243], v[176:179]\n"
    "buffer_load_dwordx4 a[48:51], v2, s[44:47], s91 offen offset:2048 sc1\n"
    "v_mfma_f32_16x16x32_bf16 v[180:183], a[176:179], a[196:199], v[180:183]\n"
    "ds_read_b64_tr_b16 a[148:149], v23 offset:41984\n"
    "v_mfma_f32_16x16x32_bf16 v[180:183], a[180:183], a[212:215], v[180:183]\n"
    "v_mfma_f32_16x16x32_bf16 v[180:183], a[184:187], a[228:231], v[180:183]\n"
    "ds_read_b64_tr_b16 a[150:151], v23 offset:46080\n"
    "v_mfma_f32_16x16x32_bf16 v[180:183], a[188:191], a[244:247], v[180:183]\n"
    "v_mfma_f32_16x16x32_bf16 v[184:187], a[176:179], a[200:203], v[184:187]\n"
    "ds_read_b64_tr_b16 a[152:153], v23 offset:50176\n"
    "buffer_load_dwordx4 a[52:55], v2, s[44:47], s91 offen offset:3072 sc1\n"
    "v_mfma_f32_16x16x32_bf16 v[184:187], a[180:183], a[216:219], v[184:187]\n"
    "v_mfma_f32_16x16x32_bf16 v[184:187], a[184:187], a[232:235], v[184:187]\n"
    "ds_read_b64_tr_b16 a[154:155], v23 offset:54272\n"
    "v_mfma_f32_16x16x32_bf16 v[184:187], a[188:191], a[248:251], v[184:187]\n"
    "s_add_u32 m0, s78, 0x1000\n"
    "v_mfma_f32_16x16x32_bf16 v[188:191], a[176:179], a[204:207], v[188:191]\n"
    "ds_read_b64_tr_b16 a[156:157], v23 offset:58368\n"
    "v_mfma_f32_16x16x32_bf16 v[188:191], a[180:183], a[220:223], v[188:191]\n"
    "buffer_load_dwordx4 v5, s[48:51], s67 offen offset:0 sc1 lds\n"
    "v_mfma_f32_16x16x32_bf16 v[188:191], a[184:187], a[236:239], v[188:191]\n"
    "ds_read_b64_tr_b16 a[158:159], v23 offset:62464\n"
    "v_mfma_f32_16x16x32_bf16 v[188:191], a[188:191], a[252:255], v[188:191]\n"
    "s_waitcnt lgkmcnt(8)\n"
    "v_mfma_f32_16x16x32_bf16 v[192:195], a[128:131], a[192:195], v[192:195]\n"
    "ds_read_b64_tr_b16 a[160:161], v22 offset:34304\n"
    "v_mfma_f32_16x16x32_bf16 v[192:195], a[132:135], a[208:211], v[192:195]\n"
    "v_mfma_f32_16x16x32_bf16 v[192:195], a[136:139], a[224:227], v[192:195]\n"
    "ds_read_b64_tr_b16 a[162:163], v22 offset:38400\n"
    "buffer_load_dwordx4 a[8:11], v2, s[44:47], s92 offen offset:0 sc1\n"
    "v_mfma_f32_16x16x32_bf16 v[192:195], a[140:143], a[240:243], v[192:195]\n"
    "v_mfma_f32_16x16x32_bf16 v[196:199], a[128:131], a[196:199], v[196:199]\n"
    "ds_read_b64_tr_b16 a[164:165], v22 offset:42496\n"
    "v_mfma_f32_16x16x32_bf16 v[196:199], a[132:135], a[212:215], v[196:199]\n"
    "v_mfma_f32_16x16x32_bf16 v[196:199], a[136:139], a[228:231], v[196:199]\n"
    "ds_read_b64_tr_b16 a[166:167], v22 offset:46592\n"
    "v_mfma_f32_16x16x32_bf16 v[196:199], a[140:143], a[244:247], v[196:199]\n"
    "buffer_load_dwordx4 a[12:15], v2, s[44:47], s92 offen offset:1024 sc1\n"
    "v_mfma_f32_16x16x32_bf16 v[200:203], a[128:131], a[200:203], v[200:203]\n"
    "ds_read_b64_tr_b16 a[168:169], v22 offset:50688\n"
    "v_mfma_f32_16x16x32_bf16 v[200:203], a[132:135], a[216:219], v[200:203]\n"
    "v_mfma_f32_16x16x32_bf16 v[200:203], a[136:139], a[232:235], v[200:203]\n"
    "ds_read_b64_tr_b16 a[170:171], v22 offset:54784\n"
    "v_mfma_f32_16x16x32_bf16 v[200:203], a[140:143], a[248:251], v[200:203]\n"
    "v_mfma_f32_16x16x32_bf16 v[204:207], a[128:131], a[204:207], v[204:207]\n"
    "ds_read_b64_tr_b16 a[172:173], v22 offset:58880\n"
    "buffer_load_dwordx4 v6, s[48:51], s67 offen offset:1024 sc1 lds\n"
    "v_mfma_f32_16x16x32_bf16 v[204:207], a[132:135], a[220:223], v[204:207]\n"
    "v_mfma_f32_16x16x32_bf16 v[204:207], a[136:139], a[236:239], v[204:207]\n"
    "ds_read_b64_tr_b16 a[174:175], v22 offset:62976\n"
    "v_mfma_f32_16x16x32_bf16 v[204:207], a[140:143], a[252:255], v[204:207]\n"
    "s_waitcnt lgkmcnt(8)\n"
    "v_mfma_f32_16x16x32_bf16 v[208:211], a[144:147], a[192:195], v[208:211]\n"
    "ds_read_b64_tr_b16 a[176:177], v23 offset:34304\n"
    "v_mfma_f32_16x16x32_bf16 v[208:211], a[148:151], a[208:211], v[208:211]\n"
    "buffer_load_dwordx4 a[24:27], v2, s[44:47], s92 offen offset:2048 sc1\n"
    "v_mfma_f32_16x16x32_bf16 v[208:211], a[152:155], a[224:227], v[208:211]\n"
    "ds_read_b64_tr_b16 a[178:179], v23 offset:38400\n"
    "v_mfma_f32_16x16x32_bf16 v[208:211], a[156:159], a[240:243], v[208:211]\n"
    "v_mfma_f32_16x16x32_bf16 v[212:215], a[144:147], a[196:199], v[212:215]\n"
    "ds_read_b64_tr_b16 a[180:181], v23 offset:42496\n"
    "v_mfma_f32_16x16x32_bf16 v[212:215], a[148:151], a[212:215], v[212:215]\n"
    "v_mfma_f32_16x16x32_bf16 v[212:215], a[152:155], a[228:231], v[212:215]\n"
    "ds_read_b64_tr_b16 a[182:183], v23 offset:46592\n"
    "buffer_load_dwordx4 a[28:31], v2, s[44:47], s92 offen offset:3072 sc1\n"
    "v_mfma_f32_16x16x32_bf16 v[212:215], a[156:159], a[244:247], v[212:215]\n"
    "v_mfma_f32_16x16x32_bf16 v[216:219], a[144:147], a[200:203], v[216:219]\n"
    "ds_read_b64_tr_b16 a[184:185], v23 offset:50688\n"
    "v_mfma_f32_16x16x32_bf16 v[216:219], a[148:151], a[216:219], v[216:219]\n"
    "v_mfma_f32_16x16x32_bf16 v[216:219], a[152:155], a[232:235], v[216:219]\n"
    "ds_read_b64_tr_b16 a[186:187], v23 offset:54784\n"
    "v_mfma_f32_16x16x32_bf16 v[216:219], a[156:159], a[248:251], v[216:219]\n"
    "buffer_load_dwordx4 v5, s[48:51], s67 offen offset:2048 sc1 lds\n"
    "v_mfma_f32_16x16x32_bf16 v[220:223], a[144:147], a[204:207], v[220:223]\n"
    "ds_read_b64_tr_b16 a[188:189], v23 offset:58880\n"
    "v_mfma_f32_16x16x32_bf16 v[220:223], a[148:151], a[220:223], v[220:223]\n"
    "v_mfma_f32_16x16x32_bf16 v[220:223], a[152:155], a[236:239], v[220:223]\n"
    "ds_read_b64_tr_b16 a[190:191], v23 offset:62976\n"
    "v_mfma_f32_16x16x32_bf16 v[220:223], a[156:159], a[252:255], v[220:223]\n"
    "s_waitcnt lgkmcnt(8)\n"
    "v_mfma_f32_16x16x32_bf16 v[224:227], a[160:163], a[192:195], v[224:227]\n"
    "buffer_load_dwordx4 a[40:43], v2, s[44:47], s93 offen offset:0 sc1\n"
    "v_mfma_f32_16x16x32_bf16 v[224:227], a[164:167], a[208:211], v[224:227]\n"
    "v_mfma_f32_16x16x32_bf16 v[224:227], a[168:171], a[224:227], v[224:227]\n"
    "v_mfma_f32_16x16x32_bf16 v[224:227], a[172:175], a[240:243], v[224:227]\n"
    "v_mfma_f32_16x16x32_bf16 v[228:231], a[160:163], a[196:199], v[228:231]\n"
    "s_cmp_eq_u32 s43, s95\n"
    "s_cbranch_scc1 .Lk3_chkO\n"
    ".Lk3_chkO_ret:\n"
    "s_waitcnt vmcnt(22)\n"
    "s_waitcnt lgkmcnt(0)\n"
    "s_barrier\n"
    "v_mfma_f32_16x16x32_bf16 v[228:231], a[164:167], a[212:215], v[228:231]\n"
    "ds_read_b64_tr_b16 a[128:129], v24 offset:0\n"
    "buffer_load_dwordx4 a[44:47], v2, s[44:47], s93 offen offset:1024 sc1\n"
    "v_mfma_f32_16x16x32_bf16 v[228:231], a[168:171], a[228:231], v[228:231]\n"
    "ds_read_b64_tr_b16 a[130:131], v24 offset:4096\n"
    "v_mfma_f32_16x16x32_bf16 v[228:231], a[172:175], a[244:247], v[228:231]\n"
    "v_mfma_f32_16x16x32_bf16 v[232:235], a[160:163], a[200:203], v[232:235]\n"
    "ds_read_b64_tr_b16 a[132:133], v24 offset:8192\n"
    "v_mfma_f32_16x16x32_bf16 v[232:235], a[164:167], a[216:219], v[232:235]\n"
    "ds_read_b64_tr_b16 a[134:135], v24 offset:12288\n"
    "v_mfma_f32_16x16x32_bf16 v[232:235], a[168:171], a[232:235], v[232:235]\n"
    "buffer_load_dwordx4 v6, s[48:51], s67 offen offset:3072 sc1 lds\n"
    "v_mfma_f32_16x16x32_bf16 v[232:235], a[172:175], a[248:251], v[232:235]\n"
    "ds_read_b64_tr_b16 a[136:137], v24 offset:16384\n"
    "v_mfma_f32_16x16x32_bf16 v[236:239], a[160:163], a[204:207], v[236:239]\n"
    "ds_read_b64_tr_b16 a[138:139], v24 offset:20480\n"
    "v_mfma_f32_16x16x32_bf16 v[236:239], a[164:167], a[220:223], v[236:239]\n"
    "v_mfma_f32_16x16x32_bf16 v[236:239], a[168:171], a[236:239], v[236:239]\n"
    "ds_read_b64_tr_b16 a[140:141], v24 offset:24576\n"
    "v_mfma_f32_16x16x32_bf16 v[236:239], a[172:175], a[252:255], v[236:239]\n"
    "ds_read_b64_tr_b16 a[142:143], v24 offset:28672\n"
    "buffer_load_dwordx4 a[56:59], v2, s[44:47], s93 offen offset:2048 sc1\n"
    "v_mfma_f32_16x16x32_bf16 v[240:243], a[176:179], a[192:195], v[240:243]\n"
    "v_mfma_f32_16x16x32_bf16 v[240:243], a[180:183], a[208:211], v[240:243]\n"
    "ds_read_b64_tr_b16 a[144:145], v25 offset:0\n"
    "v_mfma_f32_16x16x32_bf16 v[240:243], a[184:187], a[224:227], v[240:243]\n"
    "ds_read_b64_tr_b16 a[146:147], v25 offset:4096\n"
    "v_mfma_f32_16x16x32_bf16 v[240:243], a[188:191], a[240:243], v[240:243]\n"
    "v_mfma_f32_16x16x32_bf16 v[244:247], a[176:179], a[196:199], v[244:247]\n"
    "ds_read_b64_tr_b16 a[148:149], v25 offset:8192\n"
    "buffer_load_dwordx4 a[60:63], v2, s[44:47], s93 offen offset:3072 sc1\n"
    "v_mfma_f32_16x16x32_bf16 v[244:247], a[180:183], a[212:215], v[244:247]\n"
    "ds_read_b64_tr_b16 a[150:151], v25 offset:12288\n"
    "v_mfma_f32_16x16x32_bf16 v[244:247], a[184:187], a[228:231], v[244:247]\n"
    "v_mfma_f32_16x16x32_bf16 v[244:247], a[188:191], a[244:247], v[244:247]\n"
    "ds_read_b64_tr_b16 a[152:153], v25 offset:16384\n"
    "v_mfma_f32_16x16x32_bf16 v[248:251], a[176:179], a[200:203], v[248:251]\n"
    "ds_read_b64_tr_b16 a[154:155], v25 offset:20480\n"
    "v_mfma_f32_16x16x32_bf16 v[248:251], a[180:183], a[216:219], v[248:251]\n"
    "s_add_u32 s67, s67, 32768\n"
    "v_mfma_f32_16x16x32_bf16 v[248:251], a[184:187], a[232:235], v[248:251]\n"
    "ds_read_b64_tr_b16 a[156:157], v25 offset:24576\n"
    "s_add_u32 s90, s90, 8192\n"
    "v_mfma_f32_16x16x32_bf16 v[248:251], a[188:191], a[248:251], v[248:251]\n"
    "ds_read_b64_tr_b16 a[158:159], v25 offset:28672\n"
    "s_add_u32 s91, s91, 8192\n"
    "v_mfma_f32_16x16x32_bf16 v[252:255], a[176:179], a[204:207], v[252:255]\n"
    "s_add_u32 s92, s92, 8192\n"
    "v_mfma_f32_16x16x32_bf16 v[252:255], a[180:183], a[220:223], v[252:255]\n"
    "s_add_u32 s93, s93, 8192\n"
    "v_mfma_f32_16x16x32_bf16 v[252:255], a[184:187], a[236:239], v[252:255]\n"
    "v_mfma_f32_16x16x32_bf16 v[252:255], a[188:191], a[252:255], v[252:255]\n"
    "s_waitcnt lgkmcnt(8)\n"
    "s_waitcnt vmcnt(24)\n"
    "s_add_u32 s37, s37, 1\n"
    "s_cbranch_scc1 .Lk3_fin\n"
    "s_cmp_eq_u32 s43, 1\n"
    "s_cbranch_scc1 .Lk3_sw\n"
    ".Lk3_sw_ret:\n"
    "v_mfma_f32_16x16x32_bf16 v[128:131], a[128:131], a[64:67], v[128:131]\n"
    "ds_read_b64_tr_b16 a[160:161], v24 offset:512\n"
    "s_add_u32 m0, s78, 0x8000\n"
    "v_mfma_f32_16x16x32_bf16 v[128:131], a[132:135], a[80:83], v[128:131]\n"
    "buffer_load_dwordx4 v3, s[48:51], s67 offen offset:0 sc1 lds\n"
    "v_mfma_f32_16x16x32_bf16 v[128:131], a[136:139], a[96:99], v[128:131]\n"
    "ds_read_b64_tr_b16 a[162:163], v24 offset:4608\n"
    "v_mfma_f32_16x16x32_bf16 v[128:131], a[140:143], a[112:115], v[128:131]\n"
    "v_mfma_f32_16x16x32_bf16 v[132:135], a[128:131], a[68:71], v[132:135]\n"
    "ds_read_b64_tr_b16 a[164:165], v24 offset:8704\n"
    "v_mfma_f32_16x16x32_bf16 v[132:135], a[132:135], a[84:87], v[132:135]\n"
    "v_mfma_f32_16x16x32_bf16 v[132:135], a[136:139], a[100:103], v[132:135]\n"
    "ds_read_b64_tr_b16 a[166:167], v24 offset:12800\n"
    "buffer_load_dwordx4 a[192:195], v2, s[44:47], s90 offen offset:0 sc1\n"
    "v_mfma_f32_16x16x32_bf16 v[132:135], a[140:143], a[116:119], v[132:135]\n"
    "v_mfma_f32_16x16x32_bf16 v[136:139], a[128:131], a[72:75], v[136:139]\n"
    "ds_read_b64_tr_b16 a[168:169], v24 offset:16896\n"
    "v_mfma_f32_16x16x32_bf16 v[136:139], a[132:135], a[88:91], v[136:139]\n"
    "v_mfma_f32_16x16x32_bf16 v[136:139], a[136:139], a[104:107], v[136:139]\n"
    "ds_read_b64_tr_b16 a[170:171], v24 offset:20992\n"
    "v_mfma_f32_16x16x32_bf16 v[136:139], a[140:143], a[120:123], v[136:139]\n"
    "buffer_load_dwordx4 a[196:199], v2, s[44:47], s90 offen offset:1024 sc1\n"
    "v_mfma_f32_16x16x32_bf16 v[140:143], a[128:131], a[76:79], v[140:143]\n"
    "ds_read_b64_tr_b16 a[172:173], v24 offset:25088\n"
    "v_mfma_f32_16x16x32_bf16 v[140:143], a[132:135], a[92:95], v[140:143]\n"
    "v_mfma_f32_16x16x32_bf16 v[140:143], a[136:139], a[108:111], v[140:143]\n"
    "ds_read_b64_tr_b16 a[174:175], v24 offset:29184\n"
    "v_mfma_f32_16x16x32_bf16 v[140:143], a[140:143], a[124:127], v[140:143]\n"
    "s_waitcnt lgkmcnt(8)\n"
    "v_mfma_f32_16x16x32_bf16 v[144:147], a[144:147], a[64:67], v[144:147]\n"
    "ds_read_b64_tr_b16 a[176:177], v25 offset:512\n"
    "buffer_load_dwordx4 v4, s[48:51], s67 offen offset:1024 sc1 lds\n"
    "v_mfma_f32_16x16x32_bf16 v[144:147], a[148:151], a[80:83], v[144:147]\n"
    "v_mfma_f32_16x16x32_bf16 v[144:147], a[152:155], a[96:99], v[144:147]\n"
    "ds_read_b64_tr_b16 a[178:179], v25 offset:4608\n"
    "v_mfma_f32_16x16x32_bf16 v[144:147], a[156:159], a[112:115], v[144:147]\n"
    "v_mfma_f32_16x16x32_bf16 v[148:151], a[144:147], a[68:71], v[148:151]\n"
    "ds_read_b64_tr_b16 a[180:181], v25 offset:8704\n"
    "v_mfma_f32_16x16x32_bf16 v[148:151], a[148:151], a[84:87], v[148:151]\n"
    "buffer_load_dwordx4 a[208:211], v2, s[44:47], s90 offen offset:2048 sc1\n"
    "v_mfma_f32_16x16x32_bf16 v[148:151], a[152:155], a[100:103], v[148:151]\n"
    "ds_read_b64_tr_b16 a[182:183], v25 offset:12800\n"
    "v_mfma_f32_16x16x32_bf16 v[148:151], a[156:159], a[116:119], v[148:151]\n"
    "v_mfma_f32_16x16x32_bf16 v[152:155], a[144:147], a[72:75], v[152:155]\n"
    "ds_read_b64_tr_b16 a[184:185], v25 offset:16896\n"
    "v_mfma_f32_16x16x32_bf16 v[152:155], a[148:151], a[88:91], v[152:155]\n"
    "v_mfma_f32_16x16x32_bf16 v[152:155], a[152:155], a[104:107], v[152:155]\n"
    "ds_read_b64_tr_b16 a[186:187], v25 offset:20992\n"
    "buffer_load_dwordx4 a[212:215], v2, s[44:47], s90 offen offset:3072 sc1\n"
    "v_mfma_f32_16x16x32_bf16 v[152:155], a[156:159], a[120:123], v[152:155]\n"
    "v_mfma_f32_16x16x32_bf16 v[156:159], a[144:147], a[76:79], v[156:159]\n"
    "ds_read_b64_tr_b16 a[188:189], v25 offset:25088\n"
    "v_mfma_f32_16x16x32_bf16 v[156:159], a[148:151], a[92:95], v[156:159]\n"
    "v_mfma_f32_16x16x32_bf16 v[156:159], a[152:155], a[108:111], v[156:159]\n"
    "ds_read_b64_tr_b16 a[190:191], v25 offset:29184\n"
    "v_mfma_f32_16x16x32_bf16 v[156:159], a[156:159], a[124:127], v[156:159]\n"
    "s_waitcnt lgkmcnt(8)\n"
    "buffer_load_dwordx4 v3, s[48:51], s67 offen offset:2048 sc1 lds\n"
    "v_mfma_f32_16x16x32_bf16 v[160:163], a[160:163], a[64:67], v[160:163]\n"
    "ds_read_b64_tr_b16 a[128:129], v24 offset:1024\n"
    "v_mfma_f32_16x16x32_bf16 v[160:163], a[164:167], a[80:83], v[160:163]\n"
    "v_mfma_f32_16x16x32_bf16 v[160:163], a[168:171], a[96:99], v[160:163]\n"
    "ds_read_b64_tr_b16 a[130:131], v24 offset:5120\n"
    "v_mfma_f32_16x16x32_bf16 v[160:163], a[172:175], a[112:115], v[160:163]\n"
    "v_mfma_f32_16x16x32_bf16 v[164:167], a[160:163], a[68:71], v[164:167]\n"
    "ds_read_b64_tr_b16 a[132:133], v24 offset:9216\n"
    "buffer_load_dwordx4 a[224:227], v2, s[44:47], s91 offen offset:0 sc1\n"
    "v_mfma_f32_16x16x32_bf16 v[164:167], a[164:167], a[84:87], v[164:167]\n"
    "v_mfma_f32_16x16x32_bf16 v[164:167], a[168:171], a[100:103], v[164:167]\n"
    "ds_read_b64_tr_b16 a[134:135], v24 offset:13312\n"
    "v_mfma_f32_16x16x32_bf16 v[164:167], a[172:175], a[116:119], v[164:167]\n"
    "v_mfma_f32_16x16x32_bf16 v[168:171], a[160:163], a[72:75], v[168:171]\n"
    "ds_read_b64_tr_b16 a[136:137], v24 offset:17408\n"
    "v_mfma_f32_16x16x32_bf16 v[168:171], a[164:167], a[88:91], v[168:171]\n"
    "buffer_load_dwordx4 a[228:231], v2, s[44:47], s91 offen offset:1024 sc1\n"
    "v_mfma_f32_16x16x32_bf16 v[168:171], a[168:171], a[104:107], v[168:171]\n"
    "ds_read_b64_tr_b16 a[138:139], v24 offset:21504\n"
    "v_mfma_f32_16x16x32_bf16 v[168:171], a[172:175], a[120:123], v[168:171]\n"
    "v_mfma_f32_16x16x32_bf16 v[172:175], a[160:163], a[76:79], v[172:175]\n"
    "ds_read_b64_tr_b16 a[140:141], v24 offset:25600\n"
    "v_mfma_f32_16x16x32_bf16 v[172:175], a[164:167], a[92:95], v[172:175]\n"
    "v_mfma_f32_16x16x32_bf16 v[172:175], a[168:171], a[108:111], v[172:175]\n"
    "ds_read_b64_tr_b16 a[142:143], v24 offset:29696\n"
    "buffer_load_dwordx4 v4, s[48:51], s67 offen offset:3072 sc1 lds\n"
    "v_mfma_f32_16x16x32_bf16 v[172:175], a[172:175], a[124:127], v[172:175]\n"
    "s_waitcnt lgkmcnt(8)\n"
    "v_mfma_f32_16x16x32_bf16 v[176:179], a[176:179], a[64:67], v[176:179]\n"
    "ds_read_b64_tr_b16 a[144:145], v25 offset:1024\n"
    "v_mfma_f32_16x16x32_bf16 v[176:179], a[180:183], a[80:83], v[176:179]\n"
    "v_mfma_f32_16x16x32_bf16 v[176:179], a[184:187], a[96:99], v[176:179]\n"
    "ds_read_b64_tr_b16 a[146:147], v25 offset:5120\n"
    "v_mfma_f32_16x16x32_bf16 v[176:179], a[188:191], a[112:115], v[176:179]\n"
    "buffer_load_dwordx4 a[240:243], v2, s[44:47], s91 offen offset:2048 sc1\n"
    "v_mfma_f32_16x16x32_bf16 v[180:183], a[176:179], a[68:71], v[180:183]\n"
    "ds_read_b64_tr_b16 a[148:149], v25 offset:9216\n"
    "v_mfma_f32_16x16x32_bf16 v[180:183], a[180:183], a[84:87], v[180:183]\n"
    "v_mfma_f32_16x16x32_bf16 v[180:183], a[184:187], a[100:103], v[180:183]\n"
    "ds_read_b64_tr_b16 a[150:151], v25 offset:13312\n"
    "v_mfma_f32_16x16x32_bf16 v[180:183], a[188:191], a[116:119], v[180:183]\n"
    "v_mfma_f32_16x16x32_bf16 v[184:187], a[176:179], a[72:75], v[184:187]\n"
    "ds_read_b64_tr_b16 a[152:153], v25 offset:17408\n"
    "buffer_load_dwordx4 a[244:247], v2, s[44:47], s91 offen offset:3072 sc1\n"
    "v_mfma_f32_16x16x32_bf16 v[184:187], a[180:183], a[88:91], v[184:187]\n"
    "v_mfma_f32_16x16x32_bf16 v[184:187], a[184:187], a[104:107], v[184:187]\n"
    "ds_read_b64_tr_b16 a[154:155], v25 offset:21504\n"
    "v_mfma_f32_16x16x32_bf16 v[184:187], a[188:191], a[120:123], v[184:187]\n"
    "s_add_u32 m0, s78, 0x9000\n"
    "v_mfma_f32_16x16x32_bf16 v[188:191], a[176:179], a[76:79], v[188:191]\n"
    "ds_read_b64_tr_b16 a[156:157], v25 offset:25600\n"
    "v_mfma_f32_16x16x32_bf16 v[188:191], a[180:183], a[92:95], v[188:191]\n"
    "buffer_load_dwordx4 v5, s[48:51], s67 offen offset:0 sc1 lds\n"
    "v_mfma_f32_16x16x32_bf16 v[188:191], a[184:187], a[108:111], v[188:191]\n"
    "ds_read_b64_tr_b16 a[158:159], v25 offset:29696\n"
    "v_mfma_f32_16x16x32_bf16 v[188:191], a[188:191], a[124:127], v[188:191]\n"
    "s_waitcnt lgkmcnt(8)\n"
    "v_mfma_f32_16x16x32_bf16 v[192:195], a[128:131], a[64:67], v[192:195]\n"
    "ds_read_b64_tr_b16 a[160:161], v24 offset:1536\n"
    "v_mfma_f32_16x16x32_bf16 v[192:195], a[132:135], a[80:83], v[192:195]\n"
    "v_mfma_f32_16x16x32_bf16 v[192:195], a[136:139], a[96:99], v[192:195]\n"
    "ds_read_b64_tr_b16 a[162:163], v24 offset:5632\n"
    "buffer_load_dwordx4 a[200:203], v2, s[44:47], s92 offen offset:0 sc1\n"
    "v_mfma_f32_16x16x32_bf16 v[192:195], a[140:143], a[112:115], v[192:195]\n"
    "v_mfma_f32_16x16x32_bf16 v[196:199], a[128:131], a[68:71], v[196:199]\n"
    "ds_read_b64_tr_b16 a[164:165], v24 offset:9728\n"
    "v_mfma_f32_16x16x32_bf16 v[196:199], a[132:135], a[84:87], v[196:199]\n"
    "v_mfma_f32_16x16x32_bf16 v[196:199], a[136:139], a[100:103], v[196:199]\n"
    "ds_read_b64_tr_b16 a[166:167], v24 offset:13824\n"
    "v_mfma_f32_16x16x32_bf16 v[196:199], a[140:143], a[116:119], v[196:199]\n"
    "buffer_load_dwordx4 a[204:207], v2, s[44:47], s92 offen offset:1024 sc1\n"
    "v_mfma_f32_16x16x32_bf16 v[200:203], a[128:131], a[72:75], v[200:203]\n"
    "ds_read_b64_tr_b16 a[168:169], v24 offset:17920\n"
    "v_mfma_f32_16x16x32_bf16 v[200:203], a[132:135], a[88:91], v[200:203]\n"
    "v_mfma_f32_16x16x32_bf16 v[200:203], a[136:139], a[104:107], v[200:203]\n"
    "ds_read_b64_tr_b16 a[170:171], v24 offset:22016\n"
    "v_mfma_f32_16x16x32_bf16 v[200:203], a[140:143], a[120:123], v[200:203]\n"
    "v_mfma_f32_16x16x32_bf16 v[204:207], a[128:131], a[76:79], v[204:207]\n"
    "ds_read_b64_tr_b16 a[172:173], v24 offset:26112\n"
    "buffer_load_dwordx4 v6, s[48:51], s67 offen offset:1024 sc1 lds\n"
    "v_mfma_f32_16x16x32_bf16 v[204:207], a[132:135], a[92:95], v[204:207]\n"
    "v_mfma_f32_16x16x32_bf16 v[204:207], a[136:139], a[108:111], v[204:207]\n"
    "ds_read_b64_tr_b16 a[174:175], v24 offset:30208\n"
    "v_mfma_f32_16x16x32_bf16 v[204:207], a[140:143], a[124:127], v[204:207]\n"
    "s_waitcnt lgkmcnt(8)\n"
    "v_mfma_f32_16x16x32_bf16 v[208:211], a[144:147], a[64:67], v[208:211]\n"
    "ds_read_b64_tr_b16 a[176:177], v25 offset:1536\n"
    "v_mfma_f32_16x16x32_bf16 v[208:211], a[148:151], a[80:83], v[208:211]\n"
    "buffer_load_dwordx4 a[216:219], v2, s[44:47], s92 offen offset:2048 sc1\n"
    "v_mfma_f32_16x16x32_bf16 v[208:211], a[152:155], a[96:99], v[208:211]\n"
    "ds_read_b64_tr_b16 a[178:179], v25 offset:5632\n"
    "v_mfma_f32_16x16x32_bf16 v[208:211], a[156:159], a[112:115], v[208:211]\n"
    "v_mfma_f32_16x16x32_bf16 v[212:215], a[144:147], a[68:71], v[212:215]\n"
    "ds_read_b64_tr_b16 a[180:181], v25 offset:9728\n"
    "v_mfma_f32_16x16x32_bf16 v[212:215], a[148:151], a[84:87], v[212:215]\n"
    "v_mfma_f32_16x16x32_bf16 v[212:215], a[152:155], a[100:103], v[212:215]\n"
    "ds_read_b64_tr_b16 a[182:183], v25 offset:13824\n"
    "buffer_load_dwordx4 a[220:223], v2, s[44:47], s92 offen offset:3072 sc1\n"
    "v_mfma_f32_16x16x32_bf16 v[212:215], a[156:159], a[116:119], v[212:215]\n"
    "v_mfma_f32_16x16x32_bf16 v[216:219], a[144:147], a[72:75], v[216:219]\n"
    "ds_read_b64_tr_b16 a[184:185], v25 offset:17920\n"
    "v_mfma_f32_16x16x32_bf16 v[216:219], a[148:151], a[88:91], v[216:219]\n"
    "v_mfma_f32_16x16x32_bf16 v[216:219], a[152:155], a[104:107], v[216:219]\n"
    "ds_read_b64_tr_b16 a[186:187], v25 offset:22016\n"
    "v_mfma_f32_16x16x32_bf16 v[216:219], a[156:159], a[120:123], v[216:219]\n"
    "buffer_load_dwordx4 v5, s[48:51], s67 offen offset:2048 sc1 lds\n"
    "v_mfma_f32_16x16x32_bf16 v[220:223], a[144:147], a[76:79], v[220:223]\n"
    "ds_read_b64_tr_b16 a[188:189], v25 offset:26112\n"
    "v_mfma_f32_16x16x32_bf16 v[220:223], a[148:151], a[92:95], v[220:223]\n"
    "v_mfma_f32_16x16x32_bf16 v[220:223], a[152:155], a[108:111], v[220:223]\n"
    "ds_read_b64_tr_b16 a[190:191], v25 offset:30208\n"
    "v_mfma_f32_16x16x32_bf16 v[220:223], a[156:159], a[124:127], v[220:223]\n"
    "s_waitcnt lgkmcnt(8)\n"
    "v_mfma_f32_16x16x32_bf16 v[224:227], a[160:163], a[64:67], v[224:227]\n"
    "buffer_load_dwordx4 a[232:235], v2, s[44:47], s93 offen offset:0 sc1\n"
    "v_mfma_f32_16x16x32_bf16 v[224:227], a[164:167], a[80:83], v[224:227]\n"
    "v_mfma_f32_16x16x32_bf16 v[224:227], a[168:171], a[96:99], v[224:227]\n"
    "v_mfma_f32_16x16x32_bf16 v[224:227], a[172:175], a[112:115], v[224:227]\n"
    "v_mfma_f32_16x16x32_bf16 v[228:231], a[160:163], a[68:71], v[228:231]\n"
    "s_waitcnt vmcnt(22)\n"
    "s_waitcnt lgkmcnt(0)\n"
    "s_barrier\n"
    "v_mfma_f32_16x16x32_bf16 v[228:231], a[164:167], a[84:87], v[228:231]\n"
    "ds_read_b64_tr_b16 a[128:129], v22 offset:0\n"
    "buffer_load_dwordx4 a[236:239], v2, s[44:47], s93 offen offset:1024 sc1\n"
    "v_mfma_f32_16x16x32_bf16 v[228:231], a[168:171], a[100:103], v[228:231]\n"
    "ds_read_b64_tr_b16 a[130:131], v22 offset:4096\n"
    "v_mfma_f32_16x16x32_bf16 v[228:231], a[172:175], a[116:119], v[228:231]\n"
    "v_mfma_f32_16x16x32_bf16 v[232:235], a[160:163], a[72:75], v[232:235]\n"
    "ds_read_b64_tr_b16 a[132:133], v22 offset:8192\n"
    "v_mfma_f32_16x16x32_bf16 v[232:235], a[164:167], a[88:91], v[232:235]\n"
    "ds_read_b64_tr_b16 a[134:135], v22 offset:12288\n"
    "v_mfma_f32_16x16x32_bf16 v[232:235], a[168:171], a[104:107], v[232:235]\n"
    "buffer_load_dwordx4 v6, s[48:51], s67 offen offset:3072 sc1 lds\n"
    "v_mfma_f32_16x16x32_bf16 v[232:235], a[172:175], a[120:123], v[232:235]\n"
    "ds_read_b64_tr_b16 a[136:137], v22 offset:16384\n"
    "v_mfma_f32_16x16x32_bf16 v[236:239], a[160:163], a[76:79], v[236:239]\n"
    "ds_read_b64_tr_b16 a[138:139], v22 offset:20480\n"
    "v_mfma_f32_16x16x32_bf16 v[236:239], a[164:167], a[92:95], v[236:239]\n"
    "v_mfma_f32_16x16x32_bf16 v[236:239], a[168:171], a[108:111], v[236:239]\n"
    "ds_read_b64_tr_b16 a[140:141], v22 offset:24576\n"
    "v_mfma_f32_16x16x32_bf16 v[236:239], a[172:175], a[124:127], v[236:239]\n"
    "ds_read_b64_tr_b16 a[142:143], v22 offset:28672\n"
    "buffer_load_dwordx4 a[248:251], v2, s[44:47], s93 offen offset:2048 sc1\n"
    "v_mfma_f32_16x16x32_bf16 v[240:243], a[176:179], a[64:67], v[240:243]\n"
    "v_mfma_f32_16x16x32_bf16 v[240:243], a[180:183], a[80:83], v[240:243]\n"
    "ds_read_b64_tr_b16 a[144:145], v23 offset:0\n"
    "v_mfma_f32_16x16x32_bf16 v[240:243], a[184:187], a[96:99], v[240:243]\n"
    "ds_read_b64_tr_b16 a[146:147], v23 offset:4096\n"
    "v_mfma_f32_16x16x32_bf16 v[240:243], a[188:191], a[112:115], v[240:243]\n"
    "v_mfma_f32_16x16x32_bf16 v[244:247], a[176:179], a[68:71], v[244:247]\n"
    "ds_read_b64_tr_b16 a[148:149], v23 offset:8192\n"
    "buffer_load_dwordx4 a[252:255], v2, s[44:47], s93 offen offset:3072 sc1\n"
    "v_mfma_f32_16x16x32_bf16 v[244:247], a[180:183], a[84:87], v[244:247]\n"
    "ds_read_b64_tr_b16 a[150:151], v23 offset:12288\n"
    "v_mfma_f32_16x16x32_bf16 v[244:247], a[184:187], a[100:103], v[244:247]\n"
    "v_mfma_f32_16x16x32_bf16 v[244:247], a[188:191], a[116:119], v[244:247]\n"
    "ds_read_b64_tr_b16 a[152:153], v23 offset:16384\n"
    "v_mfma_f32_16x16x32_bf16 v[248:251], a[176:179], a[72:75], v[248:251]\n"
    "ds_read_b64_tr_b16 a[154:155], v23 offset:20480\n"
    "v_mfma_f32_16x16x32_bf16 v[248:251], a[180:183], a[88:91], v[248:251]\n"
    "s_add_u32 s67, s67, 32768\n"
    "v_mfma_f32_16x16x32_bf16 v[248:251], a[184:187], a[104:107], v[248:251]\n"
    "ds_read_b64_tr_b16 a[156:157], v23 offset:24576\n"
    "s_add_u32 s90, s90, 8192\n"
    "v_mfma_f32_16x16x32_bf16 v[248:251], a[188:191], a[120:123], v[248:251]\n"
    "ds_read_b64_tr_b16 a[158:159], v23 offset:28672\n"
    "s_add_u32 s91, s91, 8192\n"
    "v_mfma_f32_16x16x32_bf16 v[252:255], a[176:179], a[76:79], v[252:255]\n"
    "s_add_u32 s92, s92, 8192\n"
    "v_mfma_f32_16x16x32_bf16 v[252:255], a[180:183], a[92:95], v[252:255]\n"
    "s_add_u32 s93, s93, 8192\n"
    "v_mfma_f32_16x16x32_bf16 v[252:255], a[184:187], a[108:111], v[252:255]\n"
    "v_mfma_f32_16x16x32_bf16 v[252:255], a[188:191], a[124:127], v[252:255]\n"
    "s_waitcnt lgkmcnt(8)\n"
    "s_waitcnt vmcnt(24)\n"
    "s_add_u32 s37, s37, 1\n"
    "s_cbranch_scc1 .Lk3_fin\n"
    "s_add_u32 s43, s43, 1\n"
    "s_cmp_lt_u32 s43, 6\n"
    "s_cbranch_scc1 .Lk3_loop\n"
    "s_branch .Lk3_fin\n"
    ".Lk3_sw:\n"
    "s_mov_b32 s44, s52\n"
    "s_mov_b32 s48, s56\n"
    "s_mov_b32 s45, s53\n"
    "s_mov_b32 s49, s57\n"
    "s_mov_b32 s46, s54\n"
    "s_mov_b32 s50, s58\n"
    "s_mov_b32 s67, 0\n"
    "s_mov_b32 s90, 0x0\n"
    "s_mov_b32 s91, 0x1000\n"
    "s_mov_b32 s92, 0x40000\n"
    "s_mov_b32 s93, 0x41000\n"
    "s_branch .Lk3_sw_ret\n"
    ".Lk3_chkO:\n"
    "s_waitcnt vmcnt(20)\n"
    "s_mov_b32 s65, 0\n"
    ".Lk3_pollO:\n"
    "v_readfirstlane_b32 s66, v27\n"
    "s_nop 3\n"
    "s_cmp_ge_u32 s66, 32\n"
    "s_cbranch_scc1 .Lk3_pollO_ok\n"
    "s_sleep 1\n"
    "global_load_dword v27, v26, s[34:35] sc1\n"
    "s_add_u32 s65, s65, 1\n"
    "s_cmp_lt_u32 s65, 0x8000\n"
    "s_waitcnt vmcnt(0)\n"
    "s_cbranch_scc1 .Lk3_pollO\n"
    ".Lk3_pollO_ok:\n"
    "s_branch .Lk3_chkO_ret\n"
    ".Lk3_ev0:\n"
    "s_cmp_eq_u32 s43, 2\n"
    "s_cbranch_scc1 .Lk3_gsw\n"
    "s_cmp_eq_u32 s43, 3\n"
    "s_cbranch_scc1 .Lk3_pubg\n"
    "s_cmp_eq_u32 s43, 4\n"
    "s_cbranch_scc1 .Lk3_pg\n"
    "s_cmp_eq_u32 s40, 0\n"
    "s_cbranch_scc0 .Lk3_ev0_ret\n"
    "s_cmp_eq_u32 s43, 1\n"
    "s_cbranch_scc1 .Lk3_po\n"
    "s_lshl_b32 s65, s42, 8\n"
    "s_add_u32 s65, s65, 0x2620000\n"
    "v_mov_b32 v16, s65\n"
    "s_mov_b64 exec, 1\n"
    "global_atomic_add v16, v28, s[34:35] sc1\n"
    "s_mov_b64 exec, -1\n"
    "s_branch .Lk3_ev0_ret\n"
    ".Lk3_po:\n"
    "s_lshl_b32 s65, s85, 8\n"
    "s_add_u32 s65, s65, 0x2620000\n"
    "v_mov_b32 v27, 0\n"
    "v_mov_b32 v26, s65\n"
    "global_load_dword v27, v26, s[34:35] sc1\n"
    "s_branch .Lk3_ev0_ret\n"
    ".Lk3_pubg:\n"
    "s_lshl_b32 s65, s84, 8\n"
    "s_add_u32 s65, s65, s83\n"
    "v_mov_b32 v16, s65\n"
    "s_mov_b64 exec, 1\n"
    "global_store_dword v16, v28, s[34:35] sc1\n"
    "s_mov_b64 exec, -1\n"
    "s_branch .Lk3_ev0_ret\n"
    ".Lk3_pg:\n"
    "v_mov_b32 v27, 0\n"
    "v_and_b32 v26, 3, v1\n"
    "v_min_u32 v26, 2, v26\n"
    "v_add3_u32 v26, v26, s84, 1\n"
    "v_and_b32 v26, 3, v26\n"
    "v_lshlrev_b32 v26, 8, v26\n"
    "v_add_u32 v26, s83, v26\n"
    "global_load_dword v27, v26, s[34:35] sc1\n"
    "s_lshl_b32 s66, s85, 2\n"
    "s_add_u32 s66, s66, s40\n"
    "s_lshl_b32 s65, s66, 17\n"
    "s_lshl_b32 s79, s94, 2\n"
    "s_add_u32 s79, s79, s84\n"
    "s_lshl_b32 s79, s79, 6\n"
    "s_add_u32 s65, s65, s79\n"
    "s_mov_b64 s[52:53], %9\n"
    "s_add_u32 s52, s52, s65\n"
    "s_addc_u32 s53, s53, 0\n"
    "s_and_b32 s53, s53, 0xffff\n"
    "s_sub_u32 s54, 0x20000, s79\n"
    "s_mov_b32 s55, 0x20000\n"
    "s_mov_b64 s[56:57], %10\n"
    "s_add_u32 s56, s56, s65\n"
    "s_addc_u32 s57, s57, 0\n"
    "s_and_b32 s57, s57, 0xffff\n"
    "s_sub_u32 s58, 0x20000, s79\n"
    "s_mov_b32 s59, 0x20000\n"
    "v_lshrrev_b32 v16, 2, v1\n"
    "v_lshlrev_b32 v16, 11, v16\n"
    "v_and_b32 v17, 3, v1\n"
    "v_lshl_add_u32 v9, v17, 4, v16\n"
    "s_mov_b32 s65, 0\n"
    "buffer_load_dwordx4 v[112:115], v9, s[52:55], s65 offen\n"
    "s_mov_b32 s65, 32768\n"
    "buffer_load_dwordx4 v[116:119], v9, s[52:55], s65 offen\n"
    "s_mov_b32 s65, 65536\n"
    "buffer_load_dwordx4 v[120:123], v9, s[52:55], s65 offen\n"
    "s_mov_b32 s65, 98304\n"
    "buffer_load_dwordx4 v[124:127], v9, s[52:55], s65 offen\n"
    "s_branch .Lk3_ev0_ret\n"
    ".Lk3_gsw:\n"
    "s_nop 7\n"
    "v_mov_b64 v[32:33], v[224:225]\n"
    "v_mov_b64 v[34:35], v[226:227]\n"
    "v_mov_b64 v[36:37], v[228:229]\n"
    "v_mov_b64 v[38:39], v[230:231]\n"
    "v_mov_b64 v[40:41], v[232:233]\n"
    "v_mov_b64 v[42:43], v[234:235]\n"
    "v_mov_b64 v[44:45], v[236:237]\n"
    "v_mov_b64 v[46:47], v[238:239]\n"
    "v_mov_b64 v[48:49], v[240:241]\n"
    "v_mov_b64 v[50:51], v[242:243]\n"
    "v_mov_b64 v[52:53], v[244:245]\n"
    "v_mov_b64 v[54:55], v[246:247]\n"
    "v_mov_b64 v[56:57], v[248:249]\n"
    "v_mov_b64 v[58:59], v[250:251]\n"
    "v_mov_b64 v[60:61], v[252:253]\n"
    "v_mov_b64 v[62:63], v[254:255]\n"
    "v_cvt_pk_bf16_f32 v72, v128, v129\n"
    "v_cvt_pk_bf16_f32 v73, v130, v131\n"
    "v_cvt_pk_bf16_f32 v74, v132, v133\n"
    "v_cvt_pk_bf16_f32 v75, v134, v135\n"
    "buffer_store_dwordx4 v[72:75], v2, s[72:75], s87 offen offset:0 sc1\n"
    "v_cvt_pk_bf16_f32 v76, v136, v137\n"
    "v_cvt_pk_bf16_f32 v77, v138, v139\n"
    "v_cvt_pk_bf16_f32 v78, v140, v141\n"
    "v_cvt_pk_bf16_f32 v79, v142, v143\n"
    "buffer_store_dwordx4 v[76:79], v2, s[72:75], s87 offen offset:1024 sc1\n"
    "v_cvt_pk_bf16_f32 v80, v144, v145\n"
    "v_cvt_pk_bf16_f32 v81, v146, v147\n"
    "v_cvt_pk_bf16_f32 v82, v148, v149\n"
    "v_cvt_pk_bf16_f32 v83, v150, v151\n"
    "buffer_store_dwordx4 v[80:83], v2, s[72:75], s87 offen offset:2048 sc1\n"
    "v_cvt_pk_bf16_f32 v84, v152, v153\n"
    "v_cvt_pk_bf16_f32 v85, v154, v155\n"
    "v_cvt_pk_bf16_f32 v86, v156, v157\n"
    "v_cvt_pk_bf16_f32 v87, v158, v159\n"
    "buffer_store_dwordx4 v[84:87], v2, s[72:75], s87 offen offset:3072 sc1\n"
    "v_cvt_pk_bf16_f32 v72, v160, v161\n"
    "v_cvt_pk_bf16_f32 v73, v162, v163\n"
    "v_cvt_pk_bf16_f32 v74, v164, v165\n"
    "v_cvt_pk_bf16_f32 v75, v166, v167\n"
    "buffer_store_dwordx4 v[72:75], v2, s[72:75], s88 offen offset:0 sc1\n"
    "v_cvt_pk_bf16_f32 v76, v168, v169\n"
    "v_cvt_pk_bf16_f32 v77, v170, v171\n"
    "v_cvt_pk_bf16_f32 v78, v172, v173\n"
    "v_cvt_pk_bf16_f32 v79, v174, v175\n"
    "buffer_store_dwordx4 v[76:79], v2, s[72:75], s88 offen offset:1024 sc1\n"
    "v_cvt_pk_bf16_f32 v80, v176, v177\n"
    "v_cvt_pk_bf16_f32 v81, v178, v179\n"
    "v_cvt_pk_bf16_f32 v82, v180, v181\n"
    "v_cvt_pk_bf16_f32 v83, v182, v183\n"
    "buffer_store_dwordx4 v[80:83], v2, s[72:75], s88 offen offset:2048 sc1\n"
    "v_cvt_pk_bf16_f32 v84, v184, v185\n"
    "v_cvt_pk_bf16_f32 v85, v186, v187\n"
    "v_cvt_pk_bf16_f32 v86, v188, v189\n"
    "v_cvt_pk_bf16_f32 v87, v190, v191\n"
    "buffer_store_dwordx4 v[84:87], v2, s[72:75], s88 offen offset:3072 sc1\n"
    "v_cvt_pk_bf16_f32 v72, v192, v193\n"
    "v_cvt_pk_bf16_f32 v73, v194, v195\n"
    "v_cvt_pk_bf16_f32 v74, v196, v197\n"
    "v_cvt_pk_bf16_f32 v75, v198, v199\n"
    "buffer_store_dwordx4 v[72:75], v2, s[72:75], s89 offen offset:0 sc1\n"
    "v_cvt_pk_bf16_f32 v76, v200, v201\n"
    "v_cvt_pk_bf16_f32 v77, v202, v203\n"
    "v_cvt_pk_bf16_f32 v78, v204, v205\n"
    "v_cvt_pk_bf16_f32 v79, v206, v207\n"
    "buffer_store_dwordx4 v[76:79], v2, s[72:75], s89 offen offset:1024 sc1\n"
    "v_cvt_pk_bf16_f32 v80, v208, v209\n"
    "v_cvt_pk_bf16_f32 v81, v210, v211\n"
    "v_cvt_pk_bf16_f32 v82, v212, v213\n"
    "v_cvt_pk_bf16_f32 v83, v214, v215\n"
    "buffer_store_dwordx4 v[80:83], v2, s[72:75], s89 offen offset:2048 sc1\n"
    "v_cvt_pk_bf16_f32 v84, v216, v217\n"
    "v_cvt_pk_bf16_f32 v85, v218, v219\n"
    "v_cvt_pk_bf16_f32 v86, v220, v221\n"
    "v_cvt_pk_bf16_f32 v87, v222, v223\n"
    "buffer_store_dwordx4 v[84:87], v2, s[72:75], s89 offen offset:3072 sc1\n"
    "v_mfma_f32_32x32x16_bf16 v[128:143], v[64:67], v[64:67], 0\n"
    "v_mfma_f32_32x32x16_bf16 v[144:159], v[64:67], v[64:67], 0\n"
    "v_mfma_f32_32x32x16_bf16 v[160:175], v[64:67], v[64:67], 0\n"
    "v_mfma_f32_32x32x16_bf16 v[176:191], v[64:67], v[64:67], 0\n"
    "v_mfma_f32_32x32x16_bf16 v[192:207], v[64:67], v[64:67], 0\n"
    "v_mfma_f32_32x32x16_bf16 v[208:223], v[64:67], v[64:67], 0\n"
    "v_mfma_f32_32x32x16_bf16 v[224:239], v[64:67], v[64:67], 0\n"
    "v_mfma_f32_32x32x16_bf16 v[240:255], v[64:67], v[64:67], 0\n"
    "s_nop 7\n"
    "s_nop 7\n"
    "s_branch .Lk3_ev0_ret\n"
    ".Lk3_fin:\n"
    "v_mfma_f32_16x16x32_bf16 v[128:131], a[128:131], a[64:67], v[128:131]\n"
    "ds_read_b64_tr_b16 a[160:161], v24 offset:512\n"
    "v_mfma_f32_16x16x32_bf16 v[128:131], a[132:135], a[80:83], v[128:131]\n"
    "v_mfma_f32_16x16x32_bf16 v[128:131], a[136:139], a[96:99], v[128:131]\n"
    "ds_read_b64_tr_b16 a[162:163], v24 offset:4608\n"
    "v_mfma_f32_16x16x32_bf16 v[128:131], a[140:143], a[112:115], v[128:131]\n"
    "v_mfma_f32_16x16x32_bf16 v[132:135], a[128:131], a[68:71], v[132:135]\n"
    "ds_read_b64_tr_b16 a[164:165], v24 offset:8704\n"
    "v_mfma_f32_16x16x32_bf16 v[132:135], a[132:135], a[84:87], v[132:135]\n"
    "v_mfma_f32_16x16x32_bf16 v[132:135], a[136:139], a[100:103], v[132:135]\n"
    "ds_read_b64_tr_b16 a[166:167], v24 offset:12800\n"
    "v_mfma_f32_16x16x32_bf16 v[132:135], a[140:143], a[116:119], v[132:135]\n"
    "v_mfma_f32_16x16x32_bf16 v[136:139], a[128:131], a[72:75], v[136:139]\n"
    "ds_read_b64_tr_b16 a[168:169], v24 offset:16896\n"
    "v_mfma_f32_16x16x32_bf16 v[136:139], a[132:135], a[88:91], v[136:139]\n"
    "v_mfma_f32_16x16x32_bf16 v[136:139], a[136:139], a[104:107], v[136:139]\n"
    "ds_read_b64_tr_b16 a[170:171], v24 offset:20992\n"
    "v_mfma_f32_16x16x32_bf16 v[136:139], a[140:143], a[120:123], v[136:139]\n"
    "v_mfma_f32_16x16x32_bf16 v[140:143], a[128:131], a[76:79], v[140:143]\n"
    "ds_read_b64_tr_b16 a[172:173], v24 offset:25088\n"
    "v_mfma_f32_16x16x32_bf16 v[140:143], a[132:135], a[92:95], v[140:143]\n"
    "v_mfma_f32_16x16x32_bf16 v[140:143], a[136:139], a[108:111], v[140:143]\n"
    "ds_read_b64_tr_b16 a[174:175], v24 offset:29184\n"
    "v_mfma_f32_16x16x32_bf16 v[140:143], a[140:143], a[124:127], v[140:143]\n"
    "s_waitcnt lgkmcnt(8)\n"
    "v_mfma_f32_16x16x32_bf16 v[144:147], a[144:147], a[64:67], v[144:147]\n"
    "ds_read_b64_tr_b16 a[176:177], v25 offset:512\n"
    "v_mfma_f32_16x16x32_bf16 v[144:147], a[148:151], a[80:83], v[144:147]\n"
    "v_mfma_f32_16x16x32_bf16 v[144:147], a[152:155], a[96:99], v[144:147]\n"
    "ds_read_b64_tr_b16 a[178:179], v25 offset:4608\n"
    "v_mfma_f32_16x16x32_bf16 v[144:147], a[156:159], a[112:115], v[144:147]\n"
    "v_mfma_f32_16x16x32_bf16 v[148:151], a[144:147], a[68:71], v[148:151]\n"
    "ds_read_b64_tr_b16 a[180:181], v25 offset:8704\n"
    "v_mfma_f32_16x16x32_bf16 v[148:151], a[148:151], a[84:87], v[148:151]\n"
    "v_mfma_f32_16x16x32_bf16 v[148:151], a[152:155], a[100:103], v[148:151]\n"
    "ds_read_b64_tr_b16 a[182:183], v25 offset:12800\n"
    "v_mfma_f32_16x16x32_bf16 v[148:151], a[156:159], a[116:119], v[148:151]\n"
    "v_mfma_f32_16x16x32_bf16 v[152:155], a[144:147], a[72:75], v[152:155]\n"
    "ds_read_b64_tr_b16 a[184:185], v25 offset:16896\n"
    "v_mfma_f32_16x16x32_bf16 v[152:155], a[148:151], a[88:91], v[152:155]\n"
    "v_mfma_f32_16x16x32_bf16 v[152:155], a[152:155], a[104:107], v[152:155]\n"
    "ds_read_b64_tr_b16 a[186:187], v25 offset:20992\n"
    "v_mfma_f32_16x16x32_bf16 v[152:155], a[156:159], a[120:123], v[152:155]\n"
    "v_mfma_f32_16x16x32_bf16 v[156:159], a[144:147], a[76:79], v[156:159]\n"
    "ds_read_b64_tr_b16 a[188:189], v25 offset:25088\n"
    "v_mfma_f32_16x16x32_bf16 v[156:159], a[148:151], a[92:95], v[156:159]\n"
    "v_mfma_f32_16x16x32_bf16 v[156:159], a[152:155], a[108:111], v[156:159]\n"
    "ds_read_b64_tr_b16 a[190:191], v25 offset:29184\n"
    "v_mfma_f32_16x16x32_bf16 v[156:159], a[156:159], a[124:127], v[156:159]\n"
    "s_waitcnt lgkmcnt(8)\n"
    "v_mfma_f32_16x16x32_bf16 v[160:163], a[160:163], a[64:67], v[160:163]\n"
    "ds_read_b64_tr_b16 a[128:129], v24 offset:1024\n"
    "v_mfma_f32_16x16x32_bf16 v[160:163], a[164:167], a[80:83], v[160:163]\n"
    "v_mfma_f32_16x16x32_bf16 v[160:163], a[168:171], a[96:99], v[160:163]\n"
    "ds_read_b64_tr_b16 a[130:131], v24 offset:5120\n"
    "v_mfma_f32_16x16x32_bf16 v[160:163], a[172:175], a[112:115], v[160:163]\n"
    "s_add_u32 s87, s87, 0x1000000\n"
    "v_cvt_pk_bf16_f32 v72, v128, v129\n"
    "v_mfma_f32_16x16x32_bf16 v[164:167], a[160:163], a[68:71], v[164:167]\n"
    "ds_read_b64_tr_b16 a[132:133], v24 offset:9216\n"
    "v_cvt_pk_bf16_f32 v73, v130, v131\n"
    "v_mfma_f32_16x16x32_bf16 v[164:167], a[164:167], a[84:87], v[164:167]\n"
    "v_cvt_pk_bf16_f32 v74, v132, v133\n"
    "v_cvt_pk_bf16_f32 v75, v134, v135\n"
    "v_mfma_f32_16x16x32_bf16 v[164:167], a[168:171], a[100:103], v[164:167]\n"
    "ds_read_b64_tr_b16 a[134:135], v24 offset:13312\n"
    "buffer_store_dwordx4 v[72:75], v2, s[72:75], s87 offen offset:0 sc1\n"
    "v_mfma_f32_16x16x32_bf16 v[164:167], a[172:175], a[116:119], v[164:167]\n"
    "v_cvt_pk_bf16_f32 v76, v136, v137\n"
    "v_cvt_pk_bf16_f32 v77, v138, v139\n"
    "v_mfma_f32_16x16x32_bf16 v[168:171], a[160:163], a[72:75], v[168:171]\n"
    "ds_read_b64_tr_b16 a[136:137], v24 offset:17408\n"
    "v_cvt_pk_bf16_f32 v78, v140, v141\n"
    "v_mfma_f32_16x16x32_bf16 v[168:171], a[164:167], a[88:91], v[168:171]\n"
    "v_cvt_pk_bf16_f32 v79, v142, v143\n"
    "buffer_store_dwordx4 v[76:79], v2, s[72:75], s87 offen offset:1024 sc1\n"
    "v_mfma_f32_16x16x32_bf16 v[168:171], a[168:171], a[104:107], v[168:171]\n"
    "ds_read_b64_tr_b16 a[138:139], v24 offset:21504\n"
    "v_cvt_pk_bf16_f32 v80, v144, v145\n"
    "v_mfma_f32_16x16x32_bf16 v[168:171], a[172:175], a[120:123], v[168:171]\n"
    "v_cvt_pk_bf16_f32 v81, v146, v147\n"
    "v_cvt_pk_bf16_f32 v82, v148, v149\n"
    "v_mfma_f32_16x16x32_bf16 v[172:175], a[160:163], a[76:79], v[172:175]\n"
    "ds_read_b64_tr_b16 a[140:141], v24 offset:25600\n"
    "v_cvt_pk_bf16_f32 v83, v150, v151\n"
    "v_mfma_f32_16x16x32_bf16 v[172:175], a[164:167], a[92:95], v[172:175]\n"
    "buffer_store_dwordx4 v[80:83], v2, s[72:75], s87 offen offset:2048 sc1\n"
    "v_cvt_pk_bf16_f32 v84, v152, v153\n"
    "v_mfma_f32_16x16x32_bf16 v[172:175], a[168:171], a[108:111], v[172:175]\n"
    "ds_read_b64_tr_b16 a[142:143], v24 offset:29696\n"
    "v_cvt_pk_bf16_f32 v85, v154, v155\n"
    "v_mfma_f32_16x16x32_bf16 v[172:175], a[172:175], a[124:127], v[172:175]\n"
    "s_waitcnt lgkmcnt(8)\n"
    "v_cvt_pk_bf16_f32 v86, v156, v157\n"
    "v_cvt_pk_bf16_f32 v87, v158, v159\n"
    "v_mfma_f32_16x16x32_bf16 v[176:179], a[176:179], a[64:67], v[176:179]\n"
    "ds_read_b64_tr_b16 a[144:145], v25 offset:1024\n"
    "buffer_store_dwordx4 v[84:87], v2, s[72:75], s87 offen offset:3072 sc1\n"
    "v_mfma_f32_16x16x32_bf16 v[176:179], a[180:183], a[80:83], v[176:179]\n"
    "v_mfma_f32_16x16x32_bf16 v[176:179], a[184:187], a[96:99], v[176:179]\n"
    "ds_read_b64_tr_b16 a[146:147], v25 offset:5120\n"
    "v_mfma_f32_16x16x32_bf16 v[176:179], a[188:191], a[112:115], v[176:179]\n"
    "v_mfma_f32_16x16x32_bf16 v[180:183], a[176:179], a[68:71], v[180:183]\n"
    "ds_read_b64_tr_b16 a[148:149], v25 offset:9216\n"
    "v_mfma_f32_16x16x32_bf16 v[180:183], a[180:183], a[84:87], v[180:183]\n"
    "v_mfma_f32_16x16x32_bf16 v[180:183], a[184:187], a[100:103], v[180:183]\n"
    "ds_read_b64_tr_b16 a[150:151], v25 offset:13312\n"
    "v_mfma_f32_16x16x32_bf16 v[180:183], a[188:191], a[116:119], v[180:183]\n"
    "v_mfma_f32_16x16x32_bf16 v[184:187], a[176:179], a[72:75], v[184:187]\n"
    "ds_read_b64_tr_b16 a[152:153], v25 offset:17408\n"
    "v_mfma_f32_16x16x32_bf16 v[184:187], a[180:183], a[88:91], v[184:187]\n"
    "v_mfma_f32_16x16x32_bf16 v[184:187], a[184:187], a[104:107], v[184:187]\n"
    "ds_read_b64_tr_b16 a[154:155], v25 offset:21504\n"
    "v_mfma_f32_16x16x32_bf16 v[184:187], a[188:191], a[120:123], v[184:187]\n"
    "v_mfma_f32_16x16x32_bf16 v[188:191], a[176:179], a[76:79], v[188:191]\n"
    "ds_read_b64_tr_b16 a[156:157], v25 offset:25600\n"
    "v_mfma_f32_16x16x32_bf16 v[188:191], a[180:183], a[92:95], v[188:191]\n"
    "v_mfma_f32_16x16x32_bf16 v[188:191], a[184:187], a[108:111], v[188:191]\n"
    "ds_read_b64_tr_b16 a[158:159], v25 offset:29696\n"
    "v_mfma_f32_16x16x32_bf16 v[188:191], a[188:191], a[124:127], v[188:191]\n"
    "s_waitcnt lgkmcnt(8)\n"
    "v_mfma_f32_16x16x32_bf16 v[192:195], a[128:131], a[64:67], v[192:195]\n"
    "ds_read_b64_tr_b16 a[160:161], v24 offset:1536\n"
    "v_mfma_f32_16x16x32_bf16 v[192:195], a[132:135], a[80:83], v[192:195]\n"
    "v_mfma_f32_16x16x32_bf16 v[192:195], a[136:139], a[96:99], v[192:195]\n"
    "ds_read_b64_tr_b16 a[162:163], v24 offset:5632\n"
    "v_mfma_f32_16x16x32_bf16 v[192:195], a[140:143], a[112:115], v[192:195]\n"
    "s_add_u32 s88, s88, 0x1000000\n"
    "v_cvt_pk_bf16_f32 v72, v160, v161\n"
    "v_mfma_f32_16x16x32_bf16 v[196:199], a[128:131], a[68:71], v[196:199]\n"
    "ds_read_b64_tr_b16 a[164:165], v24 offset:9728\n"
    "v_cvt_pk_bf16_f32 v73, v162, v163\n"
    "v_mfma_f32_16x16x32_bf16 v[196:199], a[132:135], a[84:87], v[196:199]\n"
    "v_cvt_pk_bf16_f32 v74, v164, v165\n"
    "v_cvt_pk_bf16_f32 v75, v166, v167\n"
    "v_mfma_f32_16x16x32_bf16 v[196:199], a[136:139], a[100:103], v[196:199]\n"
    "ds_read_b64_tr_b16 a[166:167], v24 offset:13824\n"
    "buffer_store_dwordx4 v[72:75], v2, s[72:75], s88 offen offset:0 sc1\n"
    "v_mfma_f32_16x16x32_bf16 v[196:199], a[140:143], a[116:119], v[196:199]\n"
    "v_cvt_pk_bf16_f32 v76, v168, v169\n"
    "v_cvt_pk_bf16_f32 v77, v170, v171\n"
    "v_mfma_f32_16x16x32_bf16 v[200:203], a[128:131], a[72:75], v[200:203]\n"
    "ds_read_b64_tr_b16 a[168:169], v24 offset:17920\n"
    "v_cvt_pk_bf16_f32 v78, v172, v173\n"
    "v_mfma_f32_16x16x32_bf16 v[200:203], a[132:135], a[88:91], v[200:203]\n"
    "v_cvt_pk_bf16_f32 v79, v174, v175\n"
    "buffer_store_dwordx4 v[76:79], v2, s[72:75], s88 offen offset:1024 sc1\n"
    "v_mfma_f32_16x16x32_bf16 v[200:203], a[136:139], a[104:107], v[200:203]\n"
    "ds_read_b64_tr_b16 a[170:171], v24 offset:22016\n"
    "v_cvt_pk_bf16_f32 v80, v176, v177\n"
    "v_mfma_f32_16x16x32_bf16 v[200:203], a[140:143], a[120:123], v[200:203]\n"
    "v_cvt_pk_bf16_f32 v81, v178, v179\n"
    "v_cvt_pk_bf16_f32 v82, v180, v181\n"
    "v_mfma_f32_16x16x32_bf16 v[204:207], a[128:131], a[76:79], v[204:207]\n"
    "ds_read_b64_tr_b16 a[172:173], v24 offset:26112\n"
    "v_cvt_pk_bf16_f32 v83, v182, v183\n"
    "v_mfma_f32_16x16x32_bf16 v[204:207], a[132:135], a[92:95], v[204:207]\n"
    "buffer_store_dwordx4 v[80:83], v2, s[72:75], s88 offen offset:2048 sc1\n"
    "v_cvt_pk_bf16_f32 v84, v184, v185\n"
    "v_mfma_f32_16x16x32_bf16 v[204:207], a[136:139], a[108:111], v[204:207]\n"
    "ds_read_b64_tr_b16 a[174:175], v24 offset:30208\n"
    "v_cvt_pk_bf16_f32 v85, v186, v187\n"
    "v_mfma_f32_16x16x32_bf16 v[204:207], a[140:143], a[124:127], v[204:207]\n"
    "s_waitcnt lgkmcnt(8)\n"
    "v_cvt_pk_bf16_f32 v86, v188, v189\n"
    "v_cvt_pk_bf16_f32 v87, v190, v191\n"
    "v_mfma_f32_16x16x32_bf16 v[208:211], a[144:147], a[64:67], v[208:211]\n"
    "ds_read_b64_tr_b16 a[176:177], v25 offset:1536\n"
    "buffer_store_dwordx4 v[84:87], v2, s[72:75], s88 offen offset:3072 sc1\n"
    "v_mfma_f32_16x16x32_bf16 v[208:211], a[148:151], a[80:83], v[208:211]\n"
    "v_mfma_f32_16x16x32_bf16 v[208:211], a[152:155], a[96:99], v[208:211]\n"
    "ds_read_b64_tr_b16 a[178:179], v25 offset:5632\n"
    "v_mfma_f32_16x16x32_bf16 v[208:211], a[156:159], a[112:115], v[208:211]\n"
    "v_mfma_f32_16x16x32_bf16 v[212:215], a[144:147], a[68:71], v[212:215]\n"
    "ds_read_b64_tr_b16 a[180:181], v25 offset:9728\n"
    "v_mfma_f32_16x16x32_bf16 v[212:215], a[148:151], a[84:87], v[212:215]\n"
    "v_mfma_f32_16x16x32_bf16 v[212:215], a[152:155], a[100:103], v[212:215]\n"
    "ds_read_b64_tr_b16 a[182:183], v25 offset:13824\n"
    "v_mfma_f32_16x16x32_bf16 v[212:215], a[156:159], a[116:119], v[212:215]\n"
    "v_mfma_f32_16x16x32_bf16 v[216:219], a[144:147], a[72:75], v[216:219]\n"
    "ds_read_b64_tr_b16 a[184:185], v25 offset:17920\n"
    "v_mfma_f32_16x16x32_bf16 v[216:219], a[148:151], a[88:91], v[216:219]\n"
    "v_mfma_f32_16x16x32_bf16 v[216:219], a[152:155], a[104:107], v[216:219]\n"
    "ds_read_b64_tr_b16 a[186:187], v25 offset:22016\n"
    "v_mfma_f32_16x16x32_bf16 v[216:219], a[156:159], a[120:123], v[216:219]\n"
    "v_mfma_f32_16x16x32_bf16 v[220:223], a[144:147], a[76:79], v[220:223]\n"
    "ds_read_b64_tr_b16 a[188:189], v25 offset:26112\n"
    "v_mfma_f32_16x16x32_bf16 v[220:223], a[148:151], a[92:95], v[220:223]\n"
    "v_mfma_f32_16x16x32_bf16 v[220:223], a[152:155], a[108:111], v[220:223]\n"
    "ds_read_b64_tr_b16 a[190:191], v25 offset:30208\n"
    "v_mfma_f32_16x16x32_bf16 v[220:223], a[156:159], a[124:127], v[220:223]\n"
    "s_waitcnt lgkmcnt(8)\n"
    "v_mfma_f32_16x16x32_bf16 v[224:227], a[160:163], a[64:67], v[224:227]\n"
    "v_mfma_f32_16x16x32_bf16 v[224:227], a[164:167], a[80:83], v[224:227]\n"
    "v_mfma_f32_16x16x32_bf16 v[224:227], a[168:171], a[96:99], v[224:227]\n"
    "v_mfma_f32_16x16x32_bf16 v[224:227], a[172:175], a[112:115], v[224:227]\n"
    "s_add_u32 s89, s89, 0x1000000\n"
    "v_cvt_pk_bf16_f32 v72, v192, v193\n"
    "v_mfma_f32_16x16x32_bf16 v[228:231], a[160:163], a[68:71], v[228:231]\n"
    "v_cvt_pk_bf16_f32 v73, v194, v195\n"
    "v_cvt_pk_bf16_f32 v74, v196, v197\n"
    "v_mfma_f32_16x16x32_bf16 v[228:231], a[164:167], a[84:87], v[228:231]\n"
    "v_cvt_pk_bf16_f32 v75, v198, v199\n"
    "buffer_store_dwordx4 v[72:75], v2, s[72:75], s89 offen offset:0 sc1\n"
    "v_mfma_f32_16x16x32_bf16 v[228:231], a[168:171], a[100:103], v[228:231]\n"
    "v_cvt_pk_bf16_f32 v76, v200, v201\n"
    "v_cvt_pk_bf16_f32 v77, v202, v203\n"
    "v_mfma_f32_16x16x32_bf16 v[228:231], a[172:175], a[116:119], v[228:231]\n"
    "v_cvt_pk_bf16_f32 v78, v204, v205\n"
    "v_cvt_pk_bf16_f32 v79, v206, v207\n"
    "v_mfma_f32_16x16x32_bf16 v[232:235], a[160:163], a[72:75], v[232:235]\n"
    "buffer_store_dwordx4 v[76:79], v2, s[72:75], s89 offen offset:1024 sc1\n"
    "v_cvt_pk_bf16_f32 v80, v208, v209\n"
    "v_mfma_f32_16x16x32_bf16 v[232:235], a[164:167], a[88:91], v[232:235]\n"
    "v_cvt_pk_bf16_f32 v81, v210, v211\n"
    "v_cvt_pk_bf16_f32 v82, v212, v213\n"
    "v_mfma_f32_16x16x32_bf16 v[232:235], a[168:171], a[104:107], v[232:235]\n"
    "v_cvt_pk_bf16_f32 v83, v214, v215\n"
    "buffer_store_dwordx4 v[80:83], v2, s[72:75], s89 offen offset:2048 sc1\n"
    "v_mfma_f32_16x16x32_bf16 v[232:235], a[172:175], a[120:123], v[232:235]\n"
    "v_cvt_pk_bf16_f32 v84, v216, v217\n"
    "v_cvt_pk_bf16_f32 v85, v218, v219\n"
    "v_mfma_f32_16x16x32_bf16 v[236:239], a[160:163], a[76:79], v[236:239]\n"
    "v_cvt_pk_bf16_f32 v86, v220, v221\n"
    "v_cvt_pk_bf16_f32 v87, v222, v223\n"
    "v_mfma_f32_16x16x32_bf16 v[236:239], a[164:167], a[92:95], v[236:239]\n"
    "buffer_store_dwordx4 v[84:87], v2, s[72:75], s89 offen offset:3072 sc1\n"
    "v_mfma_f32_16x16x32_bf16 v[236:239], a[168:171], a[108:111], v[236:239]\n"
    "v_mfma_f32_16x16x32_bf16 v[236:239], a[172:175], a[124:127], v[236:239]\n"
    "s_waitcnt lgkmcnt(0)\n"
    "v_mfma_f32_16x16x32_bf16 v[240:243], a[176:179], a[64:67], v[240:243]\n"
    "v_mfma_f32_16x16x32_bf16 v[240:243], a[180:183], a[80:83], v[240:243]\n"
    "v_mfma_f32_16x16x32_bf16 v[240:243], a[184:187], a[96:99], v[240:243]\n"
    "v_mfma_f32_16x16x32_bf16 v[240:243], a[188:191], a[112:115], v[240:243]\n"
    "v_mfma_f32_16x16x32_bf16 v[244:247], a[176:179], a[68:71], v[244:247]\n"
    "v_mfma_f32_16x16x32_bf16 v[244:247], a[180:183], a[84:87], v[244:247]\n"
    "v_mfma_f32_16x16x32_bf16 v[244:247], a[184:187], a[100:103], v[244:247]\n"
    "v_mfma_f32_16x16x32_bf16 v[244:247], a[188:191], a[116:119], v[244:247]\n"
    "v_mfma_f32_16x16x32_bf16 v[248:251], a[176:179], a[72:75], v[248:251]\n"
    "v_mfma_f32_16x16x32_bf16 v[248:251], a[180:183], a[88:91], v[248:251]\n"
    "v_mfma_f32_16x16x32_bf16 v[248:251], a[184:187], a[104:107], v[248:251]\n"
    "v_mfma_f32_16x16x32_bf16 v[248:251], a[188:191], a[120:123], v[248:251]\n"
    "v_mfma_f32_16x16x32_bf16 v[252:255], a[176:179], a[76:79], v[252:255]\n"
    "v_mfma_f32_16x16x32_bf16 v[252:255], a[180:183], a[92:95], v[252:255]\n"
    "v_mfma_f32_16x16x32_bf16 v[252:255], a[184:187], a[108:111], v[252:255]\n"
    "v_mfma_f32_16x16x32_bf16 v[252:255], a[188:191], a[124:127], v[252:255]\n"
    "s_waitcnt vmcnt(0)\n"
    "s_lshl_b32 s65, s84, 8\n"
    "s_add_u32 s65, s65, s83\n"
    "s_add_u32 s65, s65, 0x10000\n"
    "v_mov_b32 v16, s65\n"
    "s_mov_b64 exec, 1\n"
    "global_store_dword v16, v28, s[34:35] sc1\n"
    "s_mov_b64 exec, -1\n"
    "s_mov_b32 s65, 0\n"
    ".Lk3_pollG:\n"
    "v_cmp_gt_u32 vcc, 1, v27\n"
    "s_nop 3\n"
    "s_cbranch_vccz .Lk3_pollG_ok\n"
    "s_sleep 1\n"
    "global_load_dword v27, v26, s[34:35] sc1\n"
    "s_add_u32 s65, s65, 1\n"
    "s_cmp_lt_u32 s65, 0x8000\n"
    "s_waitcnt vmcnt(0)\n"
    "s_cbranch_scc1 .Lk3_pollG\n"
    ".Lk3_pollG_ok:\n"
    "v_add_u32 v26, 0x10000, v26\n"
    "global_load_dword v27, v26, s[34:35] sc1\n"
    "s_add_u32 s65, s86, 0x0\n"
    "buffer_load_dwordx4 v[128:131], v2, s[72:75], s65 offen offset:0 sc1\n"
    "buffer_load_dwordx4 v[132:135], v2, s[72:75], s65 offen offset:1024 sc1\n"
    "buffer_load_dwordx4 v[136:139], v2, s[72:75], s65 offen offset:2048 sc1\n"
    "buffer_load_dwordx4 v[140:143], v2, s[72:75], s65 offen offset:3072 sc1\n"
    "s_add_u32 s65, s86, 0x4000\n"
    "buffer_load_dwordx4 v[144:147], v2, s[72:75], s65 offen offset:0 sc1\n"
    "buffer_load_dwordx4 v[148:151], v2, s[72:75], s65 offen offset:1024 sc1\n"
    "buffer_load_dwordx4 v[152:155], v2, s[72:75], s65 offen offset:2048 sc1\n"
    "buffer_load_dwordx4 v[156:159], v2, s[72:75], s65 offen offset:3072 sc1\n"
    "s_add_u32 s65, s86, 0x8000\n"
    "buffer_load_dwordx4 v[160:163], v2, s[72:75], s65 offen offset:0 sc1\n"
    "buffer_load_dwordx4 v[164:167], v2, s[72:75], s65 offen offset:1024 sc1\n"
    "buffer_load_dwordx4 v[168:171], v2, s[72:75], s65 offen offset:2048 sc1\n"
    "buffer_load_dwordx4 v[172:175], v2, s[72:75], s65 offen offset:3072 sc1\n"
    "s_barrier\n"
    "s_mul_i32 s65, s40, 5120\n"
    "s_add_u32 s65, s65, s39\n"
    "v_and_b32 v16, 15, v1\n"
    "v_mul_u32_u24 v17, 80, v16\n"
    "v_lshrrev_b32 v18, 4, v1\n"
    "v_lshl_add_u32 v17, v18, 3, v17\n"
    "v_add_u32 v3, s65, v17\n"
    "v_lshrrev_b32 v18, 2, v1\n"
    "v_mul_u32_u24 v17, 80, v18\n"
    "v_and_b32 v16, 3, v1\n"
    "v_lshl_add_u32 v17, v16, 4, v17\n"
    "v_add_u32 v4, s65, v17\n"
    "v_lshlrev_b32 v80, 16, v112\n"
    "v_and_b32 v81, 0xffff0000, v112\n"
    "v_lshlrev_b32 v82, 16, v113\n"
    "v_and_b32 v83, 0xffff0000, v113\n"
    "v_lshlrev_b32 v84, 16, v114\n"
    "v_and_b32 v85, 0xffff0000, v114\n"
    "v_lshlrev_b32 v86, 16, v115\n"
    "v_and_b32 v87, 0xffff0000, v115\n"
    "v_lshlrev_b32 v88, 16, v116\n"
    "v_and_b32 v89, 0xffff0000, v116\n"
    "v_lshlrev_b32 v90, 16, v117\n"
    "v_and_b32 v91, 0xffff0000, v117\n"
    "v_lshlrev_b32 v92, 16, v118\n"
    "v_and_b32 v93, 0xffff0000, v118\n"
    "v_lshlrev_b32 v94, 16, v119\n"
    "v_and_b32 v95, 0xffff0000, v119\n"
    "v_lshlrev_b32 v96, 16, v120\n"
    "v_and_b32 v97, 0xffff0000, v120\n"
    "v_lshlrev_b32 v98, 16, v121\n"
    "v_and_b32 v99, 0xffff0000, v121\n"
    "v_lshlrev_b32 v100, 16, v122\n"
    "v_and_b32 v101, 0xffff0000, v122\n"
    "v_lshlrev_b32 v102, 16, v123\n"
    "v_and_b32 v103, 0xffff0000, v123\n"
    "v_lshlrev_b32 v104, 16, v124\n"
    "v_and_b32 v105, 0xffff0000, v124\n"
    "v_lshlrev_b32 v106, 16, v125\n"
    "v_and_b32 v107, 0xffff0000, v125\n"
    "v_lshlrev_b32 v108, 16, v126\n"
    "v_and_b32 v109, 0xffff0000, v126\n"
    "v_lshlrev_b32 v110, 16, v127\n"
    "v_and_b32 v111, 0xffff0000, v127\n"
    "s_waitcnt vmcnt(12)\n"
    "v_cmp_gt_u32 vcc, 1, v27\n"
    "s_mov_b32 s82, 0\n"
    "s_nop 2\n"
    "s_cbranch_vccnz .Lk3_slate\n"
    "s_add_u32 s65, s86, 0x1000000\n"
    "buffer_load_dwordx4 v[176:179], v2, s[72:75], s65 offen offset:0 sc1\n"
    "buffer_load_dwordx4 v[180:183], v2, s[72:75], s65 offen offset:1024 sc1\n"
    "buffer_load_dwordx4 v[184:187], v2, s[72:75], s65 offen offset:2048 sc1\n"
    "buffer_load_dwordx4 v[188:191], v2, s[72:75], s65 offen offset:3072 sc1\n"
    "s_add_u32 s65, s86, 0x1004000\n"
    "buffer_load_dwordx4 v[192:195], v2, s[72:75], s65 offen offset:0 sc1\n"
    "buffer_load_dwordx4 v[196:199], v2, s[72:75], s65 offen offset:1024 sc1\n"
    "buffer_load_dwordx4 v[200:203], v2, s[72:75], s65 offen offset:2048 sc1\n"
    "buffer_load_dwordx4 v[204:207], v2, s[72:75], s65 offen offset:3072 sc1\n"
    "s_add_u32 s65, s86, 0x1008000\n"
    "buffer_load_dwordx4 v[208:211], v2, s[72:75], s65 offen offset:0 sc1\n"
    "buffer_load_dwordx4 v[212:215], v2, s[72:75], s65 offen offset:1024 sc1\n"
    "buffer_load_dwordx4 v[216:219], v2, s[72:75], s65 offen offset:2048 sc1\n"
    "buffer_load_dwordx4 v[220:223], v2, s[72:75], s65 offen offset:3072 sc1\n"
    "s_mov_b32 s82, 1\n"
    ".Lk3_slate:\n"
    "s_waitcnt vmcnt(11)\n"
    "v_lshlrev_b32 v64, 16, v128\n"
    "v_and_b32 v65, 0xffff0000, v128\n"
    "v_pk_add_f32 v[32:33], v[32:33], v[64:65]\n"
    "v_lshlrev_b32 v66, 16, v129\n"
    "v_and_b32 v67, 0xffff0000, v129\n"
    "v_pk_add_f32 v[34:35], v[34:35], v[66:67]\n"
    "v_lshlrev_b32 v68, 16, v130\n"
    "v_and_b32 v69, 0xffff0000, v130\n"
    "v_pk_add_f32 v[36:37], v[36:37], v[68:69]\n"
    "v_lshlrev_b32 v70, 16, v131\n"
    "v_and_b32 v71, 0xffff0000, v131\n"
    "v_pk_add_f32 v[38:39], v[38:39], v[70:71]\n"
    "s_waitcnt vmcnt(10)\n"
    "v_lshlrev_b32 v64, 16, v132\n"
    "v_and_b32 v65, 0xffff0000, v132\n"
    "v_pk_add_f32 v[40:41], v[40:41], v[64:65]\n"
    "v_lshlrev_b32 v66, 16, v133\n"
    "v_and_b32 v67, 0xffff0000, v133\n"
    "v_pk_add_f32 v[42:43], v[42:43], v[66:67]\n"
    "v_lshlrev_b32 v68, 16, v134\n"
    "v_and_b32 v69, 0xffff0000, v134\n"
    "v_pk_add_f32 v[44:45], v[44:45], v[68:69]\n"
    "v_lshlrev_b32 v70, 16, v135\n"
    "v_and_b32 v71, 0xffff0000, v135\n"
    "v_pk_add_f32 v[46:47], v[46:47], v[70:71]\n"
    "s_waitcnt vmcnt(9)\n"
    "v_lshlrev_b32 v64, 16, v136\n"
    "v_and_b32 v65, 0xffff0000, v136\n"
    "v_pk_add_f32 v[48:49], v[48:49], v[64:65]\n"
    "v_lshlrev_b32 v66, 16, v137\n"
    "v_and_b32 v67, 0xffff0000, v137\n"
    "v_pk_add_f32 v[50:51], v[50:51], v[66:67]\n"
    "v_lshlrev_b32 v68, 16, v138\n"
    "v_and_b32 v69, 0xffff0000, v138\n"
    "v_pk_add_f32 v[52:53], v[52:53], v[68:69]\n"
    "v_lshlrev_b32 v70, 16, v139\n"
    "v_and_b32 v71, 0xffff0000, v139\n"
    "v_pk_add_f32 v[54:55], v[54:55], v[70:71]\n"
    "s_waitcnt vmcnt(8)\n"
    "v_lshlrev_b32 v64, 16, v140\n"
    "v_and_b32 v65, 0xffff0000, v140\n"
    "v_pk_add_f32 v[56:57], v[56:57], v[64:65]\n"
    "v_lshlrev_b32 v66, 16, v141\n"
    "v_and_b32 v67, 0xffff0000, v141\n"
    "v_pk_add_f32 v[58:59], v[58:59], v[66:67]\n"
    "v_lshlrev_b32 v68, 16, v142\n"
    "v_and_b32 v69, 0xffff0000, v142\n"
    "v_pk_add_f32 v[60:61], v[60:61], v[68:69]\n"
    "v_lshlrev_b32 v70, 16, v143\n"
    "v_and_b32 v71, 0xffff0000, v143\n"
    "v_pk_add_f32 v[62:63], v[62:63], v[70:71]\n"
    "s_waitcnt vmcnt(7)\n"
    "v_lshlrev_b32 v64, 16, v144\n"
    "v_and_b32 v65, 0xffff0000, v144\n"
    "v_pk_add_f32 v[32:33], v[32:33], v[64:65]\n"
    "v_lshlrev_b32 v66, 16, v145\n"
    "v_and_b32 v67, 0xffff0000, v145\n"
    "v_pk_add_f32 v[34:35], v[34:35], v[66:67]\n"
    "v_lshlrev_b32 v68, 16, v146\n"
    "v_and_b32 v69, 0xffff0000, v146\n"
    "v_pk_add_f32 v[36:37], v[36:37], v[68:69]\n"
    "v_lshlrev_b32 v70, 16, v147\n"
    "v_and_b32 v71, 0xffff0000, v147\n"
    "v_pk_add_f32 v[38:39], v[38:39], v[70:71]\n"
    "s_waitcnt vmcnt(6)\n"
    "v_lshlrev_b32 v64, 16, v148\n"
    "v_and_b32 v65, 0xffff0000, v148\n"
    "v_pk_add_f32 v[40:41], v[40:41], v[64:65]\n"
    "v_lshlrev_b32 v66, 16, v149\n"
    "v_and_b32 v67, 0xffff0000, v149\n"
    "v_pk_add_f32 v[42:43], v[42:43], v[66:67]\n"
    "v_lshlrev_b32 v68, 16, v150\n"
    "v_and_b32 v69, 0xffff0000, v150\n"
    "v_pk_add_f32 v[44:45], v[44:45], v[68:69]\n"
    "v_lshlrev_b32 v70, 16, v151\n"
    "v_and_b32 v71, 0xffff0000, v151\n"
    "v_pk_add_f32 v[46:47], v[46:47], v[70:71]\n"
    "s_waitcnt vmcnt(5)\n"
    "v_lshlrev_b32 v64, 16, v152\n"
    "v_and_b32 v65, 0xffff0000, v152\n"
    "v_pk_add_f32 v[48:49], v[48:49], v[64:65]\n"
    "v_lshlrev_b32 v66, 16, v153\n"
    "v_and_b32 v67, 0xffff0000, v153\n"
    "v_pk_add_f32 v[50:51], v[50:51], v[66:67]\n"
    "v_lshlrev_b32 v68, 16, v154\n"
    "v_and_b32 v69, 0xffff0000, v154\n"
    "v_pk_add_f32 v[52:53], v[52:53], v[68:69]\n"
    "v_lshlrev_b32 v70, 16, v155\n"
    "v_and_b32 v71, 0xffff0000, v155\n"
    "v_pk_add_f32 v[54:55], v[54:55], v[70:71]\n"
    "s_waitcnt vmcnt(4)\n"
    "v_lshlrev_b32 v64, 16, v156\n"
    "v_and_b32 v65, 0xffff0000, v156\n"
    "v_pk_add_f32 v[56:57], v[56:57], v[64:65]\n"
    "v_lshlrev_b32 v66, 16, v157\n"
    "v_and_b32 v67, 0xffff0000, v157\n"
    "v_pk_add_f32 v[58:59], v[58:59], v[66:67]\n"
    "v_lshlrev_b32 v68, 16, v158\n"
    "v_and_b32 v69, 0xffff0000, v158\n"
    "v_pk_add_f32 v[60:61], v[60:61], v[68:69]\n"
    "v_lshlrev_b32 v70, 16, v159\n"
    "v_and_b32 v71, 0xffff0000, v159\n"
    "v_pk_add_f32 v[62:63], v[62:63], v[70:71]\n"
    "s_waitcnt vmcnt(3)\n"
    "v_lshlrev_b32 v64, 16, v160\n"
    "v_and_b32 v65, 0xffff0000, v160\n"
    "v_pk_add_f32 v[32:33], v[32:33], v[64:65]\n"
    "v_lshlrev_b32 v66, 16, v161\n"
    "v_and_b32 v67, 0xffff0000, v161\n"
    "v_pk_add_f32 v[34:35], v[34:35], v[66:67]\n"
    "v_lshlrev_b32 v68, 16, v162\n"
    "v_and_b32 v69, 0xffff0000, v162\n"
    "v_pk_add_f32 v[36:37], v[36:37], v[68:69]\n"
    "v_lshlrev_b32 v70, 16, v163\n"
    "v_and_b32 v71, 0xffff0000, v163\n"
    "v_pk_add_f32 v[38:39], v[38:39], v[70:71]\n"
    "s_waitcnt vmcnt(2)\n"
    "v_lshlrev_b32 v64, 16, v164\n"
    "v_and_b32 v65, 0xffff0000, v164\n"
    "v_pk_add_f32 v[40:41], v[40:41], v[64:65]\n"
    "v_lshlrev_b32 v66, 16, v165\n"
    "v_and_b32 v67, 0xffff0000, v165\n"
    "v_pk_add_f32 v[42:43], v[42:43], v[66:67]\n"
    "v_lshlrev_b32 v68, 16, v166\n"
    "v_and_b32 v69, 0xffff0000, v166\n"
    "v_pk_add_f32 v[44:45], v[44:45], v[68:69]\n"
    "v_lshlrev_b32 v70, 16, v167\n"
    "v_and_b32 v71, 0xffff0000, v167\n"
    "v_pk_add_f32 v[46:47], v[46:47], v[70:71]\n"
    "s_waitcnt vmcnt(1)\n"
    "v_lshlrev_b32 v64, 16, v168\n"
    "v_and_b32 v65, 0xffff0000, v168\n"
    "v_pk_add_f32 v[48:49], v[48:49], v[64:65]\n"
    "v_lshlrev_b32 v66, 16, v169\n"
    "v_and_b32 v67, 0xffff0000, v169\n"
    "v_pk_add_f32 v[50:51], v[50:51], v[66:67]\n"
    "v_lshlrev_b32 v68, 16, v170\n"
    "v_and_b32 v69, 0xffff0000, v170\n"
    "v_pk_add_f32 v[52:53], v[52:53], v[68:69]\n"
    "v_lshlrev_b32 v70, 16, v171\n"
    "v_and_b32 v71, 0xffff0000, v171\n"
    "v_pk_add_f32 v[54:55], v[54:55], v[70:71]\n"
    "s_waitcnt vmcnt(0)\n"
    "v_lshlrev_b32 v64, 16, v172\n"
    "v_and_b32 v65, 0xffff0000, v172\n"
    "v_pk_add_f32 v[56:57], v[56:57], v[64:65]\n"
    "v_lshlrev_b32 v66, 16, v173\n"
    "v_and_b32 v67, 0xffff0000, v173\n"
    "v_pk_add_f32 v[58:59], v[58:59], v[66:67]\n"
    "v_lshlrev_b32 v68, 16, v174\n"
    "v_and_b32 v69, 0xffff0000, v174\n"
    "v_pk_add_f32 v[60:61], v[60:61], v[68:69]\n"
    "v_lshlrev_b32 v70, 16, v175\n"
    "v_and_b32 v71, 0xffff0000, v175\n"
    "v_pk_add_f32 v[62:63], v[62:63], v[70:71]\n"
    "s_cmp_eq_u32 s82, 1\n"
    "s_cbranch_scc1 .Lk3_nos2\n"
    "global_load_dword v27, v26, s[34:35] sc1\n"
    ".Lk3_nos2:\n"
    "v_mul_f32 v64, 0xbfb8aa3b, v32\n"
    "v_mul_f32 v65, 0xbfb8aa3b, v33\n"
    "v_mul_f32 v66, 0xbfb8aa3b, v34\n"
    "v_mul_f32 v67, 0xbfb8aa3b, v35\n"
    "v_mul_f32 v68, 0xbfb8aa3b, v36\n"
    "v_mul_f32 v69, 0xbfb8aa3b, v37\n"
    "v_mul_f32 v70, 0xbfb8aa3b, v38\n"
    "v_mul_f32 v71, 0xbfb8aa3b, v39\n"
    "v_exp_f32 v64, v64\n"
    "v_exp_f32 v65, v65\n"
    "v_exp_f32 v66, v66\n"
    "v_exp_f32 v67, v67\n"
    "v_exp_f32 v68, v68\n"
    "v_exp_f32 v69, v69\n"
    "v_exp_f32 v70, v70\n"
    "v_exp_f32 v71, v71\n"
    "v_pk_add_f32 v[64:65], v[64:65], 1.0 op_sel_hi:[1,0]\n"
    "v_pk_add_f32 v[66:67], v[66:67], 1.0 op_sel_hi:[1,0]\n"
    "v_pk_add_f32 v[68:69], v[68:69], 1.0 op_sel_hi:[1,0]\n"
    "v_pk_add_f32 v[70:71], v[70:71], 1.0 op_sel_hi:[1,0]\n"
    "v_rcp_f32 v64, v64\n"
    "v_rcp_f32 v65, v65\n"
    "v_rcp_f32 v66, v66\n"
    "v_rcp_f32 v67, v67\n"
    "v_rcp_f32 v68, v68\n"
    "v_rcp_f32 v69, v69\n"
    "v_rcp_f32 v70, v70\n"
    "v_rcp_f32 v71, v71\n"
    "v_pk_mul_f32 v[32:33], v[32:33], v[64:65]\n"
    "v_pk_mul_f32 v[34:35], v[34:35], v[66:67]\n"
    "v_pk_mul_f32 v[36:37], v[36:37], v[68:69]\n"
    "v_pk_mul_f32 v[38:39], v[38:39], v[70:71]\n"
    "v_mul_f32 v64, 0xbfb8aa3b, v40\n"
    "v_mul_f32 v65, 0xbfb8aa3b, v41\n"
    "v_mul_f32 v66, 0xbfb8aa3b, v42\n"
    "v_mul_f32 v67, 0xbfb8aa3b, v43\n"
    "v_mul_f32 v68, 0xbfb8aa3b, v44\n"
    "v_mul_f32 v69, 0xbfb8aa3b, v45\n"
    "v_mul_f32 v70, 0xbfb8aa3b, v46\n"
    "v_mul_f32 v71, 0xbfb8aa3b, v47\n"
    "v_exp_f32 v64, v64\n"
    "v_exp_f32 v65, v65\n"
    "v_exp_f32 v66, v66\n"
    "v_exp_f32 v67, v67\n"
    "v_exp_f32 v68, v68\n"
    "v_exp_f32 v69, v69\n"
    "v_exp_f32 v70, v70\n"
    "v_exp_f32 v71, v71\n"
    "v_pk_add_f32 v[64:65], v[64:65], 1.0 op_sel_hi:[1,0]\n"
    "v_pk_add_f32 v[66:67], v[66:67], 1.0 op_sel_hi:[1,0]\n"
    "v_pk_add_f32 v[68:69], v[68:69], 1.0 op_sel_hi:[1,0]\n"
    "v_pk_add_f32 v[70:71], v[70:71], 1.0 op_sel_hi:[1,0]\n"
    "v_rcp_f32 v64, v64\n"
    "v_rcp_f32 v65, v65\n"
    "v_rcp_f32 v66, v66\n"
    "v_rcp_f32 v67, v67\n"
    "v_rcp_f32 v68, v68\n"
    "v_rcp_f32 v69, v69\n"
    "v_rcp_f32 v70, v70\n"
    "v_rcp_f32 v71, v71\n"
    "v_pk_mul_f32 v[40:41], v[40:41], v[64:65]\n"
    "v_pk_mul_f32 v[42:43], v[42:43], v[66:67]\n"
    "v_pk_mul_f32 v[44:45], v[44:45], v[68:69]\n"
    "v_pk_mul_f32 v[46:47], v[46:47], v[70:71]\n"
    "v_mul_f32 v64, 0xbfb8aa3b, v48\n"
    "v_mul_f32 v65, 0xbfb8aa3b, v49\n"
    "v_mul_f32 v66, 0xbfb8aa3b, v50\n"
    "v_mul_f32 v67, 0xbfb8aa3b, v51\n"
    "v_mul_f32 v68, 0xbfb8aa3b, v52\n"
    "v_mul_f32 v69, 0xbfb8aa3b, v53\n"
    "v_mul_f32 v70, 0xbfb8aa3b, v54\n"
    "v_mul_f32 v71, 0xbfb8aa3b, v55\n"
    "v_exp_f32 v64, v64\n"
    "v_exp_f32 v65, v65\n"
    "v_exp_f32 v66, v66\n"
    "v_exp_f32 v67, v67\n"
    "v_exp_f32 v68, v68\n"
    "v_exp_f32 v69, v69\n"
    "v_exp_f32 v70, v70\n"
    "v_exp_f32 v71, v71\n"
    "v_pk_add_f32 v[64:65], v[64:65], 1.0 op_sel_hi:[1,0]\n"
    "v_pk_add_f32 v[66:67], v[66:67], 1.0 op_sel_hi:[1,0]\n"
    "v_pk_add_f32 v[68:69], v[68:69], 1.0 op_sel_hi:[1,0]\n"
    "v_pk_add_f32 v[70:71], v[70:71], 1.0 op_sel_hi:[1,0]\n"
    "v_rcp_f32 v64, v64\n"
    "v_rcp_f32 v65, v65\n"
    "v_rcp_f32 v66, v66\n"
    "v_rcp_f32 v67, v67\n"
    "v_rcp_f32 v68, v68\n"
    "v_rcp_f32 v69, v69\n"
    "v_rcp_f32 v70, v70\n"
    "v_rcp_f32 v71, v71\n"
    "v_pk_mul_f32 v[48:49], v[48:49], v[64:65]\n"
    "v_pk_mul_f32 v[50:51], v[50:51], v[66:67]\n"
    "v_pk_mul_f32 v[52:53], v[52:53], v[68:69]\n"
    "v_pk_mul_f32 v[54:55], v[54:55], v[70:71]\n"
    "v_mul_f32 v64, 0xbfb8aa3b, v56\n"
    "v_mul_f32 v65, 0xbfb8aa3b, v57\n"
    "v_mul_f32 v66, 0xbfb8aa3b, v58\n"
    "v_mul_f32 v67, 0xbfb8aa3b, v59\n"
    "v_mul_f32 v68, 0xbfb8aa3b, v60\n"
    "v_mul_f32 v69, 0xbfb8aa3b, v61\n"
    "v_mul_f32 v70, 0xbfb8aa3b, v62\n"
    "v_mul_f32 v71, 0xbfb8aa3b, v63\n"
    "v_exp_f32 v64, v64\n"
    "v_exp_f32 v65, v65\n"
    "v_exp_f32 v66, v66\n"
    "v_exp_f32 v67, v67\n"
    "v_exp_f32 v68, v68\n"
    "v_exp_f32 v69, v69\n"
    "v_exp_f32 v70, v70\n"
    "v_exp_f32 v71, v71\n"
    "v_pk_add_f32 v[64:65], v[64:65], 1.0 op_sel_hi:[1,0]\n"
    "v_pk_add_f32 v[66:67], v[66:67], 1.0 op_sel_hi:[1,0]\n"
    "v_pk_add_f32 v[68:69], v[68:69], 1.0 op_sel_hi:[1,0]\n"
    "v_pk_add_f32 v[70:71], v[70:71], 1.0 op_sel_hi:[1,0]\n"
    "v_rcp_f32 v64, v64\n"
    "v_rcp_f32 v65, v65\n"
    "v_rcp_f32 v66, v66\n"
    "v_rcp_f32 v67, v67\n"
    "v_rcp_f32 v68, v68\n"
    "v_rcp_f32 v69, v69\n"
    "v_rcp_f32 v70, v70\n"
    "v_rcp_f32 v71, v71\n"
    "v_pk_mul_f32 v[56:57], v[56:57], v[64:65]\n"
    "v_pk_mul_f32 v[58:59], v[58:59], v[66:67]\n"
    "v_pk_mul_f32 v[60:61], v[60:61], v[68:69]\n"
    "v_pk_mul_f32 v[62:63], v[62:63], v[70:71]\n"
    "s_waitcnt vmcnt(0)\n"
    "s_cmp_eq_u32 s82, 1\n"
    "s_cbranch_scc1 .Lk3_haves\n"
    "s_mov_b32 s65, 0\n"
    ".Lk3_pollS:\n"
    "v_cmp_gt_u32 vcc, 1, v27\n"
    "s_nop 3\n"
    "s_cbranch_vccz .Lk3_pollS_ok\n"
    "s_sleep 1\n"
    "global_load_dword v27, v26, s[34:35] sc1\n"
    "s_add_u32 s65, s65, 1\n"
    "s_cmp_lt_u32 s65, 0x8000\n"
    "s_waitcnt vmcnt(0)\n"
    "s_cbranch_scc1 .Lk3_pollS\n"
    ".Lk3_pollS_ok:\n"
    "s_add_u32 s65, s86, 0x1000000\n"
    "buffer_load_dwordx4 v[176:179], v2, s[72:75], s65 offen offset:0 sc1\n"
    "buffer_load_dwordx4 v[180:183], v2, s[72:75], s65 offen offset:1024 sc1\n"
    "buffer_load_dwordx4 v[184:187], v2, s[72:75], s65 offen offset:2048 sc1\n"
    "buffer_load_dwordx4 v[188:191], v2, s[72:75], s65 offen offset:3072 sc1\n"
    "s_add_u32 s65, s86, 0x1004000\n"
    "buffer_load_dwordx4 v[192:195], v2, s[72:75], s65 offen offset:0 sc1\n"
    "buffer_load_dwordx4 v[196:199], v2, s[72:75], s65 offen offset:1024 sc1\n"
    "buffer_load_dwordx4 v[200:203], v2, s[72:75], s65 offen offset:2048 sc1\n"
    "buffer_load_dwordx4 v[204:207], v2, s[72:75], s65 offen offset:3072 sc1\n"
    "s_add_u32 s65, s86, 0x1008000\n"
    "buffer_load_dwordx4 v[208:211], v2, s[72:75], s65 offen offset:0 sc1\n"
    "buffer_load_dwordx4 v[212:215], v2, s[72:75], s65 offen offset:1024 sc1\n"
    "buffer_load_dwordx4 v[216:219], v2, s[72:75], s65 offen offset:2048 sc1\n"
    "buffer_load_dwordx4 v[220:223], v2, s[72:75], s65 offen offset:3072 sc1\n"
    "s_waitcnt vmcnt(8)\n"
    "v_lshlrev_b32 v64, 16, v176\n"
    "v_and_b32 v65, 0xffff0000, v176\n"
    "v_pk_add_f32 v[224:225], v[224:225], v[64:65]\n"
    "v_lshlrev_b32 v66, 16, v177\n"
    "v_and_b32 v67, 0xffff0000, v177\n"
    "v_pk_add_f32 v[226:227], v[226:227], v[66:67]\n"
    "v_lshlrev_b32 v68, 16, v178\n"
    "v_and_b32 v69, 0xffff0000, v178\n"
    "v_pk_add_f32 v[228:229], v[228:229], v[68:69]\n"
    "v_lshlrev_b32 v70, 16, v179\n"
    "v_and_b32 v71, 0xffff0000, v179\n"
    "v_pk_add_f32 v[230:231], v[230:231], v[70:71]\n"
    "v_lshlrev_b32 v64, 16, v180\n"
    "v_and_b32 v65, 0xffff0000, v180\n"
    "v_pk_add_f32 v[232:233], v[232:233], v[64:65]\n"
    "v_lshlrev_b32 v66, 16, v181\n"
    "v_and_b32 v67, 0xffff0000, v181\n"
    "v_pk_add_f32 v[234:235], v[234:235], v[66:67]\n"
    "v_lshlrev_b32 v68, 16, v182\n"
    "v_and_b32 v69, 0xffff0000, v182\n"
    "v_pk_add_f32 v[236:237], v[236:237], v[68:69]\n"
    "v_lshlrev_b32 v70, 16, v183\n"
    "v_and_b32 v71, 0xffff0000, v183\n"
    "v_pk_add_f32 v[238:239], v[238:239], v[70:71]\n"
    "v_lshlrev_b32 v64, 16, v184\n"
    "v_and_b32 v65, 0xffff0000, v184\n"
    "v_pk_add_f32 v[240:241], v[240:241], v[64:65]\n"
    "v_lshlrev_b32 v66, 16, v185\n"
    "v_and_b32 v67, 0xffff0000, v185\n"
    "v_pk_add_f32 v[242:243], v[242:243], v[66:67]\n"
    "v_lshlrev_b32 v68, 16, v186\n"
    "v_and_b32 v69, 0xffff0000, v186\n"
    "v_pk_add_f32 v[244:245], v[244:245], v[68:69]\n"
    "v_lshlrev_b32 v70, 16, v187\n"
    "v_and_b32 v71, 0xffff0000, v187\n"
    "v_pk_add_f32 v[246:247], v[246:247], v[70:71]\n"
    "v_lshlrev_b32 v64, 16, v188\n"
    "v_and_b32 v65, 0xffff0000, v188\n"
    "v_pk_add_f32 v[248:249], v[248:249], v[64:65]\n"
    "v_lshlrev_b32 v66, 16, v189\n"
    "v_and_b32 v67, 0xffff0000, v189\n"
    "v_pk_add_f32 v[250:251], v[250:251], v[66:67]\n"
    "v_lshlrev_b32 v68, 16, v190\n"
    "v_and_b32 v69, 0xffff0000, v190\n"
    "v_pk_add_f32 v[252:253], v[252:253], v[68:69]\n"
    "v_lshlrev_b32 v70, 16, v191\n"
    "v_and_b32 v71, 0xffff0000, v191\n"
    "v_pk_add_f32 v[254:255], v[254:255], v[70:71]\n"
    "s_waitcnt vmcnt(4)\n"
    "v_lshlrev_b32 v64, 16, v192\n"
    "v_and_b32 v65, 0xffff0000, v192\n"
    "v_pk_add_f32 v[224:225], v[224:225], v[64:65]\n"
    "v_lshlrev_b32 v66, 16, v193\n"
    "v_and_b32 v67, 0xffff0000, v193\n"
    "v_pk_add_f32 v[226:227], v[226:227], v[66:67]\n"
    "v_lshlrev_b32 v68, 16, v194\n"
    "v_and_b32 v69, 0xffff0000, v194\n"
    "v_pk_add_f32 v[228:229], v[228:229], v[68:69]\n"
    "v_lshlrev_b32 v70, 16, v195\n"
    "v_and_b32 v71, 0xffff0000, v195\n"
    "v_pk_add_f32 v[230:231], v[230:231], v[70:71]\n"
    "v_lshlrev_b32 v64, 16, v196\n"
    "v_and_b32 v65, 0xffff0000, v196\n"
    "v_pk_add_f32 v[232:233], v[232:233], v[64:65]\n"
    "v_lshlrev_b32 v66, 16, v197\n"
    "v_and_b32 v67, 0xffff0000, v197\n"
    "v_pk_add_f32 v[234:235], v[234:235], v[66:67]\n"
    "v_lshlrev_b32 v68, 16, v198\n"
    "v_and_b32 v69, 0xffff0000, v198\n"
    "v_pk_add_f32 v[236:237], v[236:237], v[68:69]\n"
    "v_lshlrev_b32 v70, 16, v199\n"
    "v_and_b32 v71, 0xffff0000, v199\n"
    "v_pk_add_f32 v[238:239], v[238:239], v[70:71]\n"
    "v_lshlrev_b32 v64, 16, v200\n"
    "v_and_b32 v65, 0xffff0000, v200\n"
    "v_pk_add_f32 v[240:241], v[240:241], v[64:65]\n"
    "v_lshlrev_b32 v66, 16, v201\n"
    "v_and_b32 v67, 0xffff0000, v201\n"
    "v_pk_add_f32 v[242:243], v[242:243], v[66:67]\n"
    "v_lshlrev_b32 v68, 16, v202\n"
    "v_and_b32 v69, 0xffff0000, v202\n"
    "v_pk_add_f32 v[244:245], v[244:245], v[68:69]\n"
    "v_lshlrev_b32 v70, 16, v203\n"
    "v_and_b32 v71, 0xffff0000, v203\n"
    "v_pk_add_f32 v[246:247], v[246:247], v[70:71]\n"
    "v_lshlrev_b32 v64, 16, v204\n"
    "v_and_b32 v65, 0xffff0000, v204\n"
    "v_pk_add_f32 v[248:249], v[248:249], v[64:65]\n"
    "v_lshlrev_b32 v66, 16, v205\n"
    "v_and_b32 v67, 0xffff0000, v205\n"
    "v_pk_add_f32 v[250:251], v[250:251], v[66:67]\n"
    "v_lshlrev_b32 v68, 16, v206\n"
    "v_and_b32 v69, 0xffff0000, v206\n"
    "v_pk_add_f32 v[252:253], v[252:253], v[68:69]\n"
    "v_lshlrev_b32 v70, 16, v207\n"
    "v_and_b32 v71, 0xffff0000, v207\n"
    "v_pk_add_f32 v[254:255], v[254:255], v[70:71]\n"
    "s_waitcnt vmcnt(0)\n"
    "v_lshlrev_b32 v64, 16, v208\n"
    "v_and_b32 v65, 0xffff0000, v208\n"
    "v_pk_add_f32 v[224:225], v[224:225], v[64:65]\n"
    "v_lshlrev_b32 v66, 16, v209\n"
    "v_and_b32 v67, 0xffff0000, v209\n"
    "v_pk_add_f32 v[226:227], v[226:227], v[66:67]\n"
    "v_lshlrev_b32 v68, 16, v210\n"
    "v_and_b32 v69, 0xffff0000, v210\n"
    "v_pk_add_f32 v[228:229], v[228:229], v[68:69]\n"
    "v_lshlrev_b32 v70, 16, v211\n"
    "v_and_b32 v71, 0xffff0000, v211\n"
    "v_pk_add_f32 v[230:231], v[230:231], v[70:71]\n"
    "v_lshlrev_b32 v64, 16, v212\n"
    "v_and_b32 v65, 0xffff0000, v212\n"
    "v_pk_add_f32 v[232:233], v[232:233], v[64:65]\n"
    "v_lshlrev_b32 v66, 16, v213\n"
    "v_and_b32 v67, 0xffff0000, v213\n"
    "v_pk_add_f32 v[234:235], v[234:235], v[66:67]\n"
    "v_lshlrev_b32 v68, 16, v214\n"
    "v_and_b32 v69, 0xffff0000, v214\n"
    "v_pk_add_f32 v[236:237], v[236:237], v[68:69]\n"
    "v_lshlrev_b32 v70, 16, v215\n"
    "v_and_b32 v71, 0xffff0000, v215\n"
    "v_pk_add_f32 v[238:239], v[238:239], v[70:71]\n"
    "v_lshlrev_b32 v64, 16, v216\n"
    "v_and_b32 v65, 0xffff0000, v216\n"
    "v_pk_add_f32 v[240:241], v[240:241], v[64:65]\n"
    "v_lshlrev_b32 v66, 16, v217\n"
    "v_and_b32 v67, 0xffff0000, v217\n"
    "v_pk_add_f32 v[242:243], v[242:243], v[66:67]\n"
    "v_lshlrev_b32 v68, 16, v218\n"
    "v_and_b32 v69, 0xffff0000, v218\n"
    "v_pk_add_f32 v[244:245], v[244:245], v[68:69]\n"
    "v_lshlrev_b32 v70, 16, v219\n"
    "v_and_b32 v71, 0xffff0000, v219\n"
    "v_pk_add_f32 v[246:247], v[246:247], v[70:71]\n"
    "v_lshlrev_b32 v64, 16, v220\n"
    "v_and_b32 v65, 0xffff0000, v220\n"
    "v_pk_add_f32 v[248:249], v[248:249], v[64:65]\n"
    "v_lshlrev_b32 v66, 16, v221\n"
    "v_and_b32 v67, 0xffff0000, v221\n"
    "v_pk_add_f32 v[250:251], v[250:251], v[66:67]\n"
    "v_lshlrev_b32 v68, 16, v222\n"
    "v_and_b32 v69, 0xffff0000, v222\n"
    "v_pk_add_f32 v[252:253], v[252:253], v[68:69]\n"
    "v_lshlrev_b32 v70, 16, v223\n"
    "v_and_b32 v71, 0xffff0000, v223\n"
    "v_pk_add_f32 v[254:255], v[254:255], v[70:71]\n"
    "s_branch .Lk3_sdone\n"
    ".Lk3_haves:\n"
    "v_lshlrev_b32 v64, 16, v176\n"
    "v_and_b32 v65, 0xffff0000, v176\n"
    "v_pk_add_f32 v[224:225], v[224:225], v[64:65]\n"
    "v_lshlrev_b32 v66, 16, v177\n"
    "v_and_b32 v67, 0xffff0000, v177\n"
    "v_pk_add_f32 v[226:227], v[226:227], v[66:67]\n"
    "v_lshlrev_b32 v68, 16, v178\n"
    "v_and_b32 v69, 0xffff0000, v178\n"
    "v_pk_add_f32 v[228:229], v[228:229], v[68:69]\n"
    "v_lshlrev_b32 v70, 16, v179\n"
    "v_and_b32 v71, 0xffff0000, v179\n"
    "v_pk_add_f32 v[230:231], v[230:231], v[70:71]\n"
    "v_lshlrev_b32 v64, 16, v180\n"
    "v_and_b32 v65, 0xffff0000, v180\n"
    "v_pk_add_f32 v[232:233], v[232:233], v[64:65]\n"
    "v_lshlrev_b32 v66, 16, v181\n"
    "v_and_b32 v67, 0xffff0000, v181\n"
    "v_pk_add_f32 v[234:235], v[234:235], v[66:67]\n"
    "v_lshlrev_b32 v68, 16, v182\n"
    "v_and_b32 v69, 0xffff0000, v182\n"
    "v_pk_add_f32 v[236:237], v[236:237], v[68:69]\n"
    "v_lshlrev_b32 v70, 16, v183\n"
    "v_and_b32 v71, 0xffff0000, v183\n"
    "v_pk_add_f32 v[238:239], v[238:239], v[70:71]\n"
    "v_lshlrev_b32 v64, 16, v184\n"
    "v_and_b32 v65, 0xffff0000, v184\n"
    "v_pk_add_f32 v[240:241], v[240:241], v[64:65]\n"
    "v_lshlrev_b32 v66, 16, v185\n"
    "v_and_b32 v67, 0xffff0000, v185\n"
    "v_pk_add_f32 v[242:243], v[242:243], v[66:67]\n"
    "v_lshlrev_b32 v68, 16, v186\n"
    "v_and_b32 v69, 0xffff0000, v186\n"
    "v_pk_add_f32 v[244:245], v[244:245], v[68:69]\n"
    "v_lshlrev_b32 v70, 16, v187\n"
    "v_and_b32 v71, 0xffff0000, v187\n"
    "v_pk_add_f32 v[246:247], v[246:247], v[70:71]\n"
    "v_lshlrev_b32 v64, 16, v188\n"
    "v_and_b32 v65, 0xffff0000, v188\n"
    "v_pk_add_f32 v[248:249], v[248:249], v[64:65]\n"
    "v_lshlrev_b32 v66, 16, v189\n"
    "v_and_b32 v67, 0xffff0000, v189\n"
    "v_pk_add_f32 v[250:251], v[250:251], v[66:67]\n"
    "v_lshlrev_b32 v68, 16, v190\n"
    "v_and_b32 v69, 0xffff0000, v190\n"
    "v_pk_add_f32 v[252:253], v[252:253], v[68:69]\n"
    "v_lshlrev_b32 v70, 16, v191\n"
    "v_and_b32 v71, 0xffff0000, v191\n"
    "v_pk_add_f32 v[254:255], v[254:255], v[70:71]\n"
    "v_lshlrev_b32 v64, 16, v192\n"
    "v_and_b32 v65, 0xffff0000, v192\n"
    "v_pk_add_f32 v[224:225], v[224:225], v[64:65]\n"
    "v_lshlrev_b32 v66, 16, v193\n"
    "v_and_b32 v67, 0xffff0000, v193\n"
    "v_pk_add_f32 v[226:227], v[226:227], v[66:67]\n"
    "v_lshlrev_b32 v68, 16, v194\n"
    "v_and_b32 v69, 0xffff0000, v194\n"
    "v_pk_add_f32 v[228:229], v[228:229], v[68:69]\n"
    "v_lshlrev_b32 v70, 16, v195\n"
    "v_and_b32 v71, 0xffff0000, v195\n"
    "v_pk_add_f32 v[230:231], v[230:231], v[70:71]\n"
    "v_lshlrev_b32 v64, 16, v196\n"
    "v_and_b32 v65, 0xffff0000, v196\n"
    "v_pk_add_f32 v[232:233], v[232:233], v[64:65]\n"
    "v_lshlrev_b32 v66, 16, v197\n"
    "v_and_b32 v67, 0xffff0000, v197\n"
    "v_pk_add_f32 v[234:235], v[234:235], v[66:67]\n"
    "v_lshlrev_b32 v68, 16, v198\n"
    "v_and_b32 v69, 0xffff0000, v198\n"
    "v_pk_add_f32 v[236:237], v[236:237], v[68:69]\n"
    "v_lshlrev_b32 v70, 16, v199\n"
    "v_and_b32 v71, 0xffff0000, v199\n"
    "v_pk_add_f32 v[238:239], v[238:239], v[70:71]\n"
    "v_lshlrev_b32 v64, 16, v200\n"
    "v_and_b32 v65, 0xffff0000, v200\n"
    "v_pk_add_f32 v[240:241], v[240:241], v[64:65]\n"
    "v_lshlrev_b32 v66, 16, v201\n"
    "v_and_b32 v67, 0xffff0000, v201\n"
    "v_pk_add_f32 v[242:243], v[242:243], v[66:67]\n"
    "v_lshlrev_b32 v68, 16, v202\n"
    "v_and_b32 v69, 0xffff0000, v202\n"
    "v_pk_add_f32 v[244:245], v[244:245], v[68:69]\n"
    "v_lshlrev_b32 v70, 16, v203\n"
    "v_and_b32 v71, 0xffff0000, v203\n"
    "v_pk_add_f32 v[246:247], v[246:247], v[70:71]\n"
    "v_lshlrev_b32 v64, 16, v204\n"
    "v_and_b32 v65, 0xffff0000, v204\n"
    "v_pk_add_f32 v[248:249], v[248:249], v[64:65]\n"
    "v_lshlrev_b32 v66, 16, v205\n"
    "v_and_b32 v67, 0xffff0000, v205\n"
    "v_pk_add_f32 v[250:251], v[250:251], v[66:67]\n"
    "v_lshlrev_b32 v68, 16, v206\n"
    "v_and_b32 v69, 0xffff0000, v206\n"
    "v_pk_add_f32 v[252:253], v[252:253], v[68:69]\n"
    "v_lshlrev_b32 v70, 16, v207\n"
    "v_and_b32 v71, 0xffff0000, v207\n"
    "v_pk_add_f32 v[254:255], v[254:255], v[70:71]\n"
    "v_lshlrev_b32 v64, 16, v208\n"
    "v_and_b32 v65, 0xffff0000, v208\n"
    "v_pk_add_f32 v[224:225], v[224:225], v[64:65]\n"
    "v_lshlrev_b32 v66, 16, v209\n"
    "v_and_b32 v67, 0xffff0000, v209\n"
    "v_pk_add_f32 v[226:227], v[226:227], v[66:67]\n"
    "v_lshlrev_b32 v68, 16, v210\n"
    "v_and_b32 v69, 0xffff0000, v210\n"
    "v_pk_add_f32 v[228:229], v[228:229], v[68:69]\n"
    "v_lshlrev_b32 v70, 16, v211\n"
    "v_and_b32 v71, 0xffff0000, v211\n"
    "v_pk_add_f32 v[230:231], v[230:231], v[70:71]\n"
    "v_lshlrev_b32 v64, 16, v212\n"
    "v_and_b32 v65, 0xffff0000, v212\n"
    "v_pk_add_f32 v[232:233], v[232:233], v[64:65]\n"
    "v_lshlrev_b32 v66, 16, v213\n"
    "v_and_b32 v67, 0xffff0000, v213\n"
    "v_pk_add_f32 v[234:235], v[234:235], v[66:67]\n"
    "v_lshlrev_b32 v68, 16, v214\n"
    "v_and_b32 v69, 0xffff0000, v214\n"
    "v_pk_add_f32 v[236:237], v[236:237], v[68:69]\n"
    "v_lshlrev_b32 v70, 16, v215\n"
    "v_and_b32 v71, 0xffff0000, v215\n"
    "v_pk_add_f32 v[238:239], v[238:239], v[70:71]\n"
    "v_lshlrev_b32 v64, 16, v216\n"
    "v_and_b32 v65, 0xffff0000, v216\n"
    "v_pk_add_f32 v[240:241], v[240:241], v[64:65]\n"
    "v_lshlrev_b32 v66, 16, v217\n"
    "v_and_b32 v67, 0xffff0000, v217\n"
    "v_pk_add_f32 v[242:243], v[242:243], v[66:67]\n"
    "v_lshlrev_b32 v68, 16, v218\n"
    "v_and_b32 v69, 0xffff0000, v218\n"
    "v_pk_add_f32 v[244:245], v[244:245], v[68:69]\n"
    "v_lshlrev_b32 v70, 16, v219\n"
    "v_and_b32 v71, 0xffff0000, v219\n"
    "v_pk_add_f32 v[246:247], v[246:247], v[70:71]\n"
    "v_lshlrev_b32 v64, 16, v220\n"
    "v_and_b32 v65, 0xffff0000, v220\n"
    "v_pk_add_f32 v[248:249], v[248:249], v[64:65]\n"
    "v_lshlrev_b32 v66, 16, v221\n"
    "v_and_b32 v67, 0xffff0000, v221\n"
    "v_pk_add_f32 v[250:251], v[250:251], v[66:67]\n"
    "v_lshlrev_b32 v68, 16, v222\n"
    "v_and_b32 v69, 0xffff0000, v222\n"
    "v_pk_add_f32 v[252:253], v[252:253], v[68:69]\n"
    "v_lshlrev_b32 v70, 16, v223\n"
    "v_and_b32 v71, 0xffff0000, v223\n"
    "v_pk_add_f32 v[254:255], v[254:255], v[70:71]\n"
    ".Lk3_sdone:\n"
    "v_pk_mul_f32 v[224:225], v[224:225], v[32:33]\n"
    "v_pk_mul_f32 v[226:227], v[226:227], v[34:35]\n"
    "v_cvt_pk_bf16_f32 v112, v224, v225\n"
    "v_cvt_pk_bf16_f32 v113, v226, v227\n"
    "ds_write_b64 v3, v[112:113] offset:0\n"
    "v_pk_mul_f32 v[228:229], v[228:229], v[36:37]\n"
    "v_pk_mul_f32 v[230:231], v[230:231], v[38:39]\n"
    "v_cvt_pk_bf16_f32 v114, v228, v229\n"
    "v_cvt_pk_bf16_f32 v115, v230, v231\n"
    "ds_write_b64 v3, v[114:115] offset:1280\n"
    "v_pk_mul_f32 v[232:233], v[232:233], v[40:41]\n"
    "v_pk_mul_f32 v[234:235], v[234:235], v[42:43]\n"
    "v_cvt_pk_bf16_f32 v116, v232, v233\n"
    "v_cvt_pk_bf16_f32 v117, v234, v235\n"
    "ds_write_b64 v3, v[116:117] offset:2560\n"
    "v_pk_mul_f32 v[236:237], v[236:237], v[44:45]\n"
    "v_pk_mul_f32 v[238:239], v[238:239], v[46:47]\n"
    "v_cvt_pk_bf16_f32 v118, v236, v237\n"
    "v_cvt_pk_bf16_f32 v119, v238, v239\n"
    "ds_write_b64 v3, v[118:119] offset:3840\n"
    "v_pk_mul_f32 v[240:241], v[240:241], v[48:49]\n"
    "v_pk_mul_f32 v[242:243], v[242:243], v[50:51]\n"
    "v_cvt_pk_bf16_f32 v120, v240, v241\n"
    "v_cvt_pk_bf16_f32 v121, v242, v243\n"
    "ds_write_b64 v3, v[120:121] offset:32\n"
    "v_pk_mul_f32 v[244:245], v[244:245], v[52:53]\n"
    "v_pk_mul_f32 v[246:247], v[246:247], v[54:55]\n"
    "v_cvt_pk_bf16_f32 v122, v244, v245\n"
    "v_cvt_pk_bf16_f32 v123, v246, v247\n"
    "ds_write_b64 v3, v[122:123] offset:1312\n"
    "v_pk_mul_f32 v[248:249], v[248:249], v[56:57]\n"
    "v_pk_mul_f32 v[250:251], v[250:251], v[58:59]\n"
    "v_cvt_pk_bf16_f32 v124, v248, v249\n"
    "v_cvt_pk_bf16_f32 v125, v250, v251\n"
    "ds_write_b64 v3, v[124:125] offset:2592\n"
    "v_pk_mul_f32 v[252:253], v[252:253], v[60:61]\n"
    "v_pk_mul_f32 v[254:255], v[254:255], v[62:63]\n"
    "v_cvt_pk_bf16_f32 v126, v252, v253\n"
    "v_cvt_pk_bf16_f32 v127, v254, v255\n"
    "ds_write_b64 v3, v[126:127] offset:3872\n"
    "s_waitcnt lgkmcnt(0)\n"
    "ds_read_b128 v[128:131], v4 offset:0\n"
    "ds_read_b128 v[132:135], v4 offset:1280\n"
    "ds_read_b128 v[136:139], v4 offset:2560\n"
    "ds_read_b128 v[140:143], v4 offset:3840\n"
    "s_waitcnt lgkmcnt(3)\n"
    "v_lshlrev_b32 v64, 16, v128\n"
    "v_and_b32 v65, 0xffff0000, v128\n"
    "v_lshlrev_b32 v66, 16, v129\n"
    "v_and_b32 v67, 0xffff0000, v129\n"
    "v_lshlrev_b32 v68, 16, v130\n"
    "v_and_b32 v69, 0xffff0000, v130\n"
    "v_lshlrev_b32 v70, 16, v131\n"
    "v_and_b32 v71, 0xffff0000, v131\n"
    "v_pk_add_f32 v[64:65], v[64:65], v[80:81]\n"
    "v_pk_add_f32 v[66:67], v[66:67], v[82:83]\n"
    "v_pk_add_f32 v[68:69], v[68:69], v[84:85]\n"
    "v_pk_add_f32 v[70:71], v[70:71], v[86:87]\n"
    "v_cvt_pk_bf16_f32 v112, v64, v65\n"
    "v_cvt_pk_bf16_f32 v113, v66, v67\n"
    "v_cvt_pk_bf16_f32 v114, v68, v69\n"
    "v_cvt_pk_bf16_f32 v115, v70, v71\n"
    "s_mov_b32 s65, 0\n"
    "buffer_store_dwordx4 v[112:115], v9, s[56:59], s65 offen sc1\n"
    "s_waitcnt lgkmcnt(2)\n"
    "v_lshlrev_b32 v72, 16, v132\n"
    "v_and_b32 v73, 0xffff0000, v132\n"
    "v_lshlrev_b32 v74, 16, v133\n"
    "v_and_b32 v75, 0xffff0000, v133\n"
    "v_lshlrev_b32 v76, 16, v134\n"
    "v_and_b32 v77, 0xffff0000, v134\n"
    "v_lshlrev_b32 v78, 16, v135\n"
    "v_and_b32 v79, 0xffff0000, v135\n"
    "v_pk_add_f32 v[72:73], v[72:73], v[88:89]\n"
    "v_pk_add_f32 v[74:75], v[74:75], v[90:91]\n"
    "v_pk_add_f32 v[76:77], v[76:77], v[92:93]\n"
    "v_pk_add_f32 v[78:79], v[78:79], v[94:95]\n"
    "v_cvt_pk_bf16_f32 v116, v72, v73\n"
    "v_cvt_pk_bf16_f32 v117, v74, v75\n"
    "v_cvt_pk_bf16_f32 v118, v76, v77\n"
    "v_cvt_pk_bf16_f32 v119, v78, v79\n"
    "s_mov_b32 s65, 32768\n"
    "buffer_store_dwordx4 v[116:119], v9, s[56:59], s65 offen sc1\n"
    "s_waitcnt lgkmcnt(1)\n"
    "v_lshlrev_b32 v64, 16, v136\n"
    "v_and_b32 v65, 0xffff0000, v136\n"
    "v_lshlrev_b32 v66, 16, v137\n"
    "v_and_b32 v67, 0xffff0000, v137\n"
    "v_lshlrev_b32 v68, 16, v138\n"
    "v_and_b32 v69, 0xffff0000, v138\n"
    "v_lshlrev_b32 v70, 16, v139\n"
    "v_and_b32 v71, 0xffff0000, v139\n"
    "v_pk_add_f32 v[64:65], v[64:65], v[96:97]\n"
    "v_pk_add_f32 v[66:67], v[66:67], v[98:99]\n"
    "v_pk_add_f32 v[68:69], v[68:69], v[100:101]\n"
    "v_pk_add_f32 v[70:71], v[70:71], v[102:103]\n"
    "v_cvt_pk_bf16_f32 v120, v64, v65\n"
    "v_cvt_pk_bf16_f32 v121, v66, v67\n"
    "v_cvt_pk_bf16_f32 v122, v68, v69\n"
    "v_cvt_pk_bf16_f32 v123, v70, v71\n"
    "s_mov_b32 s65, 65536\n"
    "buffer_store_dwordx4 v[120:123], v9, s[56:59], s65 offen sc1\n"
    "s_waitcnt lgkmcnt(0)\n"
    "v_lshlrev_b32 v72, 16, v140\n"
    "v_and_b32 v73, 0xffff0000, v140\n"
    "v_lshlrev_b32 v74, 16, v141\n"
    "v_and_b32 v75, 0xffff0000, v141\n"
    "v_lshlrev_b32 v76, 16, v142\n"
    "v_and_b32 v77, 0xffff0000, v142\n"
    "v_lshlrev_b32 v78, 16, v143\n"
    "v_and_b32 v79, 0xffff0000, v143\n"
    "v_pk_add_f32 v[72:73], v[72:73], v[104:105]\n"
    "v_pk_add_f32 v[74:75], v[74:75], v[106:107]\n"
    "v_pk_add_f32 v[76:77], v[76:77], v[108:109]\n"
    "v_pk_add_f32 v[78:79], v[78:79], v[110:111]\n"
    "v_cvt_pk_bf16_f32 v124, v72, v73\n"
    "v_cvt_pk_bf16_f32 v125, v74, v75\n"
    "v_cvt_pk_bf16_f32 v126, v76, v77\n"
    "v_cvt_pk_bf16_f32 v127, v78, v79\n"
    "s_mov_b32 s65, 98304\n"
    "buffer_store_dwordx4 v[124:127], v9, s[56:59], s65 offen sc1\n"
    "s_waitcnt vmcnt(0)\n"
    "s_endpgm\n"
    ".Lk2_gev:\n"
    "s_lshl_b32 s86, s41, 19\n"
    "s_and_b32 s87, s40, 1\n"
    "s_cmp_eq_u32 s87, 0\n"
    "s_cselect_b64 s[60:61], %5, %6\n"
    "s_add_u32 s60, s60, s86\n"
    "s_addc_u32 s61, s61, 0\n"
    "s_and_b32 s61, s61, 0xffff\n"
    "s_mov_b32 s62, 0x80000\n"
    "s_mov_b32 s63, 0x20000\n"
    "s_lshr_b32 s87, s40, 1\n"
    "s_lshl_b32 s86, s42, 1\n"
    "s_add_u32 s86, s86, s87\n"
    "s_lshl_b32 s86, s86, 12\n"
    "v_lshlrev_b32 v12, 6, v1\n"
    "buffer_load_dword v31, v12, s[60:63], s86 offen\n"
    "s_branch .Lk2_gev_ret\n"
    ".Lk2_ev:\n"
    "s_cmp_gt_u32 s43, 8\n"
    "s_cbranch_scc1 .Lk2_evb\n"
    "s_cmp_eq_u32 s43, 0\n"
    "s_cselect_b32 s50, 0x100000, s50\n"
    "s_cmp_eq_u32 s43, 7\n"
    "s_cselect_b32 s46, 0, s46\n"
    "s_cmp_eq_u32 s43, 8\n"
    "s_cselect_b32 s50, 0, s50\n"
    "s_cmp_eq_u32 s43, 3\n"
    "s_cbranch_scc0 .Lk2_c4\n"
    "s_lshl_b32 s87, s83, 8\n"
    "s_mov_b64 s[44:45], %8\n"
    "s_add_u32 s44, s44, s87\n"
    "s_addc_u32 s45, s45, 0\n"
    "s_and_b32 s45, s45, 0xffff\n"
    "s_sub_u32 s46, 0x700000, s87\n"
    "s_mov_b32 s92, s84\n"
    ".Lk2_c4:\n"
    "s_cmp_eq_u32 s43, 4\n"
    "s_cbranch_scc0 .Lk2_ev_ret\n"
    "s_mul_i32 s87, s83, 0xe0000\n"
    "s_add_u32 s87, s87, 0x3000000\n"
    "s_add_u32 s48, s34, s87\n"
    "s_addc_u32 s49, s35, 0\n"
    "s_and_b32 s49, s49, 0xffff\n"
    "s_mov_b32 s50, 0xe0000\n"
    "s_mov_b32 s93, s85\n"
    "s_branch .Lk2_ev_ret\n"
    ".Lk2_evb:\n"
    "s_cmp_eq_u32 s43, 13\n"
    "s_cbranch_scc1 .Lk2_ev13\n"
    "v_mov_b32 v12, 0x2620800\n"
    "s_cmp_eq_u32 s43, 12\n"
    "s_cbranch_scc1 .Lk2_ev12\n"
    "s_cmp_eq_u32 s40, 0\n"
    "s_cbranch_scc0 .Lk2_ev_ret\n"
    "v_mov_b32 v13, 1\n"
    "s_mov_b64 exec, 1\n"
    "global_atomic_add v12, v13, s[34:35] sc1\n"
    "s_mov_b64 exec, -1\n"
    "s_branch .Lk2_ev_ret\n"
    ".Lk2_ev12:\n"
    "s_getpc_b64 s[72:73]\n"
    ".Lk2_pc2:\n"
    "s_sub_u32 s72, s72, .Lk2_pc2-.Lk2_oepi\n"
    "s_subb_u32 s73, s73, 0\n"
    "s_and_b32 s73, s73, 0xffff\n"
    "s_mov_b32 s74, .Lk2_pc2-.Lk2_oepi\n"
    "s_mov_b32 s75, 0x20000\n"
    "s_lshl_b32 s86, s40, 12\n"
    "v_lshlrev_b32 v14, 6, v1\n"
    "buffer_load_dword v15, v14, s[72:75], s86 offen\n"
    "s_add_u32 s86, s86, 16384\n"
    "buffer_load_dword v15, v14, s[72:75], s86 offen\n"
    "v_mov_b32 v13, 0\n"
    "global_load_dword v13, v12, s[34:35] sc1\n"
    "s_branch .Lk2_ev_ret\n"
    ".Lk2_ev13:\n"
    "s_mov_b32 s36, s38\n"
    "s_mov_b32 s86, 0\n"
    ".Lk3_pollA:\n"
    "v_readfirstlane_b32 s87, v13\n"
    "s_nop 3\n"
    "s_cmp_ge_u32 s87, 256\n"
    "s_cbranch_scc1 .Lk3_pollA_ok\n"
    "s_sleep 1\n"
    "global_load_dword v13, v12, s[34:35] sc1\n"
    "s_add_u32 s86, s86, 1\n"
    "s_cmp_lt_u32 s86, 0x8000\n"
    "s_waitcnt vmcnt(0)\n"
    "s_cbranch_scc1 .Lk3_pollA\n"
    ".Lk3_pollA_ok:\n"
    "s_bfe_u32 s88, s36, 0x20001\n"
    "s_lshr_b32 s86, s36, 5\n"
    "s_lshl_b32 s86, s86, 2\n"
    "s_add_u32 s86, s86, s88\n"
    "s_mul_i32 s87, s86, 0x38000\n"
    "s_add_u32 s87, s87, 0x2fffc00\n"
    "s_add_u32 s60, s34, s87\n"
    "s_addc_u32 s61, s35, 0\n"
    "s_and_b32 s61, s61, 0xffff\n"
    "s_mov_b32 s62, 0x10400\n"
    "s_mov_b32 s63, 0x20000\n"
    "s_mov_b32 s66, 0\n"
    "s_lshl_b32 s86, s40, 12\n"
    "v_lshrrev_b32 v14, 5, v1\n"
    "v_add3_u32 v14, s88, v14, 1\n"
    "v_and_b32 v15, 31, v1\n"
    "v_lshlrev_b32 v15, 4, v15\n"
    "v_add_u32 v15, s86, v15\n"
    "v_and_b32 v3, 3, v14\n"
    "v_lshl_add_u32 v3, v3, 9, v15\n"
    "v_add_u32 v3, 1024, v3\n"
    "v_add_u32 v4, 2, v14\n"
    "v_and_b32 v4, 3, v4\n"
    "v_lshl_add_u32 v4, v4, 9, v15\n"
    "v_mov_b32 v5, v3\n"
    "v_mov_b32 v6, v4\n"
    "s_mov_b32 s80, 0\n"
    "s_branch .Lk2_ev_ret\n"
    ".Lk2_slow:\n"
    "s_waitcnt vmcnt(0) lgkmcnt(0)\n"
    "s_nop 7\n"
    "s_nop 7\n"
    "s_lshl_b32 s86, s41, 19\n"
    "s_mov_b64 s[60:61], %5\n"
    "s_add_u32 s60, s60, s86\n"
    "s_addc_u32 s61, s61, 0\n"
    "s_and_b32 s61, s61, 0xffff\n"
    "s_mov_b32 s62, 0x80000\n"
    "s_mov_b32 s63, 0x20000\n"
    "s_mov_b64 s[64:65], %6\n"
    "s_add_u32 s64, s64, s86\n"
    "s_addc_u32 s65, s65, 0\n"
    "s_and_b32 s65, s65, 0xffff\n"
    "s_mov_b32 s66, 0x80000\n"
    "s_mov_b32 s67, 0x20000\n"
    "v_bfe_u32 v12, v1, 2, 3\n"
    "v_lshrrev_b32 v13, 2, v12\n"
    "v_lshlrev_b32 v13, 1, v13\n"
    "v_and_b32 v14, 3, v1\n"
    "v_xor_b32 v14, v14, v13\n"
    "v_lshlrev_b32 v14, 4, v14\n"
    "v_lshrrev_b32 v13, 5, v1\n"
    "v_lshl_add_u32 v14, v13, 6, v14\n"
    "v_lshl_add_u32 v49, v12, 8, v14\n"
    "v_and_b32 v14, 15, v1\n"
    "v_lshrrev_b32 v15, 4, v1\n"
    "v_lshlrev_b32 v15, 4, v15\n"
    "v_lshl_add_u32 v48, v14, 8, v15\n"
    "s_lshl_b32 s87, s40, 13\n"
    "s_add_u32 s87, s87, 0x20000\n"
    "s_add_u32 s87, s87, s39\n"
    "v_and_b32 v50, 0xfff, v20\n"
    "v_add_u32 v50, s87, v50\n"
    "v_and_b32 v51, 0xfff, v21\n"
    "v_add_u32 v51, s87, v51\n"
    "v_mov_b64 v[128:129], 0\n"
    "v_mov_b64 v[130:131], 0\n"
    "v_mov_b64 v[132:133], 0\n"
    "v_mov_b64 v[134:135], 0\n"
    "v_mov_b64 v[136:137], 0\n"
    "v_mov_b64 v[138:139], 0\n"
    "v_mov_b64 v[140:141], 0\n"
    "v_mov_b64 v[142:143], 0\n"
    "v_mov_b64 v[144:145], 0\n"
    "v_mov_b64 v[146:147], 0\n"
    "v_mov_b64 v[148:149], 0\n"
    "v_mov_b64 v[150:151], 0\n"
    "v_mov_b64 v[152:153], 0\n"
    "v_mov_b64 v[154:155], 0\n"
    "v_mov_b64 v[156:157], 0\n"
    "v_mov_b64 v[158:159], 0\n"
    "v_mov_b64 v[160:161], 0\n"
    "v_mov_b64 v[162:163], 0\n"
    "v_mov_b64 v[164:165], 0\n"
    "v_mov_b64 v[166:167], 0\n"
    "v_mov_b64 v[168:169], 0\n"
    "v_mov_b64 v[170:171], 0\n"
    "v_mov_b64 v[172:173], 0\n"
    "v_mov_b64 v[174:175], 0\n"
    "v_mov_b64 v[176:177], 0\n"
    "v_mov_b64 v[178:179], 0\n"
    "v_mov_b64 v[180:181], 0\n"
    "v_mov_b64 v[182:183], 0\n"
    "v_mov_b64 v[184:185], 0\n"
    "v_mov_b64 v[186:187], 0\n"
    "v_mov_b64 v[188:189], 0\n"
    "v_mov_b64 v[190:191], 0\n"
    "v_mov_b64 v[192:193], 0\n"
    "v_mov_b64 v[194:195], 0\n"
    "v_mov_b64 v[196:197], 0\n"
    "v_mov_b64 v[198:199], 0\n"
    "v_mov_b64 v[200:201], 0\n"
    "v_mov_b64 v[202:203], 0\n"
    "v_mov_b64 v[204:205], 0\n"
    "v_mov_b64 v[206:207], 0\n"
    "v_mov_b64 v[208:209], 0\n"
    "v_mov_b64 v[210:211], 0\n"
    "v_mov_b64 v[212:213], 0\n"
    "v_mov_b64 v[214:215], 0\n"
    "v_mov_b64 v[216:217], 0\n"
    "v_mov_b64 v[218:219], 0\n"
    "v_mov_b64 v[220:221], 0\n"
    "v_mov_b64 v[222:223], 0\n"
    "v_mov_b64 v[224:225], 0\n"
    "v_mov_b64 v[226:227], 0\n"
    "v_mov_b64 v[228:229], 0\n"
    "v_mov_b64 v[230:231], 0\n"
    "v_mov_b64 v[232:233], 0\n"
    "v_mov_b64 v[234:235], 0\n"
    "v_mov_b64 v[236:237], 0\n"
    "v_mov_b64 v[238:239], 0\n"
    "v_mov_b64 v[240:241], 0\n"
    "v_mov_b64 v[242:243], 0\n"
    "v_mov_b64 v[244:245], 0\n"
    "v_mov_b64 v[246:247], 0\n"
    "v_mov_b64 v[248:249], 0\n"
    "v_mov_b64 v[250:251], 0\n"
    "v_mov_b64 v[252:253], 0\n"
    "v_mov_b64 v[254:255], 0\n"
    "v_mov_b64 v[64:65], 0\n"
    "v_mov_b64 v[66:67], 0\n"
    "v_mov_b64 v[68:69], 0\n"
    "v_mov_b64 v[70:71], 0\n"
    "v_mov_b64 v[72:73], 0\n"
    "v_mov_b64 v[74:75], 0\n"
    "v_mov_b64 v[76:77], 0\n"
    "v_mov_b64 v[78:79], 0\n"
    "v_mov_b32 v80, 0xff7fffff\n"
    "v_mov_b32 v84, 0xff7fffff\n"
    "v_mov_b32 v88, 0xff7fffff\n"
    "v_mov_b32 v92, 0xff7fffff\n"
    "s_mov_b32 s88, 0\n"
    "s_mov_b32 s89, 4096\n"
    ".Lk2_slow_loop:\n"
    "buffer_load_dwordx4 a[192:195], v48, s[60:63], s88 offen offset:0\n"
    "buffer_load_dwordx4 a[196:199], v48, s[60:63], s88 offen offset:64\n"
    "buffer_load_dwordx4 a[200:203], v48, s[60:63], s88 offen offset:128\n"
    "buffer_load_dwordx4 a[204:207], v48, s[60:63], s88 offen offset:192\n"
    "buffer_load_dwordx4 a[208:211], v48, s[60:63], s89 offen offset:0\n"
    "buffer_load_dwordx4 a[212:215], v48, s[60:63], s89 offen offset:64\n"
    "buffer_load_dwordx4 a[216:219], v48, s[60:63], s89 offen offset:128\n"
    "buffer_load_dwordx4 a[220:223], v48, s[60:63], s89 offen offset:192\n"
    "s_add_u32 m0, s87, 0\n"
    "s_nop 0\n"
    "buffer_load_dwordx4 v49, s[64:67], s88 offen offset:0 lds\n"
    "s_add_u32 m0, s87, 896\n"
    "s_nop 0\n"
    "buffer_load_dwordx4 v49, s[64:67], s88 offen offset:128 lds\n"
    "s_add_u32 m0, s87, 0\n"
    "s_nop 0\n"
    "buffer_load_dwordx4 v49, s[64:67], s88 offen offset:2048 lds\n"
    "s_add_u32 m0, s87, 896\n"
    "s_nop 0\n"
    "buffer_load_dwordx4 v49, s[64:67], s88 offen offset:2176 lds\n"
    "s_add_u32 m0, s87, 4096\n"
    "s_nop 0\n"
    "buffer_load_dwordx4 v49, s[64:67], s89 offen offset:0 lds\n"
    "s_add_u32 m0, s87, 4992\n"
    "s_nop 0\n"
    "buffer_load_dwordx4 v49, s[64:67], s89 offen offset:128 lds\n"
    "s_add_u32 m0, s87, 4096\n"
    "s_nop 0\n"
    "buffer_load_dwordx4 v49, s[64:67], s89 offen offset:2048 lds\n"
    "s_add_u32 m0, s87, 4992\n"
    "s_nop 0\n"
    "buffer_load_dwordx4 v49, s[64:67], s89 offen offset:2176 lds\n"
    "s_waitcnt vmcnt(0)\n"
    "ds_read_b64_tr_b16 a[64:65], v50 offset:0\n"
    "ds_read_b64_tr_b16 a[66:67], v50 offset:4096\n"
    "ds_read_b64_tr_b16 a[72:73], v51 offset:0\n"
    "ds_read_b64_tr_b16 a[74:75], v51 offset:4096\n"
    "ds_read_b64_tr_b16 a[80:81], v50 offset:512\n"
    "ds_read_b64_tr_b16 a[82:83], v50 offset:4608\n"
    "ds_read_b64_tr_b16 a[88:89], v51 offset:512\n"
    "ds_read_b64_tr_b16 a[90:91], v51 offset:4608\n"
    "ds_read_b64_tr_b16 a[96:97], v50 offset:1024\n"
    "ds_read_b64_tr_b16 a[98:99], v50 offset:5120\n"
    "ds_read_b64_tr_b16 a[104:105], v51 offset:1024\n"
    "ds_read_b64_tr_b16 a[106:107], v51 offset:5120\n"
    "ds_read_b64_tr_b16 a[112:113], v50 offset:1536\n"
    "ds_read_b64_tr_b16 a[114:115], v50 offset:5632\n"
    "ds_read_b64_tr_b16 a[120:121], v51 offset:1536\n"
    "ds_read_b64_tr_b16 a[122:123], v51 offset:5632\n"
    "v_mfma_f32_16x16x32_bf16 v[96:99], a[192:195], a[128:131], 0\n"
    "v_mfma_f32_16x16x32_bf16 v[96:99], a[196:199], a[132:135], v[96:99]\n"
    "v_mfma_f32_16x16x32_bf16 v[96:99], a[200:203], a[136:139], v[96:99]\n"
    "v_mfma_f32_16x16x32_bf16 v[96:99], a[204:207], a[140:143], v[96:99]\n"
    "v_mfma_f32_16x16x32_bf16 v[100:103], a[208:211], a[128:131], 0\n"
    "v_mfma_f32_16x16x32_bf16 v[100:103], a[212:215], a[132:135], v[100:103]\n"
    "v_mfma_f32_16x16x32_bf16 v[100:103], a[216:219], a[136:139], v[100:103]\n"
    "v_mfma_f32_16x16x32_bf16 v[100:103], a[220:223], a[140:143], v[100:103]\n"
    "s_nop 3\n"
    "v_max3_f32 v52, v96, v97, v80\n"
    "v_max3_f32 v52, v98, v99, v52\n"
    "s_nop 1\n"
    "v_max3_f32 v52, v100, v101, v52\n"
    "v_max3_f32 v52, v102, v103, v52\n"
    "v_mov_b32 v53, v52\n"
    "s_nop 1\n"
    "v_permlane32_swap_b32 v52, v53\n"
    "v_max_f32 v52, v52, v53\n"
    "v_mov_b32 v53, v52\n"
    "s_nop 1\n"
    "v_permlane16_swap_b32 v52, v53\n"
    "v_max_f32 v52, v52, v53\n"
    "v_sub_f32 v54, v80, v52\n"
    "v_exp_f32 v54, v54\n"
    "v_mov_b32 v80, v52\n"
    "v_sub_f32 v96, v96, v52\n"
    "v_sub_f32 v97, v97, v52\n"
    "v_sub_f32 v98, v98, v52\n"
    "v_sub_f32 v99, v99, v52\n"
    "v_sub_f32 v100, v100, v52\n"
    "v_sub_f32 v101, v101, v52\n"
    "v_sub_f32 v102, v102, v52\n"
    "v_sub_f32 v103, v103, v52\n"
    "v_exp_f32 v96, v96\n"
    "v_exp_f32 v97, v97\n"
    "v_exp_f32 v98, v98\n"
    "v_exp_f32 v99, v99\n"
    "v_exp_f32 v100, v100\n"
    "v_exp_f32 v101, v101\n"
    "v_exp_f32 v102, v102\n"
    "v_exp_f32 v103, v103\n"
    "v_cvt_pk_bf16_f32 v96, v96, v97\n"
    "v_cvt_pk_bf16_f32 v97, v98, v99\n"
    "v_cvt_pk_bf16_f32 v98, v100, v101\n"
    "v_cvt_pk_bf16_f32 v99, v102, v103\n"
    "v_pk_mul_f32 v[128:129], v[128:129], v[54:55] op_sel_hi:[1,0]\n"
    "v_pk_mul_f32 v[130:131], v[130:131], v[54:55] op_sel_hi:[1,0]\n"
    "v_pk_mul_f32 v[144:145], v[144:145], v[54:55] op_sel_hi:[1,0]\n"
    "v_pk_mul_f32 v[146:147], v[146:147], v[54:55] op_sel_hi:[1,0]\n"
    "v_pk_mul_f32 v[160:161], v[160:161], v[54:55] op_sel_hi:[1,0]\n"
    "v_pk_mul_f32 v[162:163], v[162:163], v[54:55] op_sel_hi:[1,0]\n"
    "v_pk_mul_f32 v[176:177], v[176:177], v[54:55] op_sel_hi:[1,0]\n"
    "v_pk_mul_f32 v[178:179], v[178:179], v[54:55] op_sel_hi:[1,0]\n"
    "v_pk_mul_f32 v[192:193], v[192:193], v[54:55] op_sel_hi:[1,0]\n"
    "v_pk_mul_f32 v[194:195], v[194:195], v[54:55] op_sel_hi:[1,0]\n"
    "v_pk_mul_f32 v[208:209], v[208:209], v[54:55] op_sel_hi:[1,0]\n"
    "v_pk_mul_f32 v[210:211], v[210:211], v[54:55] op_sel_hi:[1,0]\n"
    "v_pk_mul_f32 v[224:225], v[224:225], v[54:55] op_sel_hi:[1,0]\n"
    "v_pk_mul_f32 v[226:227], v[226:227], v[54:55] op_sel_hi:[1,0]\n"
    "v_pk_mul_f32 v[240:241], v[240:241], v[54:55] op_sel_hi:[1,0]\n"
    "v_pk_mul_f32 v[242:243], v[242:243], v[54:55] op_sel_hi:[1,0]\n"
    "v_pk_mul_f32 v[64:65], v[64:65], v[54:55] op_sel_hi:[1,0]\n"
    "v_pk_mul_f32 v[66:67], v[66:67], v[54:55] op_sel_hi:[1,0]\n"
    "s_waitcnt lgkmcnt(14)\n"
    "v_mfma_f32_16x16x32_bf16 v[128:131], a[64:67], v[96:99], v[128:131]\n"
    "s_waitcnt lgkmcnt(12)\n"
    "v_mfma_f32_16x16x32_bf16 v[144:147], a[72:75], v[96:99], v[144:147]\n"
    "s_waitcnt lgkmcnt(10)\n"
    "v_mfma_f32_16x16x32_bf16 v[160:163], a[80:83], v[96:99], v[160:163]\n"
    "s_waitcnt lgkmcnt(8)\n"
    "v_mfma_f32_16x16x32_bf16 v[176:179], a[88:91], v[96:99], v[176:179]\n"
    "s_waitcnt lgkmcnt(6)\n"
    "v_mfma_f32_16x16x32_bf16 v[192:195], a[96:99], v[96:99], v[192:195]\n"
    "s_waitcnt lgkmcnt(4)\n"
    "v_mfma_f32_16x16x32_bf16 v[208:211], a[104:107], v[96:99], v[208:211]\n"
    "s_waitcnt lgkmcnt(2)\n"
    "v_mfma_f32_16x16x32_bf16 v[224:227], a[112:115], v[96:99], v[224:227]\n"
    "s_waitcnt lgkmcnt(0)\n"
    "v_mfma_f32_16x16x32_bf16 v[240:243], a[120:123], v[96:99], v[240:243]\n"
    "v_mfma_f32_16x16x32_bf16 v[64:67], v[60:63], v[96:99], v[64:67]\n"
    "v_mfma_f32_16x16x32_bf16 v[96:99], a[192:195], a[144:147], 0\n"
    "v_mfma_f32_16x16x32_bf16 v[96:99], a[196:199], a[148:151], v[96:99]\n"
    "v_mfma_f32_16x16x32_bf16 v[96:99], a[200:203], a[152:155], v[96:99]\n"
    "v_mfma_f32_16x16x32_bf16 v[96:99], a[204:207], a[156:159], v[96:99]\n"
    "v_mfma_f32_16x16x32_bf16 v[100:103], a[208:211], a[144:147], 0\n"
    "v_mfma_f32_16x16x32_bf16 v[100:103], a[212:215], a[148:151], v[100:103]\n"
    "v_mfma_f32_16x16x32_bf16 v[100:103], a[216:219], a[152:155], v[100:103]\n"
    "v_mfma_f32_16x16x32_bf16 v[100:103], a[220:223], a[156:159], v[100:103]\n"
    "s_nop 3\n"
    "v_max3_f32 v52, v96, v97, v84\n"
    "v_max3_f32 v52, v98, v99, v52\n"
    "s_nop 1\n"
    "v_max3_f32 v52, v100, v101, v52\n"
    "v_max3_f32 v52, v102, v103, v52\n"
    "v_mov_b32 v53, v52\n"
    "s_nop 1\n"
    "v_permlane32_swap_b32 v52, v53\n"
    "v_max_f32 v52, v52, v53\n"
    "v_mov_b32 v53, v52\n"
    "s_nop 1\n"
    "v_permlane16_swap_b32 v52, v53\n"
    "v_max_f32 v52, v52, v53\n"
    "v_sub_f32 v54, v84, v52\n"
    "v_exp_f32 v54, v54\n"
    "v_mov_b32 v84, v52\n"
    "v_sub_f32 v96, v96, v52\n"
    "v_sub_f32 v97, v97, v52\n"
    "v_sub_f32 v98, v98, v52\n"
    "v_sub_f32 v99, v99, v52\n"
    "v_sub_f32 v100, v100, v52\n"
    "v_sub_f32 v101, v101, v52\n"
    "v_sub_f32 v102, v102, v52\n"
    "v_sub_f32 v103, v103, v52\n"
    "v_exp_f32 v96, v96\n"
    "v_exp_f32 v97, v97\n"
    "v_exp_f32 v98, v98\n"
    "v_exp_f32 v99, v99\n"
    "v_exp_f32 v100, v100\n"
    "v_exp_f32 v101, v101\n"
    "v_exp_f32 v102, v102\n"
    "v_exp_f32 v103, v103\n"
    "v_cvt_pk_bf16_f32 v96, v96, v97\n"
    "v_cvt_pk_bf16_f32 v97, v98, v99\n"
    "v_cvt_pk_bf16_f32 v98, v100, v101\n"
    "v_cvt_pk_bf16_f32 v99, v102, v103\n"
    "v_pk_mul_f32 v[132:133], v[132:133], v[54:55] op_sel_hi:[1,0]\n"
    "v_pk_mul_f32 v[134:135], v[134:135], v[54:55] op_sel_hi:[1,0]\n"
    "v_pk_mul_f32 v[148:149], v[148:149], v[54:55] op_sel_hi:[1,0]\n"
    "v_pk_mul_f32 v[150:151], v[150:151], v[54:55] op_sel_hi:[1,0]\n"
    "v_pk_mul_f32 v[164:165], v[164:165], v[54:55] op_sel_hi:[1,0]\n"
    "v_pk_mul_f32 v[166:167], v[166:167], v[54:55] op_sel_hi:[1,0]\n"
    "v_pk_mul_f32 v[180:181], v[180:181], v[54:55] op_sel_hi:[1,0]\n"
    "v_pk_mul_f32 v[182:183], v[182:183], v[54:55] op_sel_hi:[1,0]\n"
    "v_pk_mul_f32 v[196:197], v[196:197], v[54:55] op_sel_hi:[1,0]\n"
    "v_pk_mul_f32 v[198:199], v[198:199], v[54:55] op_sel_hi:[1,0]\n"
    "v_pk_mul_f32 v[212:213], v[212:213], v[54:55] op_sel_hi:[1,0]\n"
    "v_pk_mul_f32 v[214:215], v[214:215], v[54:55] op_sel_hi:[1,0]\n"
    "v_pk_mul_f32 v[228:229], v[228:229], v[54:55] op_sel_hi:[1,0]\n"
    "v_pk_mul_f32 v[230:231], v[230:231], v[54:55] op_sel_hi:[1,0]\n"
    "v_pk_mul_f32 v[244:245], v[244:245], v[54:55] op_sel_hi:[1,0]\n"
    "v_pk_mul_f32 v[246:247], v[246:247], v[54:55] op_sel_hi:[1,0]\n"
    "v_pk_mul_f32 v[68:69], v[68:69], v[54:55] op_sel_hi:[1,0]\n"
    "v_pk_mul_f32 v[70:71], v[70:71], v[54:55] op_sel_hi:[1,0]\n"
    "v_mfma_f32_16x16x32_bf16 v[132:135], a[64:67], v[96:99], v[132:135]\n"
    "v_mfma_f32_16x16x32_bf16 v[148:151], a[72:75], v[96:99], v[148:151]\n"
    "v_mfma_f32_16x16x32_bf16 v[164:167], a[80:83], v[96:99], v[164:167]\n"
    "v_mfma_f32_16x16x32_bf16 v[180:183], a[88:91], v[96:99], v[180:183]\n"
    "v_mfma_f32_16x16x32_bf16 v[196:199], a[96:99], v[96:99], v[196:199]\n"
    "v_mfma_f32_16x16x32_bf16 v[212:215], a[104:107], v[96:99], v[212:215]\n"
    "v_mfma_f32_16x16x32_bf16 v[228:231], a[112:115], v[96:99], v[228:231]\n"
    "v_mfma_f32_16x16x32_bf16 v[244:247], a[120:123], v[96:99], v[244:247]\n"
    "v_mfma_f32_16x16x32_bf16 v[68:71], v[60:63], v[96:99], v[68:71]\n"
    "v_mfma_f32_16x16x32_bf16 v[96:99], a[192:195], a[160:163], 0\n"
    "v_mfma_f32_16x16x32_bf16 v[96:99], a[196:199], a[164:167], v[96:99]\n"
    "v_mfma_f32_16x16x32_bf16 v[96:99], a[200:203], a[168:171], v[96:99]\n"
    "v_mfma_f32_16x16x32_bf16 v[96:99], a[204:207], a[172:175], v[96:99]\n"
    "v_mfma_f32_16x16x32_bf16 v[100:103], a[208:211], a[160:163], 0\n"
    "v_mfma_f32_16x16x32_bf16 v[100:103], a[212:215], a[164:167], v[100:103]\n"
    "v_mfma_f32_16x16x32_bf16 v[100:103], a[216:219], a[168:171], v[100:103]\n"
    "v_mfma_f32_16x16x32_bf16 v[100:103], a[220:223], a[172:175], v[100:103]\n"
    "s_nop 3\n"
    "v_max3_f32 v52, v96, v97, v88\n"
    "v_max3_f32 v52, v98, v99, v52\n"
    "s_nop 1\n"
    "v_max3_f32 v52, v100, v101, v52\n"
    "v_max3_f32 v52, v102, v103, v52\n"
    "v_mov_b32 v53, v52\n"
    "s_nop 1\n"
    "v_permlane32_swap_b32 v52, v53\n"
    "v_max_f32 v52, v52, v53\n"
    "v_mov_b32 v53, v52\n"
    "s_nop 1\n"
    "v_permlane16_swap_b32 v52, v53\n"
    "v_max_f32 v52, v52, v53\n"
    "v_sub_f32 v54, v88, v52\n"
    "v_exp_f32 v54, v54\n"
    "v_mov_b32 v88, v52\n"
    "v_sub_f32 v96, v96, v52\n"
    "v_sub_f32 v97, v97, v52\n"
    "v_sub_f32 v98, v98, v52\n"
    "v_sub_f32 v99, v99, v52\n"
    "v_sub_f32 v100, v100, v52\n"
    "v_sub_f32 v101, v101, v52\n"
    "v_sub_f32 v102, v102, v52\n"
    "v_sub_f32 v103, v103, v52\n"
    "v_exp_f32 v96, v96\n"
    "v_exp_f32 v97, v97\n"
    "v_exp_f32 v98, v98\n"
    "v_exp_f32 v99, v99\n"
    "v_exp_f32 v100, v100\n"
    "v_exp_f32 v101, v101\n"
    "v_exp_f32 v102, v102\n"
    "v_exp_f32 v103, v103\n"
    "v_cvt_pk_bf16_f32 v96, v96, v97\n"
    "v_cvt_pk_bf16_f32 v97, v98, v99\n"
    "v_cvt_pk_bf16_f32 v98, v100, v101\n"
    "v_cvt_pk_bf16_f32 v99, v102, v103\n"
    "v_pk_mul_f32 v[136:137], v[136:137], v[54:55] op_sel_hi:[1,0]\n"
    "v_pk_mul_f32 v[138:139], v[138:139], v[54:55] op_sel_hi:[1,0]\n"
    "v_pk_mul_f32 v[152:153], v[152:153], v[54:55] op_sel_hi:[1,0]\n"
    "v_pk_mul_f32 v[154:155], v[154:155], v[54:55] op_sel_hi:[1,0]\n"
    "v_pk_mul_f32 v[168:169], v[168:169], v[54:55] op_sel_hi:[1,0]\n"
    "v_pk_mul_f32 v[170:171], v[170:171], v[54:55] op_sel_hi:[1,0]\n"
    "v_pk_mul_f32 v[184:185], v[184:185], v[54:55] op_sel_hi:[1,0]\n"
    "v_pk_mul_f32 v[186:187], v[186:187], v[54:55] op_sel_hi:[1,0]\n"
    "v_pk_mul_f32 v[200:201], v[200:201], v[54:55] op_sel_hi:[1,0]\n"
    "v_pk_mul_f32 v[202:203], v[202:203], v[54:55] op_sel_hi:[1,0]\n"
    "v_pk_mul_f32 v[216:217], v[216:217], v[54:55] op_sel_hi:[1,0]\n"
    "v_pk_mul_f32 v[218:219], v[218:219], v[54:55] op_sel_hi:[1,0]\n"
    "v_pk_mul_f32 v[232:233], v[232:233], v[54:55] op_sel_hi:[1,0]\n"
    "v_pk_mul_f32 v[234:235], v[234:235], v[54:55] op_sel_hi:[1,0]\n"
    "v_pk_mul_f32 v[248:249], v[248:249], v[54:55] op_sel_hi:[1,0]\n"
    "v_pk_mul_f32 v[250:251], v[250:251], v[54:55] op_sel_hi:[1,0]\n"
    "v_pk_mul_f32 v[72:73], v[72:73], v[54:55] op_sel_hi:[1,0]\n"
    "v_pk_mul_f32 v[74:75], v[74:75], v[54:55] op_sel_hi:[1,0]\n"
    "v_mfma_f32_16x16x32_bf16 v[136:139], a[64:67], v[96:99], v[136:139]\n"
    "v_mfma_f32_16x16x32_bf16 v[152:155], a[72:75], v[96:99], v[152:155]\n"
    "v_mfma_f32_16x16x32_bf16 v[168:171], a[80:83], v[96:99], v[168:171]\n"
    "v_mfma_f32_16x16x32_bf16 v[184:187], a[88:91], v[96:99], v[184:187]\n"
    "v_mfma_f32_16x16x32_bf16 v[200:203], a[96:99], v[96:99], v[200:203]\n"
    "v_mfma_f32_16x16x32_bf16 v[216:219], a[104:107], v[96:99], v[216:219]\n"
    "v_mfma_f32_16x16x32_bf16 v[232:235], a[112:115], v[96:99], v[232:235]\n"
    "v_mfma_f32_16x16x32_bf16 v[248:251], a[120:123], v[96:99], v[248:251]\n"
    "v_mfma_f32_16x16x32_bf16 v[72:75], v[60:63], v[96:99], v[72:75]\n"
    "v_mfma_f32_16x16x32_bf16 v[96:99], a[192:195], a[176:179], 0\n"
    "v_mfma_f32_16x16x32_bf16 v[96:99], a[196:199], a[180:183], v[96:99]\n"
    "v_mfma_f32_16x16x32_bf16 v[96:99], a[200:203], a[184:187], v[96:99]\n"
    "v_mfma_f32_16x16x32_bf16 v[96:99], a[204:207], a[188:191], v[96:99]\n"
    "v_mfma_f32_16x16x32_bf16 v[100:103], a[208:211], a[176:179], 0\n"
    "v_mfma_f32_16x16x32_bf16 v[100:103], a[212:215], a[180:183], v[100:103]\n"
    "v_mfma_f32_16x16x32_bf16 v[100:103], a[216:219], a[184:187], v[100:103]\n"
    "v_mfma_f32_16x16x32_bf16 v[100:103], a[220:223], a[188:191], v[100:103]\n"
    "s_nop 3\n"
    "v_max3_f32 v52, v96, v97, v92\n"
    "v_max3_f32 v52, v98, v99, v52\n"
    "s_nop 1\n"
    "v_max3_f32 v52, v100, v101, v52\n"
    "v_max3_f32 v52, v102, v103, v52\n"
    "v_mov_b32 v53, v52\n"
    "s_nop 1\n"
    "v_permlane32_swap_b32 v52, v53\n"
    "v_max_f32 v52, v52, v53\n"
    "v_mov_b32 v53, v52\n"
    "s_nop 1\n"
    "v_permlane16_swap_b32 v52, v53\n"
    "v_max_f32 v52, v52, v53\n"
    "v_sub_f32 v54, v92, v52\n"
    "v_exp_f32 v54, v54\n"
    "v_mov_b32 v92, v52\n"
    "v_sub_f32 v96, v96, v52\n"
    "v_sub_f32 v97, v97, v52\n"
    "v_sub_f32 v98, v98, v52\n"
    "v_sub_f32 v99, v99, v52\n"
    "v_sub_f32 v100, v100, v52\n"
    "v_sub_f32 v101, v101, v52\n"
    "v_sub_f32 v102, v102, v52\n"
    "v_sub_f32 v103, v103, v52\n"
    "v_exp_f32 v96, v96\n"
    "v_exp_f32 v97, v97\n"
    "v_exp_f32 v98, v98\n"
    "v_exp_f32 v99, v99\n"
    "v_exp_f32 v100, v100\n"
    "v_exp_f32 v101, v101\n"
    "v_exp_f32 v102, v102\n"
    "v_exp_f32 v103, v103\n"
    "v_cvt_pk_bf16_f32 v96, v96, v97\n"
    "v_cvt_pk_bf16_f32 v97, v98, v99\n"
    "v_cvt_pk_bf16_f32 v98, v100, v101\n"
    "v_cvt_pk_bf16_f32 v99, v102, v103\n"
    "v_pk_mul_f32 v[140:141], v[140:141], v[54:55] op_sel_hi:[1,0]\n"
    "v_pk_mul_f32 v[142:143], v[142:143], v[54:55] op_sel_hi:[1,0]\n"
    "v_pk_mul_f32 v[156:157], v[156:157], v[54:55] op_sel_hi:[1,0]\n"
    "v_pk_mul_f32 v[158:159], v[158:159], v[54:55] op_sel_hi:[1,0]\n"
    "v_pk_mul_f32 v[172:173], v[172:173], v[54:55] op_sel_hi:[1,0]\n"
    "v_pk_mul_f32 v[174:175], v[174:175], v[54:55] op_sel_hi:[1,0]\n"
    "v_pk_mul_f32 v[188:189], v[188:189], v[54:55] op_sel_hi:[1,0]\n"
    "v_pk_mul_f32 v[190:191], v[190:191], v[54:55] op_sel_hi:[1,0]\n"
    "v_pk_mul_f32 v[204:205], v[204:205], v[54:55] op_sel_hi:[1,0]\n"
    "v_pk_mul_f32 v[206:207], v[206:207], v[54:55] op_sel_hi:[1,0]\n"
    "v_pk_mul_f32 v[220:221], v[220:221], v[54:55] op_sel_hi:[1,0]\n"
    "v_pk_mul_f32 v[222:223], v[222:223], v[54:55] op_sel_hi:[1,0]\n"
    "v_pk_mul_f32 v[236:237], v[236:237], v[54:55] op_sel_hi:[1,0]\n"
    "v_pk_mul_f32 v[238:239], v[238:239], v[54:55] op_sel_hi:[1,0]\n"
    "v_pk_mul_f32 v[252:253], v[252:253], v[54:55] op_sel_hi:[1,0]\n"
    "v_pk_mul_f32 v[254:255], v[254:255], v[54:55] op_sel_hi:[1,0]\n"
    "v_pk_mul_f32 v[76:77], v[76:77], v[54:55] op_sel_hi:[1,0]\n"
    "v_pk_mul_f32 v[78:79], v[78:79], v[54:55] op_sel_hi:[1,0]\n"
    "v_mfma_f32_16x16x32_bf16 v[140:143], a[64:67], v[96:99], v[140:143]\n"
    "v_mfma_f32_16x16x32_bf16 v[156:159], a[72:75], v[96:99], v[156:159]\n"
    "v_mfma_f32_16x16x32_bf16 v[172:175], a[80:83], v[96:99], v[172:175]\n"
    "v_mfma_f32_16x16x32_bf16 v[188:191], a[88:91], v[96:99], v[188:191]\n"
    "v_mfma_f32_16x16x32_bf16 v[204:207], a[96:99], v[96:99], v[204:207]\n"
    "v_mfma_f32_16x16x32_bf16 v[220:223], a[104:107], v[96:99], v[220:223]\n"
    "v_mfma_f32_16x16x32_bf16 v[236:239], a[112:115], v[96:99], v[236:239]\n"
    "v_mfma_f32_16x16x32_bf16 v[252:255], a[120:123], v[96:99], v[252:255]\n"
    "v_mfma_f32_16x16x32_bf16 v[76:79], v[60:63], v[96:99], v[76:79]\n"
    "s_waitcnt lgkmcnt(0)\n"
    "s_add_u32 s88, s88, 8192\n"
    "s_add_u32 s89, s89, 8192\n"
    "s_nop 7\n"
    "s_nop 7\n"
    "s_cmp_lt_u32 s88, 0x80000\n"
    "s_cbranch_scc1 .Lk2_slow_loop\n"
    "s_lshl_b32 s76, s41, 13\n"
    "s_add_u32 s77, s76, 0x40000\n"
    "s_add_u32 s86, s76, 4096\n"
    "s_add_u32 s87, s77, 4096\n"
    "s_mov_b32 s89, 0x5f800000\n"
    "v_rcp_f32 v80, v64\n"
    "s_nop 0\n"
    "v_pk_mul_f32 v[128:129], v[128:129], v[80:81] op_sel_hi:[1,0]\n"
    "v_pk_mul_f32 v[130:131], v[130:131], v[80:81] op_sel_hi:[1,0]\n"
    "v_pk_mul_f32 v[144:145], v[144:145], v[80:81] op_sel_hi:[1,0]\n"
    "v_pk_mul_f32 v[146:147], v[146:147], v[80:81] op_sel_hi:[1,0]\n"
    "v_cvt_pk_bf16_f32 v32, v128, v129\n"
    "v_cvt_pk_bf16_f32 v33, v130, v131\n"
    "v_cvt_pk_bf16_f32 v34, v144, v145\n"
    "v_cvt_pk_bf16_f32 v35, v146, v147\n"
    "buffer_store_dwordx4 v[32:35], v2, s[68:71], s76 offen offset:0 sc1\n"
    "v_pk_mul_f32 v[160:161], v[160:161], v[80:81] op_sel_hi:[1,0]\n"
    "v_pk_mul_f32 v[162:163], v[162:163], v[80:81] op_sel_hi:[1,0]\n"
    "v_pk_mul_f32 v[176:177], v[176:177], v[80:81] op_sel_hi:[1,0]\n"
    "v_pk_mul_f32 v[178:179], v[178:179], v[80:81] op_sel_hi:[1,0]\n"
    "v_cvt_pk_bf16_f32 v36, v160, v161\n"
    "v_cvt_pk_bf16_f32 v37, v162, v163\n"
    "v_cvt_pk_bf16_f32 v38, v176, v177\n"
    "v_cvt_pk_bf16_f32 v39, v178, v179\n"
    "buffer_store_dwordx4 v[36:39], v2, s[68:71], s76 offen offset:2048 sc1\n"
    "v_pk_mul_f32 v[192:193], v[192:193], v[80:81] op_sel_hi:[1,0]\n"
    "v_pk_mul_f32 v[194:195], v[194:195], v[80:81] op_sel_hi:[1,0]\n"
    "v_pk_mul_f32 v[208:209], v[208:209], v[80:81] op_sel_hi:[1,0]\n"
    "v_pk_mul_f32 v[210:211], v[210:211], v[80:81] op_sel_hi:[1,0]\n"
    "v_cvt_pk_bf16_f32 v32, v192, v193\n"
    "v_cvt_pk_bf16_f32 v33, v194, v195\n"
    "v_cvt_pk_bf16_f32 v34, v208, v209\n"
    "v_cvt_pk_bf16_f32 v35, v210, v211\n"
    "buffer_store_dwordx4 v[32:35], v2, s[68:71], s86 offen offset:0 sc1\n"
    "v_pk_mul_f32 v[224:225], v[224:225], v[80:81] op_sel_hi:[1,0]\n"
    "v_pk_mul_f32 v[226:227], v[226:227], v[80:81] op_sel_hi:[1,0]\n"
    "v_pk_mul_f32 v[240:241], v[240:241], v[80:81] op_sel_hi:[1,0]\n"
    "v_pk_mul_f32 v[242:243], v[242:243], v[80:81] op_sel_hi:[1,0]\n"
    "v_cvt_pk_bf16_f32 v36, v224, v225\n"
    "v_cvt_pk_bf16_f32 v37, v226, v227\n"
    "v_cvt_pk_bf16_f32 v38, v240, v241\n"
    "v_cvt_pk_bf16_f32 v39, v242, v243\n"
    "buffer_store_dwordx4 v[36:39], v2, s[68:71], s86 offen offset:2048 sc1\n"
    "v_rcp_f32 v84, v68\n"
    "s_nop 0\n"
    "v_pk_mul_f32 v[132:133], v[132:133], v[84:85] op_sel_hi:[1,0]\n"
    "v_pk_mul_f32 v[134:135], v[134:135], v[84:85] op_sel_hi:[1,0]\n"
    "v_pk_mul_f32 v[148:149], v[148:149], v[84:85] op_sel_hi:[1,0]\n"
    "v_pk_mul_f32 v[150:151], v[150:151], v[84:85] op_sel_hi:[1,0]\n"
    "v_cvt_pk_bf16_f32 v40, v132, v133\n"
    "v_cvt_pk_bf16_f32 v41, v134, v135\n"
    "v_cvt_pk_bf16_f32 v42, v148, v149\n"
    "v_cvt_pk_bf16_f32 v43, v150, v151\n"
    "buffer_store_dwordx4 v[40:43], v2, s[68:71], s76 offen offset:1024 sc1\n"
    "v_pk_mul_f32 v[164:165], v[164:165], v[84:85] op_sel_hi:[1,0]\n"
    "v_pk_mul_f32 v[166:167], v[166:167], v[84:85] op_sel_hi:[1,0]\n"
    "v_pk_mul_f32 v[180:181], v[180:181], v[84:85] op_sel_hi:[1,0]\n"
    "v_pk_mul_f32 v[182:183], v[182:183], v[84:85] op_sel_hi:[1,0]\n"
    "v_cvt_pk_bf16_f32 v44, v164, v165\n"
    "v_cvt_pk_bf16_f32 v45, v166, v167\n"
    "v_cvt_pk_bf16_f32 v46, v180, v181\n"
    "v_cvt_pk_bf16_f32 v47, v182, v183\n"
    "buffer_store_dwordx4 v[44:47], v2, s[68:71], s76 offen offset:3072 sc1\n"
    "v_pk_mul_f32 v[196:197], v[196:197], v[84:85] op_sel_hi:[1,0]\n"
    "v_pk_mul_f32 v[198:199], v[198:199], v[84:85] op_sel_hi:[1,0]\n"
    "v_pk_mul_f32 v[212:213], v[212:213], v[84:85] op_sel_hi:[1,0]\n"
    "v_pk_mul_f32 v[214:215], v[214:215], v[84:85] op_sel_hi:[1,0]\n"
    "v_cvt_pk_bf16_f32 v40, v196, v197\n"
    "v_cvt_pk_bf16_f32 v41, v198, v199\n"
    "v_cvt_pk_bf16_f32 v42, v212, v213\n"
    "v_cvt_pk_bf16_f32 v43, v214, v215\n"
    "buffer_store_dwordx4 v[40:43], v2, s[68:71], s86 offen offset:1024 sc1\n"
    "v_pk_mul_f32 v[228:229], v[228:229], v[84:85] op_sel_hi:[1,0]\n"
    "v_pk_mul_f32 v[230:231], v[230:231], v[84:85] op_sel_hi:[1,0]\n"
    "v_pk_mul_f32 v[244:245], v[244:245], v[84:85] op_sel_hi:[1,0]\n"
    "v_pk_mul_f32 v[246:247], v[246:247], v[84:85] op_sel_hi:[1,0]\n"
    "v_cvt_pk_bf16_f32 v44, v228, v229\n"
    "v_cvt_pk_bf16_f32 v45, v230, v231\n"
    "v_cvt_pk_bf16_f32 v46, v244, v245\n"
    "v_cvt_pk_bf16_f32 v47, v246, v247\n"
    "buffer_store_dwordx4 v[44:47], v2, s[68:71], s86 offen offset:3072 sc1\n"
    "v_rcp_f32 v88, v72\n"
    "s_nop 0\n"
    "v_pk_mul_f32 v[136:137], v[136:137], v[88:89] op_sel_hi:[1,0]\n"
    "v_pk_mul_f32 v[138:139], v[138:139], v[88:89] op_sel_hi:[1,0]\n"
    "v_pk_mul_f32 v[152:153], v[152:153], v[88:89] op_sel_hi:[1,0]\n"
    "v_pk_mul_f32 v[154:155], v[154:155], v[88:89] op_sel_hi:[1,0]\n"
    "v_cvt_pk_bf16_f32 v32, v136, v137\n"
    "v_cvt_pk_bf16_f32 v33, v138, v139\n"
    "v_cvt_pk_bf16_f32 v34, v152, v153\n"
    "v_cvt_pk_bf16_f32 v35, v154, v155\n"
    "buffer_store_dwordx4 v[32:35], v2, s[68:71], s77 offen offset:0 sc1\n"
    "v_pk_mul_f32 v[168:169], v[168:169], v[88:89] op_sel_hi:[1,0]\n"
    "v_pk_mul_f32 v[170:171], v[170:171], v[88:89] op_sel_hi:[1,0]\n"
    "v_pk_mul_f32 v[184:185], v[184:185], v[88:89] op_sel_hi:[1,0]\n"
    "v_pk_mul_f32 v[186:187], v[186:187], v[88:89] op_sel_hi:[1,0]\n"
    "v_cvt_pk_bf16_f32 v36, v168, v169\n"
    "v_cvt_pk_bf16_f32 v37, v170, v171\n"
    "v_cvt_pk_bf16_f32 v38, v184, v185\n"
    "v_cvt_pk_bf16_f32 v39, v186, v187\n"
    "buffer_store_dwordx4 v[36:39], v2, s[68:71], s77 offen offset:2048 sc1\n"
    "v_pk_mul_f32 v[200:201], v[200:201], v[88:89] op_sel_hi:[1,0]\n"
    "v_pk_mul_f32 v[202:203], v[202:203], v[88:89] op_sel_hi:[1,0]\n"
    "v_pk_mul_f32 v[216:217], v[216:217], v[88:89] op_sel_hi:[1,0]\n"
    "v_pk_mul_f32 v[218:219], v[218:219], v[88:89] op_sel_hi:[1,0]\n"
    "v_cvt_pk_bf16_f32 v32, v200, v201\n"
    "v_cvt_pk_bf16_f32 v33, v202, v203\n"
    "v_cvt_pk_bf16_f32 v34, v216, v217\n"
    "v_cvt_pk_bf16_f32 v35, v218, v219\n"
    "buffer_store_dwordx4 v[32:35], v2, s[68:71], s87 offen offset:0 sc1\n"
    "v_pk_mul_f32 v[232:233], v[232:233], v[88:89] op_sel_hi:[1,0]\n"
    "v_pk_mul_f32 v[234:235], v[234:235], v[88:89] op_sel_hi:[1,0]\n"
    "v_pk_mul_f32 v[248:249], v[248:249], v[88:89] op_sel_hi:[1,0]\n"
    "v_pk_mul_f32 v[250:251], v[250:251], v[88:89] op_sel_hi:[1,0]\n"
    "v_cvt_pk_bf16_f32 v36, v232, v233\n"
    "v_cvt_pk_bf16_f32 v37, v234, v235\n"
    "v_cvt_pk_bf16_f32 v38, v248, v249\n"
    "v_cvt_pk_bf16_f32 v39, v250, v251\n"
    "buffer_store_dwordx4 v[36:39], v2, s[68:71], s87 offen offset:2048 sc1\n"
    "v_rcp_f32 v92, v76\n"
    "s_nop 0\n"
    "v_pk_mul_f32 v[140:141], v[140:141], v[92:93] op_sel_hi:[1,0]\n"
    "v_pk_mul_f32 v[142:143], v[142:143], v[92:93] op_sel_hi:[1,0]\n"
    "v_pk_mul_f32 v[156:157], v[156:157], v[92:93] op_sel_hi:[1,0]\n"
    "v_pk_mul_f32 v[158:159], v[158:159], v[92:93] op_sel_hi:[1,0]\n"
    "v_cvt_pk_bf16_f32 v40, v140, v141\n"
    "v_cvt_pk_bf16_f32 v41, v142, v143\n"
    "v_cvt_pk_bf16_f32 v42, v156, v157\n"
    "v_cvt_pk_bf16_f32 v43, v158, v159\n"
    "buffer_store_dwordx4 v[40:43], v2, s[68:71], s77 offen offset:1024 sc1\n"
    "v_pk_mul_f32 v[172:173], v[172:173], v[92:93] op_sel_hi:[1,0]\n"
    "v_pk_mul_f32 v[174:175], v[174:175], v[92:93] op_sel_hi:[1,0]\n"
    "v_pk_mul_f32 v[188:189], v[188:189], v[92:93] op_sel_hi:[1,0]\n"
    "v_pk_mul_f32 v[190:191], v[190:191], v[92:93] op_sel_hi:[1,0]\n"
    "v_cvt_pk_bf16_f32 v44, v172, v173\n"
    "v_cvt_pk_bf16_f32 v45, v174, v175\n"
    "v_cvt_pk_bf16_f32 v46, v188, v189\n"
    "v_cvt_pk_bf16_f32 v47, v190, v191\n"
    "buffer_store_dwordx4 v[44:47], v2, s[68:71], s77 offen offset:3072 sc1\n"
    "v_pk_mul_f32 v[204:205], v[204:205], v[92:93] op_sel_hi:[1,0]\n"
    "v_pk_mul_f32 v[206:207], v[206:207], v[92:93] op_sel_hi:[1,0]\n"
    "v_pk_mul_f32 v[220:221], v[220:221], v[92:93] op_sel_hi:[1,0]\n"
    "v_pk_mul_f32 v[222:223], v[222:223], v[92:93] op_sel_hi:[1,0]\n"
    "v_cvt_pk_bf16_f32 v40, v204, v205\n"
    "v_cvt_pk_bf16_f32 v41, v206, v207\n"
    "v_cvt_pk_bf16_f32 v42, v220, v221\n"
    "v_cvt_pk_bf16_f32 v43, v222, v223\n"
    "buffer_store_dwordx4 v[40:43], v2, s[68:71], s87 offen offset:1024 sc1\n"
    "v_pk_mul_f32 v[236:237], v[236:237], v[92:93] op_sel_hi:[1,0]\n"
    "v_pk_mul_f32 v[238:239], v[238:239], v[92:93] op_sel_hi:[1,0]\n"
    "v_pk_mul_f32 v[252:253], v[252:253], v[92:93] op_sel_hi:[1,0]\n"
    "v_pk_mul_f32 v[254:255], v[254:255], v[92:93] op_sel_hi:[1,0]\n"
    "v_cvt_pk_bf16_f32 v44, v236, v237\n"
    "v_cvt_pk_bf16_f32 v45, v238, v239\n"
    "v_cvt_pk_bf16_f32 v46, v252, v253\n"
    "v_cvt_pk_bf16_f32 v47, v254, v255\n"
    "buffer_store_dwordx4 v[44:47], v2, s[68:71], s87 offen offset:3072 sc1\n"
    "buffer_load_dwordx4 a[192:195], v2, s[44:47], s90 offen offset:0 sc1\n"
    "buffer_load_dwordx4 a[196:199], v2, s[44:47], s90 offen offset:1024 sc1\n"
    "buffer_load_dwordx4 a[208:211], v2, s[44:47], s90 offen offset:2048 sc1\n"
    "buffer_load_dwordx4 a[212:215], v2, s[44:47], s90 offen offset:3072 sc1\n"
    "buffer_load_dwordx4 a[224:227], v2, s[44:47], s91 offen offset:0 sc1\n"
    "buffer_load_dwordx4 a[228:231], v2, s[44:47], s91 offen offset:1024 sc1\n"
    "buffer_load_dwordx4 a[240:243], v2, s[44:47], s91 offen offset:2048 sc1\n"
    "buffer_load_dwordx4 a[244:247], v2, s[44:47], s91 offen offset:3072 sc1\n"
    "buffer_load_dwordx4 a[200:203], v2, s[44:47], s92 offen offset:0 sc1\n"
    "buffer_load_dwordx4 a[204:207], v2, s[44:47], s92 offen offset:1024 sc1\n"
    "buffer_load_dwordx4 a[216:219], v2, s[44:47], s92 offen offset:2048 sc1\n"
    "buffer_load_dwordx4 a[220:223], v2, s[44:47], s92 offen offset:3072 sc1\n"
    "s_mov_b32 s67, 0x10000\n"
    "v_mov_b32 v64, 0\n"
    "v_mov_b32 v65, 0\n"
    "v_mov_b32 v66, 0\n"
    "v_mov_b32 v67, 0\n"
    "s_waitcnt vmcnt(0)\n"
    "s_branch .Lk2_odone\n"
    ".Lk2_code_end:\n"
    : "+v"(tid_)
    : "s"(ws), "s"(wq), "s"(wg), "s"(wu), "s"(kc), "s"(vc), "s"(wo), "s"(wd), "s"(x), "s"(out), "s"(bid_), "s"(ldsb_)
    : "memory", "vcc", "scc", "v1", "v2", "v3", "v4", "v5", "v6", "v7", "v8", "v9", "v10", "v11", "v12", "v13", "v14", "v15", "v16", "v17", "v18", "v19", "v20", "v21", "v22", "v23", "v24", "v25", "v26", "v27", "v28", "v29", "v30", "v31", "v32", "v33", "v34", "v35", "v36", "v37", "v38", "v39", "v40", "v41", "v42", "v43", "v44", "v45", "v46", "v47", "v48", "v49", "v50", "v51", "v52", "v53", "v54", "v55", "v56", "v57", "v58", "v59", "v60", "v61", "v62", "v63", "v64", "v65", "v66", "v67", "v68", "v69", "v70", "v71", "v72", "v73", "v74", "v75", "v76", "v77", "v78", "v79", "v80", "v81", "v82", "v83", "v84", "v85", "v86", "v87", "v88", "v89", "v90", "v91", "v92", "v93", "v94", "v95", "v96", "v97", "v98", "v99", "v100", "v101", "v102", "v103", "v104", "v105", "v106", "v107", "v108", "v109", "v110", "v111", "v112", "v113", "v114", "v115", "v116", "v117", "v118", "v119", "v120", "v121", "v122", "v123", "v124", "v125", "v126", "v127", "v128", "v129", "v130", "v131", "v132", "v133", "v134", "v135", "v136", "v137", "v138", "v139", "v140", "v141", "v142", "v143", "v144", "v145", "v146", "v147", "v148", "v149", "v150", "v151", "v152", "v153", "v154", "v155", "v156", "v157", "v158", "v159", "v160", "v161", "v162", "v163", "v164", "v165", "v166", "v167", "v168", "v169", "v170", "v171", "v172", "v173", "v174", "v175", "v176", "v177", "v178", "v179", "v180", "v181", "v182", "v183", "v184", "v185", "v186", "v187", "v188", "v189", "v190", "v191", "v192", "v193", "v194", "v195", "v196", "v197", "v198", "v199", "v200", "v201", "v202", "v203", "v204", "v205", "v206", "v207", "v208", "v209", "v210", "v211", "v212", "v213", "v214", "v215", "v216", "v217", "v218", "v219", "v220", "v221", "v222", "v223", "v224", "v225", "v226", "v227", "v228", "v229", "v230", "v231", "v232", "v233", "v234", "v235", "v236", "v237", "v238", "v239", "v240", "v241", "v242", "v243", "v244", "v245", "v246", "v247", "v248", "v249", "v250", "v251", "v252", "v253", "v254", "v255", "a0", "a1", "a2", "a3", "a4", "a5", "a6", "a7", "a8", "a9", "a10", "a11", "a12", "a13", "a14", "a15", "a16", "a17", "a18", "a19", "a20", "a21", "a22", "a23", "a24", "a25", "a26", "a27", "a28", "a29", "a30", "a31", "a32", "a33", "a34", "a35", "a36", "a37", "a38", "a39", "a40", "a41", "a42", "a43", "a44", "a45", "a46", "a47", "a48", "a49", "a50", "a51", "a52", "a53", "a54", "a55", "a56", "a57", "a58", "a59", "a60", "a61", "a62", "a63", "a64", "a65", "a66", "a67", "a68", "a69", "a70", "a71", "a72", "a73", "a74", "a75", "a76", "a77", "a78", "a79", "a80", "a81", "a82", "a83", "a84", "a85", "a86", "a87", "a88", "a89", "a90", "a91", "a92", "a93", "a94", "a95", "a96", "a97", "a98", "a99", "a100", "a101", "a102", "a103", "a104", "a105", "a106", "a107", "a108", "a109", "a110", "a111", "a112", "a113", "a114", "a115", "a116", "a117", "a118", "a119", "a120", "a121", "a122", "a123", "a124", "a125", "a126", "a127", "a128", "a129", "a130", "a131", "a132", "a133", "a134", "a135", "a136", "a137", "a138", "a139", "a140", "a141", "a142", "a143", "a144", "a145", "a146", "a147", "a148", "a149", "a150", "a151", "a152", "a153", "a154", "a155", "a156", "a157", "a158", "a159", "a160", "a161", "a162", "a163", "a164", "a165", "a166", "a167", "a168", "a169", "a170", "a171", "a172", "a173", "a174", "a175", "a176", "a177", "a178", "a179", "a180", "a181", "a182", "a183", "a184", "a185", "a186", "a187", "a188", "a189", "a190", "a191", "a192", "a193", "a194", "a195", "a196", "a197", "a198", "a199", "a200", "a201", "a202", "a203", "a204", "a205", "a206", "a207", "a208", "a209", "a210", "a211", "a212", "a213", "a214", "a215", "a216", "a217", "a218", "a219", "a220", "a221", "a222", "a223", "a224", "a225", "a226", "a227", "a228", "a229", "a230", "a231", "a232", "a233", "a234", "a235", "a236", "a237", "a238", "a239", "a240", "a241", "a242", "a243", "a244", "a245", "a246", "a247", "a248", "a249", "a250", "a251", "a252", "a253", "a254", "a255", "s34", "s35", "s36", "s37", "s38", "s39", "s40", "s41", "s42", "s43", "s44", "s45", "s46", "s47", "s48", "s49", "s50", "s51", "s52", "s53", "s54", "s55", "s56", "s57", "s58", "s59", "s60", "s61", "s62", "s63", "s64", "s65", "s66", "s67", "s68", "s69", "s70", "s71", "s72", "s73", "s74", "s75", "s76", "s77", "s78", "s79", "s80", "s81", "s82", "s83", "s84", "s85", "s86", "s87", "s88", "s89", "s90", "s91", "s92", "s93", "s94", "s95", "s96", "s97", "s98", "s99");
}

extern "C" void kernel_launch(void* const* d_in, const int* in_sizes, int n_in, void* d_out, int out_size, void* d_ws, size_t ws_size, hipStream_t stream) {
  k1_ln<<<256, 512, 0, stream>>>((const uint4*)d_in[0], (uint4*)d_ws, (unsigned*)((char*)d_ws + 39845888));
  k2_main<<<256, 256, 0, stream>>>(d_ws, d_in[3], d_in[6], d_in[7], d_in[1], d_in[2], d_in[4], d_in[8], d_in[0], d_out);
}
```
